```python
import torch

def _softmax(x, axis=-1):
    return torch.softmax(x, dim=axis)
def _gelu(x):
    return torch.nn.functional.gelu(x, approximate="tanh")
def _silu(x):
    return torch.nn.functional.silu(x)
def _sigmoid(x):
    return torch.sigmoid(x)
def _layernorm(x, axis=-1, eps=1e-5):
    m = x.mean(dim=axis, keepdim=True)
    v = x.var(dim=axis, keepdim=True, unbiased=False)
    return (x - m) / torch.sqrt(v + eps)
def _scatter_add(tgt, idx, src, axis=0):
    return tgt.clone().index_add_(axis, idx, src)
def _segment_sum(src, seg, n):
    out = torch.zeros((n,) + src.shape[1:], dtype=src.dtype)
    return out.index_add_(0, seg, src)
def _pad(x, pw):
    flat = tuple(v for pair in reversed(pw) for v in pair)
    return torch.nn.functional.pad(x, flat)

kernel_name = 'interleave_layers__gqa_block_cached__glu_gated_bilinear__decode'

def setup_inputs():

    _w = (lambda a: (lambda t: t.to(torch.bfloat16) if t.is_floating_point() else t)(torch.as_tensor(a)))
    def _A_setup():
        B, Sq, D = 2, 1, 512
        Hq, Hkv, Sk, Dh = 32, 32, 16384, 128
        assert Hq % Hkv == 0, (Hq, Hkv)
        return {
            "x": _w((lambda *s: torch.randn(*s, dtype=torch.bfloat16) * 0.05)(B, Sq, D)),
            "k_cache": _w((lambda *s: torch.randn(*s, dtype=torch.bfloat16) * 0.8)(B, Hkv, Sk, Dh)),
            "v_cache": _w((lambda *s: torch.randn(*s, dtype=torch.bfloat16) * 1.6)(B, Hkv, Sk, Dh)),
            "wq": _w((lambda *s: torch.randn(*s, dtype=torch.bfloat16) * 0.8)(D, Hq * Dh) * (D ** -0.5)),
            "wo": _w((lambda *s: torch.randn(*s, dtype=torch.bfloat16) * 1.6)(Hq * Dh, D) * ((Hq * Dh) ** -0.5)),
        }
    _A_in = _A_setup()

    def _B_setup():
        B, S, D, F = 2, 1, 512, 1376
        return {
            "x": _w((lambda *s: torch.randn(*s, dtype=torch.bfloat16) * 0.05)(B, S, D)),
            "w_gate": _w((lambda *s: torch.randn(*s, dtype=torch.bfloat16) * 0.4)(D, F) * (D ** -0.5)),
            "w_up": _w((lambda *s: torch.randn(*s, dtype=torch.bfloat16) * 0.4)(D, F) * (D ** -0.5)),
            "w_down": _w((lambda *s: torch.randn(*s, dtype=torch.bfloat16) * 0.4)(F, D) * (F ** -0.5)),
        }
    _B_in = _B_setup()

    return {
        "A_x": _A_in["x"],
        "A_k_cache": _A_in["k_cache"],
        "A_v_cache": _A_in["v_cache"],
        "A_wq": _A_in["wq"],
        "A_wo": _A_in["wo"],
        "B_x": _B_in["x"],
        "B_w_gate": _B_in["w_gate"],
        "B_w_up": _B_in["w_up"],
        "B_w_down": _B_in["w_down"],
    }

def reference(A_x, A_k_cache, A_v_cache, A_wq, A_wo, B_x, B_w_gate, B_w_up, B_w_down):
    def _A_ref(x, k_cache, v_cache, wq, wo):
        B, Sq, D = x.shape
        Hq, Dh = 32, 128
        Hkv = k_cache.shape[1]
        rep = Hq // Hkv
        q = (lambda x, *d: x.permute(*d))((x @ wq).reshape(B, Sq, Hq, Dh), 0, 2, 1, 3)
        k = (lambda x, n, axis: torch.repeat_interleave(x, n, dim=axis))(k_cache, rep, axis=1)
        v = (lambda x, n, axis: torch.repeat_interleave(x, n, dim=axis))(v_cache, rep, axis=1)
        scores = torch.einsum("bhqd,bhkd->bhqk", q, k) * (Dh ** -0.5)
        p = _softmax(scores, axis=-1)
        o = torch.einsum("bhqk,bhkd->bhqd", p, v)
        return (lambda x, *d: x.permute(*d))(o, 0, 2, 1, 3).reshape(B, Sq, Hq * Dh) @ wo

    def _B_ref(x, w_gate, w_up, w_down):
        g = torch.einsum("bsd,df->bsf", x, w_gate)
        u = torch.einsum("bsd,df->bsf", x, w_up)
        h = g * u
        return torch.einsum("bsf,fd->bsd", h, w_down)

    x = A_x
    x = x + _A_ref(_layernorm(x, axis=-1), A_k_cache, A_v_cache, A_wq, A_wo)
    x = x + _B_ref(_layernorm(x, axis=-1), B_w_gate, B_w_up, B_w_down)
    x = x + _A_ref(_layernorm(x, axis=-1), A_k_cache, A_v_cache, A_wq, A_wo)
    x = x + _B_ref(_layernorm(x, axis=-1), B_w_gate, B_w_up, B_w_down)
    return x
```

```cpp
#include <hip/hip_runtime.h>
#include <hip/hip_bf16.h>
#include <stdint.h>
#define POL 1
typedef unsigned u32x4 __attribute__((ext_vector_type(4)));
typedef float f32x4 __attribute__((ext_vector_type(4)));
typedef short bf16x8 __attribute__((ext_vector_type(8)));
typedef unsigned short u16;
constexpr int B=2, D=512, H=32, SK=16384, DH=128, F=1376;
constexpr int NWAVE_PER_BH = 16;
constexpr int KEYS_PER_WAVE = SK/NWAVE_PER_BH;
constexpr int UNITS = KEYS_PER_WAVE/16;

__device__ __forceinline__ float bf2f(u16 v){ return __uint_as_float(((unsigned)v)<<16); }
__device__ __forceinline__ u16 f2bf(float f){ unsigned u=__float_as_uint(f); u += 0x7FFFu + ((u>>16)&1u); return (u16)(u>>16); }

template<int OFF> __device__ __forceinline__ void ldx(u32x4& d, unsigned voff, const unsigned char* sb){
  asm volatile("global_load_dwordx4 %0, %1, %2 offset:%c3 nt" : "+v"(d) : "v"(voff), "s"(sb), "i"(OFF) : "memory");
}
template<int N> __device__ __forceinline__ void waitv4(u32x4 (&d)[4]){ asm volatile("s_waitcnt vmcnt(%c4)" : "+v"(d[0]),"+v"(d[1]),"+v"(d[2]),"+v"(d[3]) : "i"(N) : "memory"); }
__device__ __forceinline__ f32x4 mfma(u32x4 a, u32x4 b, f32x4 c){
  return __builtin_amdgcn_mfma_f32_16x16x32_bf16(__builtin_bit_cast(__attribute__((ext_vector_type(8))) __bf16,a), __builtin_bit_cast(__attribute__((ext_vector_type(8))) __bf16,b), c, 0,0,0);
}
template<int CTRL,int RM> __device__ __forceinline__ float dppf(float old, float x){
  return __int_as_float(__builtin_amdgcn_update_dpp(__float_as_int(old), __float_as_int(x), CTRL, RM, 0xf, false));
}
template<int CTRL> __device__ __forceinline__ unsigned dppu(unsigned x){
  return (unsigned)__builtin_amdgcn_update_dpp(0, (int)x, CTRL, 0xf, 0xf, true);
}
__device__ __forceinline__ void ldK(u32x4 (&k)[4], unsigned vo, const unsigned char* sb){ ldx<0>(k[0],vo,sb); ldx<128>(k[1],vo,sb); ldx<2048>(k[2],vo,sb); ldx<2048+128>(k[3],vo,sb); }
__device__ __forceinline__ void ldV(u32x4 (&v)[4], unsigned vo, const unsigned char* sb){ ldx<0>(v[0],vo,sb); ldx<256>(v[1],vo,sb); ldx<512>(v[2],vo,sb); ldx<768>(v[3],vo,sb); }

struct St { f32x4 O; float m, l; };
__device__ __forceinline__ float wave_max(float x){
  for(int o=32;o>=1;o>>=1) x=fmaxf(x,__shfl_xor(x,o,64));
  return x;
}
__device__ __forceinline__ float wave_sum(float x){
  for(int o=32;o>=1;o>>=1) x+=__shfl_xor(x,o,64);
  return x;
}
constexpr float CS = 0.08838834764831845f*1.4426950408889634f;

__device__ __forceinline__ unsigned qk_softmax(const u32x4 (&k)[4], const u32x4 (&qa)[2], St& s, bool used, int g){
  f32x4 z={0,0,0,0};
  f32x4 d0=mfma(qa[0],k[0],z); d0=mfma(qa[1],k[1],d0);
  f32x4 d1=mfma(qa[0],k[2],z); d1=mfma(qa[1],k[3],d1);
  float a0 = d0[0] + __int_as_float(__builtin_amdgcn_update_dpp(0,__float_as_int(d0[1]),0xF5 ,0xf,0xf,true));
  float a1 = d1[0] + __int_as_float(__builtin_amdgcn_update_dpp(0,__float_as_int(d1[1]),0xF5,0xf,0xf,true));
  float Z = (g>=2)? a1 : a0;
  Z = used? Z : -INFINITY;
  if(__any(Z > s.m)){
    float mx=wave_max(Z); float mn=fmaxf(s.m,mx);
    float al=__builtin_amdgcn_exp2f((s.m-mn)*CS);
    s.O*=al; s.l*=al; s.m=mn;
  }
  float p=__builtin_amdgcn_exp2f(Z*CS - s.m*CS);
  s.l+=p;
  unsigned pb=f2bf(p); pb |= pb<<16;
  pb=(unsigned)__builtin_amdgcn_update_dpp((int)pb,(int)pb,0x128 ,0xA,0xf,false);
  return pb;
}
template<int U> __device__ __forceinline__ void pv1(unsigned pb, const u32x4& v, const u32x4& mask, St& s){
  unsigned bc=dppu<0x150+2*U>(pb);
  u32x4 a={bc&mask[0],bc&mask[1],bc&mask[2],bc&mask[3]};
  s.O=mfma(a,v,s.O);
}
__device__ __forceinline__ void pv(unsigned pb, const u32x4 (&v)[4], const u32x4& mask, St& s){
  pv1<0>(pb,v[0],mask,s); pv1<1>(pb,v[1],mask,s); pv1<2>(pb,v[2],mask,s); pv1<3>(pb,v[3],mask,s);
}

__global__ void __launch_bounds__(256,1) k_attn(const unsigned char* K, const unsigned char* V, const float* q, float* part){
  extern __shared__ unsigned char smem[];
  const int lane=threadIdx.x&63, w=__builtin_amdgcn_readfirstlane(threadIdx.x>>6);
  const int bh=blockIdx.x>>2, sp=(blockIdx.x&3)*4+w;
  const int n=lane&15, g=lane>>4;
  u32x4 qa[2];
  for(int L=0;L<2;L++){ for(int d=0;d<4;d++){ int e=64*L+32*(n&1)+8*g+2*d; unsigned lo=f2bf(q[bh*128+e]), hi=f2bf(q[bh*128+e+1]); qa[L][d]=lo|(hi<<16);} }
  u32x4 mask; for(int d=0;d<4;d++) mask[d]= (n<8 && (n>>1)==d)? ((n&1)?0xFFFF0000u:0x0000FFFFu):0u;
  const bool used = ((n&1)==0) && ((n>>3)==(g&1));
  const unsigned voK=(n>>1)*256+(n&1)*64+g*16, voV=g*1024+n*16;
  const size_t base=((size_t)bh*SK+(size_t)sp*KEYS_PER_WAVE)*256;
  const unsigned char* kb=K+base; const unsigned char* vb=V+base;
  St s; s.O=f32x4{0,0,0,0}; s.m=-INFINITY; s.l=0.f;
  u32x4 k0[4],v0[4],k1[4],v1[4];
  for(int i=0;i<4;i++){k0[i]=v0[i]=k1[i]=v1[i]=u32x4{0,0,0,0};}
  ldK(k0,voK,kb); ldV(v0,voV,vb); ldK(k1,voK,kb+4096); ldV(v1,voV,vb+4096);
  for(int u=0;u<UNITS-2;u+=2){
    waitv4<12>(k0); unsigned pb=qk_softmax(k0,qa,s,used,g); ldK(k0,voK,kb+(size_t)(u+2)*4096);
    waitv4<12>(v0); pv(pb,v0,mask,s); ldV(v0,voV,vb+(size_t)(u+2)*4096);
    waitv4<12>(k1); pb=qk_softmax(k1,qa,s,used,g); ldK(k1,voK,kb+(size_t)(u+3)*4096);
    waitv4<12>(v1); pv(pb,v1,mask,s); ldV(v1,voV,vb+(size_t)(u+3)*4096);
  }
  { waitv4<12>(k0); unsigned pb=qk_softmax(k0,qa,s,used,g);
    waitv4<8>(v0); pv(pb,v0,mask,s);
    waitv4<4>(k1); pb=qk_softmax(k1,qa,s,used,g);
    waitv4<0>(v1); pv(pb,v1,mask,s); }
  float l=wave_sum(s.l);
  float* o=part+(size_t)(bh*16+sp)*132;
  if(lane==0){o[0]=s.m;o[1]=l;}
  if(g<2) *(f32x4*)(o+4+8*n+4*g)=s.O;
}

__global__ void k_q(const float* x, const u16* wq, float* q){
  __shared__ float xn[D]; __shared__ float red[4];
  const int h=blockIdx.x,b=blockIdx.y,t=threadIdx.x;
  float v[4],s=0; for(int i=0;i<4;i++){v[i]=x[b*D+t*4+i]; s+=v[i];}
  s=wave_sum(s); if((t&63)==0) red[t>>6]=s; __syncthreads(); float mean=(red[0]+red[1])/D; __syncthreads();
  float vs=0; for(int i=0;i<4;i++){float d=v[i]-mean; vs+=d*d;}
  vs=wave_sum(vs); if((t&63)==0) red[t>>6]=vs; __syncthreads(); float rstd=1.0f/sqrtf((red[0]+red[1])/D+1e-5f);
  for(int i=0;i<4;i++) xn[t*4+i]=(v[i]-mean)*rstd;
  __syncthreads();
  float acc=0; for(int k=0;k<D;k++) acc+=xn[k]*bf2f(wq[(size_t)k*4096+h*128+t]);
  q[(b*H+h)*128+t]=acc;
}
__global__ void k_init(const u16* x, float* xf){ int i=blockIdx.x*256+threadIdx.x; if(i<B*D) xf[i]=bf2f(x[i]); }
__global__ void k_comb(const float* part, float* o){
  const int bh=blockIdx.x,t=threadIdx.x; const float* p=part+(size_t)bh*16*132;
  float M=-INFINITY; for(int s=0;s<16;s++) M=fmaxf(M,p[s*132]);
  float L=0,a=0; for(int s=0;s<16;s++){ float w=__builtin_amdgcn_exp2f((p[s*132]-M)*CS); L+=w*p[s*132+1]; a+=w*p[s*132+4+t]; }
  o[bh*128+t]=a/L;
}
__global__ void k_proj(const float* a, int Kd, const u16* W, const float* xin, float* out, u16* outb){
  __shared__ float red[4][64];
  const int c=blockIdx.x*64+(threadIdx.x&63), ks=threadIdx.x>>6, b=blockIdx.y;
  float acc=0; for(int k=ks;k<Kd;k+=4) acc+=a[b*Kd+k]*bf2f(W[(size_t)k*D+c]);
  red[ks][threadIdx.x&63]=acc; __syncthreads();
  if(ks==0){ float r=xin[b*D+c]+red[0][threadIdx.x]+red[1][threadIdx.x]+red[2][threadIdx.x]+red[3][threadIdx.x]; out[b*D+c]=r; if(outb) outb[b*D+c]=f2bf(r);}
}
__global__ void k_gu(const float* x, const u16* wg, const u16* wu, float* hh){
  __shared__ float xn[D]; __shared__ float red[4]; __shared__ float pr[2][4][32];
  const int b=blockIdx.y,t=threadIdx.x;
  float v[4],s=0; for(int i=0;i<4;i++){v[i]=x[b*D+t*4+i]; s+=v[i];}
  s=wave_sum(s); if((t&63)==0) red[t>>6]=s; __syncthreads(); float mean=(red[0]+red[1])/D; __syncthreads();
  float vs=0; for(int i=0;i<4;i++){float d=v[i]-mean; vs+=d*d;}
  vs=wave_sum(vs); if((t&63)==0) red[t>>6]=vs; __syncthreads(); float rstd=1.0f/sqrtf((red[0]+red[1])/D+1e-5f);
  for(int i=0;i<4;i++) xn[t*4+i]=(v[i]-mean)*rstd;
  __syncthreads();
  const int c=blockIdx.x*32+(t&31), ks=t>>5; float g=0,u=0;
  for(int k=ks;k<D;k+=4){ g+=xn[k]*bf2f(wg[(size_t)k*F+c]); u+=xn[k]*bf2f(wu[(size_t)k*F+c]); }
  pr[0][ks][t&31]=g; pr[1][ks][t&31]=u; __syncthreads();
  if(t<32){ float G=pr[0][0][t]+pr[0][1][t]+pr[0][2][t]+pr[0][3][t], U=pr[1][0][t]+pr[1][1][t]+pr[1][2][t]+pr[1][3][t]; hh[b*F+c]=G*U; }
}

extern "C" void kernel_launch(void* const* d_in, const int* in_sizes, int n_in, void* d_out, int out_size, void* d_ws, size_t ws_size, hipStream_t stream) {
  const u16* x=(const u16*)d_in[0]; const unsigned char* K=(const unsigned char*)d_in[1]; const unsigned char* V=(const unsigned char*)d_in[2];
  const u16* wq=(const u16*)d_in[3]; const u16* wo=(const u16*)d_in[4]; const u16* wg=(const u16*)d_in[6]; const u16* wu=(const u16*)d_in[7]; const u16* wd=(const u16*)d_in[8];
  float* ws=(float*)d_ws; float* x0=ws; float* x1=ws+1024; float* q=ws+2048; float* o=q+8192; float* hh=o+8192; float* part=hh+4096;
  (void)hipFuncSetAttribute((const void*)k_attn, hipFuncAttributeMaxDynamicSharedMemorySize, 100*1024);
  k_init<<<4,256,0,stream>>>(x,x0);
  for(int it=0;it<2;it++){
    k_q<<<dim3(H,B),128,0,stream>>>(x0,wq,q);
    k_attn<<<256,256,100*1024,stream>>>(K,V,q,part);
    k_comb<<<64,128,0,stream>>>(part,o);
    k_proj<<<dim3(8,B),256,0,stream>>>(o,4096,wo,x0,x1,nullptr);
    k_gu<<<dim3(F/32,B),128,0,stream>>>(x1,wg,wu,hh);
    k_proj<<<dim3(8,B),256,0,stream>>>(hh,F,wd,x1,x0,it==1?(u16*)d_out:nullptr);
  }
}
```

```cpp
#include <hip/hip_runtime.h>
#include <hip/hip_bf16.h>
#include <stdint.h>
#define POL1 1
#define POL2 1
typedef unsigned u32x4 __attribute__((ext_vector_type(4)));
typedef float f32x4 __attribute__((ext_vector_type(4)));
typedef short bf16x8 __attribute__((ext_vector_type(8)));
typedef unsigned short u16;
constexpr int B=2, D=512, H=32, SK=16384, DH=128, F=1376;
constexpr int NWAVE_PER_BH = 16;
constexpr int KEYS_PER_WAVE = SK/NWAVE_PER_BH;
constexpr int UNITS = KEYS_PER_WAVE/16;

__device__ __forceinline__ float bf2f(u16 v){ return __uint_as_float(((unsigned)v)<<16); }
__device__ __forceinline__ u16 f2bf(float f){ unsigned u=__float_as_uint(f); u += 0x7FFFu + ((u>>16)&1u); return (u16)(u>>16); }

template<int OFF,int NT> __device__ __forceinline__ void ldx(u32x4& d, unsigned voff, const unsigned char* sb){
  if constexpr(NT) asm volatile("global_load_dwordx4 %0, %1, %2 offset:%c3 nt" : "+v"(d) : "v"(voff), "s"(sb), "i"(OFF) : "memory");
  else asm volatile("global_load_dwordx4 %0, %1, %2 offset:%c3" : "+v"(d) : "v"(voff), "s"(sb), "i"(OFF) : "memory");
}
template<int N> __device__ __forceinline__ void waitv4(u32x4 (&d)[4]){ asm volatile("s_waitcnt vmcnt(%c4)" : "+v"(d[0]),"+v"(d[1]),"+v"(d[2]),"+v"(d[3]) : "i"(N) : "memory"); }
__device__ __forceinline__ f32x4 mfma(u32x4 a, u32x4 b, f32x4 c){
  return __builtin_amdgcn_mfma_f32_16x16x32_bf16(__builtin_bit_cast(__attribute__((ext_vector_type(8))) __bf16,a), __builtin_bit_cast(__attribute__((ext_vector_type(8))) __bf16,b), c, 0,0,0);
}
template<int CTRL,int RM> __device__ __forceinline__ float dppf(float old, float x){
  return __int_as_float(__builtin_amdgcn_update_dpp(__float_as_int(old), __float_as_int(x), CTRL, RM, 0xf, false));
}
template<int CTRL> __device__ __forceinline__ unsigned dppu(unsigned x){
  return (unsigned)__builtin_amdgcn_update_dpp(0, (int)x, CTRL, 0xf, 0xf, true);
}
template<int NT> __device__ __forceinline__ void ldK_(u32x4 (&k)[4], unsigned vo, const unsigned char* sb){ ldx<0,NT>(k[0],vo,sb); ldx<128,NT>(k[1],vo,sb); ldx<2048,NT>(k[2],vo,sb); ldx<2048+128,NT>(k[3],vo,sb); }
template<int NT> __device__ __forceinline__ void ldV_(u32x4 (&v)[4], unsigned vo, const unsigned char* sb){ ldx<0,NT>(v[0],vo,sb); ldx<256,NT>(v[1],vo,sb); ldx<512,NT>(v[2],vo,sb); ldx<768,NT>(v[3],vo,sb); }
#define ldK(k,vo,sb) ldK_<NT>(k,vo,sb)
#define ldV(k,vo,sb) ldV_<NT>(k,vo,sb)

struct St { f32x4 O; float m, l; };
__device__ __forceinline__ float wave_max(float x){
  for(int o=32;o>=1;o>>=1) x=fmaxf(x,__shfl_xor(x,o,64));
  return x;
}
__device__ __forceinline__ float wave_sum(float x){
  for(int o=32;o>=1;o>>=1) x+=__shfl_xor(x,o,64);
  return x;
}
constexpr float CS = 0.08838834764831845f*1.4426950408889634f;

__device__ __forceinline__ unsigned qk_softmax(const u32x4 (&k)[4], const u32x4 (&qa)[2], St& s, bool used, int g){
  f32x4 z={0,0,0,0};
  f32x4 d0=mfma(qa[0],k[0],z); d0=mfma(qa[1],k[1],d0);
  f32x4 d1=mfma(qa[0],k[2],z); d1=mfma(qa[1],k[3],d1);
  float a0 = d0[0] + __int_as_float(__builtin_amdgcn_update_dpp(0,__float_as_int(d0[1]),0xF5 ,0xf,0xf,true));
  float a1 = d1[0] + __int_as_float(__builtin_amdgcn_update_dpp(0,__float_as_int(d1[1]),0xF5,0xf,0xf,true));
  float Z = (g>=2)? a1 : a0;
  Z = used? Z : -INFINITY;
  if(__any(Z > s.m)){
    float mx=wave_max(Z); float mn=fmaxf(s.m,mx);
    float al=__builtin_amdgcn_exp2f((s.m-mn)*CS);
    s.O*=al; s.l*=al; s.m=mn;
  }
  float p=__builtin_amdgcn_exp2f(Z*CS - s.m*CS);
  s.l+=p;
  unsigned pb=f2bf(p); pb |= pb<<16;
  pb=(unsigned)__builtin_amdgcn_update_dpp((int)pb,(int)pb,0x128 ,0xA,0xf,false);
  return pb;
}
template<int U> __device__ __forceinline__ void pv1(unsigned pb, const u32x4& v, const u32x4& mask, St& s){
  unsigned bc=dppu<0x150+2*U>(pb);
  u32x4 a={bc&mask[0],bc&mask[1],bc&mask[2],bc&mask[3]};
  s.O=mfma(a,v,s.O);
}
__device__ __forceinline__ void pv(unsigned pb, const u32x4 (&v)[4], const u32x4& mask, St& s){
  pv1<0>(pb,v[0],mask,s); pv1<1>(pb,v[1],mask,s); pv1<2>(pb,v[2],mask,s); pv1<3>(pb,v[3],mask,s);
}

template<int NT> __global__ void __launch_bounds__(256,1) k_attn(const unsigned char* K, const unsigned char* V, const float* q, float* part){
  extern __shared__ unsigned char smem[];
  const int lane=threadIdx.x&63, w=__builtin_amdgcn_readfirstlane(threadIdx.x>>6);
  const int bh=blockIdx.x>>2, sp=(blockIdx.x&3)*4+w;
  const int n=lane&15, g=lane>>4;
  u32x4 qa[2];
  for(int L=0;L<2;L++){ for(int d=0;d<4;d++){ int e=64*L+32*(n&1)+8*g+2*d; unsigned lo=f2bf(q[bh*128+e]), hi=f2bf(q[bh*128+e+1]); qa[L][d]=lo|(hi<<16);} }
  u32x4 mask; for(int d=0;d<4;d++) mask[d]= (n<8 && (n>>1)==d)? ((n&1)?0xFFFF0000u:0x0000FFFFu):0u;
  const bool used = ((n&1)==0) && ((n>>3)==(g&1));
  const unsigned voK=(n>>1)*256+(n&1)*64+g*16, voV=g*1024+n*16;
  const size_t base=((size_t)bh*SK+(size_t)sp*KEYS_PER_WAVE)*256;
  const unsigned char* kb=K+base; const unsigned char* vb=V+base;
  St s; s.O=f32x4{0,0,0,0}; s.m=-INFINITY; s.l=0.f;
  u32x4 k0[4],v0[4],k1[4],v1[4];
  for(int i=0;i<4;i++){k0[i]=v0[i]=k1[i]=v1[i]=u32x4{0,0,0,0};}
  ldK(k0,voK,kb); ldV(v0,voV,vb); ldK(k1,voK,kb+4096); ldV(v1,voV,vb+4096);
  for(int u=0;u<UNITS-2;u+=2){
    waitv4<12>(k0); unsigned pb=qk_softmax(k0,qa,s,used,g); ldK(k0,voK,kb+(size_t)(u+2)*4096);
    waitv4<12>(v0); pv(pb,v0,mask,s); ldV(v0,voV,vb+(size_t)(u+2)*4096);
    waitv4<12>(k1); pb=qk_softmax(k1,qa,s,used,g); ldK(k1,voK,kb+(size_t)(u+3)*4096);
    waitv4<12>(v1); pv(pb,v1,mask,s); ldV(v1,voV,vb+(size_t)(u+3)*4096);
  }
  { waitv4<12>(k0); unsigned pb=qk_softmax(k0,qa,s,used,g);
    waitv4<8>(v0); pv(pb,v0,mask,s);
    waitv4<4>(k1); pb=qk_softmax(k1,qa,s,used,g);
    waitv4<0>(v1); pv(pb,v1,mask,s); }
  float l=wave_sum(s.l);
  float* o=part+(size_t)(bh*16+sp)*132;
  if(lane==0){o[0]=s.m;o[1]=l;}
  if(g<2) *(f32x4*)(o+4+8*n+4*g)=s.O;
}


__device__ __forceinline__ float block_sum(float v, float* red){
  v=wave_sum(v); __syncthreads(); if((threadIdx.x&63)==0) red[threadIdx.x>>6]=v; __syncthreads(); return red[0]+red[1]+red[2]+red[3];
}
__device__ __forceinline__ void ln2(const float* x, float (*xn)[D], float* red){
  const int t=threadIdx.x;
  for(int b=0;b<2;b++){
    float v0=x[b*D+t*2], v1=x[b*D+t*2+1];
    float mean=block_sum(v0+v1,red)/D; float d0=v0-mean,d1=v1-mean;
    float var=block_sum(d0*d0+d1*d1,red)/D; float r=1.0f/sqrtf(var+1e-5f);
    xn[b][t*2]=d0*r; xn[b][t*2+1]=d1*r;
  }
  __syncthreads();
}
template<int K> __device__ __forceinline__ void gemv8(const float* a0, const float* a1, const u16* W, int N, int c0, float (&acc)[2][8]){
  const int t=threadIdx.x;
#pragma unroll
  for(int i=0;i<(K+255)/256;i++){ int k=t+i*256; if(k<K){ u32x4 w=*(const u32x4*)(W+(size_t)k*N+c0); float x0=a0[k],x1=a1[k];
#pragma unroll
    for(int d=0;d<4;d++){ float lo=__uint_as_float(w[d]<<16), hi=__uint_as_float(w[d]&0xffff0000u); acc[0][2*d]+=x0*lo; acc[0][2*d+1]+=x0*hi; acc[1][2*d]+=x1*lo; acc[1][2*d+1]+=x1*hi; } } }
}
__device__ __forceinline__ float red16(float (&acc)[2][8], float (*sm)[16]){
  const int t=threadIdx.x;
#pragma unroll
  for(int b=0;b<2;b++)
#pragma unroll
    for(int c=0;c<8;c++){ float v=wave_sum(acc[b][c]); if((t&63)==0) sm[t>>6][b*8+c]=v; }
  __syncthreads();
  float r=0; if(t<16) r=sm[0][t]+sm[1][t]+sm[2][t]+sm[3][t];
  __syncthreads();
  return r;
}
__global__ void __launch_bounds__(256) k_q(const float* x, const u16* wq, float* q){
  __shared__ float xn[2][D]; __shared__ float red[4]; __shared__ float sm[4][16];
  ln2(x,xn,red);
  float acc[2][8]={}; gemv8<D>(xn[0],xn[1],wq,4096,blockIdx.x*8,acc);
  float r=red16(acc,sm); const int t=threadIdx.x; if(t<16) q[(t>>3)*4096+blockIdx.x*8+(t&7)]=r;
}
__global__ void k_init(const u16* x, float* xf){ int i=blockIdx.x*256+threadIdx.x; if(i<B*D) xf[i]=bf2f(x[i]); }
__global__ void k_comb(const float* part, float* o){
  const int bh=blockIdx.x,t=threadIdx.x; const float* p=part+(size_t)bh*16*132;
  float M=-INFINITY; for(int s=0;s<16;s++) M=fmaxf(M,p[s*132]);
  float L=0,a=0; for(int s=0;s<16;s++){ float w=__builtin_amdgcn_exp2f((p[s*132]-M)*CS); L+=w*p[s*132+1]; a+=w*p[s*132+4+t]; }
  o[bh*128+t]=a/L;
}
template<int K> __global__ void __launch_bounds__(256) k_proj(const float* a, const u16* W, const float* xin, float* out, u16* outb){
  __shared__ float sm[4][16];
  float acc[2][8]={}; gemv8<K>(a,a+K,W,D,blockIdx.x*8,acc);
  float r=red16(acc,sm); const int t=threadIdx.x;
  if(t<16){ int idx=(t>>3)*D+blockIdx.x*8+(t&7); r+=xin[idx]; out[idx]=r; if(outb) outb[idx]=f2bf(r);}
}
__global__ void __launch_bounds__(256) k_gu(const float* x, const u16* wg, const u16* wu, float* hh){
  __shared__ float xn[2][D]; __shared__ float red[4]; __shared__ float sm[4][16];
  ln2(x,xn,red);
  float g[2][8]={},u[2][8]={}; gemv8<D>(xn[0],xn[1],wg,F,blockIdx.x*8,g); gemv8<D>(xn[0],xn[1],wu,F,blockIdx.x*8,u);
  float G=red16(g,sm); float U=red16(u,sm); const int t=threadIdx.x; if(t<16) hh[(t>>3)*F+blockIdx.x*8+(t&7)]=G*U;
}

extern "C" void kernel_launch(void* const* d_in, const int* in_sizes, int n_in, void* d_out, int out_size, void* d_ws, size_t ws_size, hipStream_t stream) {
  const u16* x=(const u16*)d_in[0]; const unsigned char* K=(const unsigned char*)d_in[1]; const unsigned char* V=(const unsigned char*)d_in[2];
  const u16* wq=(const u16*)d_in[3]; const u16* wo=(const u16*)d_in[4]; const u16* wg=(const u16*)d_in[6]; const u16* wu=(const u16*)d_in[7]; const u16* wd=(const u16*)d_in[8];
  float* ws=(float*)d_ws; float* x0=ws; float* x1=ws+1024; float* q=ws+2048; float* o=q+8192; float* hh=o+8192; float* part=hh+4096;
  (void)hipFuncSetAttribute((const void*)k_attn<0>, hipFuncAttributeMaxDynamicSharedMemorySize, 100*1024);
  (void)hipFuncSetAttribute((const void*)k_attn<1>, hipFuncAttributeMaxDynamicSharedMemorySize, 100*1024);
  k_init<<<4,256,0,stream>>>(x,x0);
  for(int it=0;it<2;it++){
    k_q<<<512,256,0,stream>>>(x0,wq,q);
    if(it==0) k_attn<POL1><<<256,256,100*1024,stream>>>(K,V,q,part); else k_attn<POL2><<<256,256,100*1024,stream>>>(K,V,q,part);
    k_comb<<<64,128,0,stream>>>(part,o);
    k_proj<4096><<<64,256,0,stream>>>(o,wo,x0,x1,nullptr);
    k_gu<<<172,256,0,stream>>>(x1,wg,wu,hh);
    k_proj<F><<<64,256,0,stream>>>(hh,wd,x1,x0,it==1?(u16*)d_out:nullptr);
  }
}
```

```cpp
#include <hip/hip_runtime.h>
#include <hip/hip_bf16.h>
#include <stdint.h>

namespace {
typedef unsigned short u16;
typedef unsigned u32x2 __attribute__((ext_vector_type(2)));
typedef unsigned u32x4 __attribute__((ext_vector_type(4)));
typedef float f32x4 __attribute__((ext_vector_type(4)));
typedef short s16x4 __attribute__((ext_vector_type(4)));
#define GAS __attribute__((address_space(1)))
#define LAS __attribute__((address_space(3)))

constexpr int DM = 512, NH = 32, SK = 16384, FF = 1376;
constexpr int NSERV = 32, NSTREAM = 224, WPH = 28;
constexpr int R_RET = 18;
constexpr int U_HI = 36, U_LO = 36, N_HI = 0;
constexpr int SVC_UNITS = 16;
static_assert(U_HI * N_HI + U_LO * (WPH - N_HI) + SVC_UNITS == SK / 16, "units of a head");
constexpr float CS = 0.08838834764831845f * 1.4426950408889634f;
constexpr int LDS_BYTES = 163840;
constexpr unsigned PSTR = 1152, PWAVES = 8;
constexpr int NREC = WPH / 4; static_assert(WPH % 4 == 0 && NREC <= 8, "a workgroup serves one head");
constexpr int WAVE_LDS = 39936, LDS_XCH = 4 * WAVE_LDS;
constexpr unsigned WS_EPOCH = 0;
constexpr unsigned WS_Q = 65536;
constexpr unsigned WS_Y = WS_Q + 4 * 32 * 512;
constexpr unsigned WS_X1 = WS_Y + 4 * 32 * 4096;
constexpr unsigned WS_H = WS_X1 + 4 * 4096;
constexpr unsigned WS_X2 = WS_H + 4 * 11264;
constexpr unsigned WS_REP = WS_X2 + 2 * 4096;
constexpr unsigned WS_PART = WS_REP + 32 * 8 * 128;
constexpr unsigned WS_END = WS_PART + 4 * 32 * PWAVES * PSTR;
constexpr unsigned SPIN_CAP = 1u << 14;

struct Params { const u16* x; const unsigned char* K; const unsigned char* V; const u16* wq; const u16* wo; const u16* wg; const u16* wu; const u16* wd; u16* out; unsigned char* ws; };

__device__ __forceinline__ float bf2f(unsigned v) { return __uint_as_float(v << 16); }
__device__ __forceinline__ unsigned cvt_pk(float lo, float hi) {
  typedef float f32x2 __attribute__((ext_vector_type(2))); typedef __bf16 bf16x2 __attribute__((ext_vector_type(2)));
  f32x2 v = {lo, hi}; bf16x2 b = __builtin_convertvector(v, bf16x2); return __builtin_bit_cast(unsigned, b);
}
template <int CTRL> __device__ __forceinline__ float dppf(float x) { return __int_as_float(__builtin_amdgcn_update_dpp(0, __float_as_int(x), CTRL, 0xf, 0xf, true)); }
__device__ __forceinline__ float rdlane(float x, int l) { return __int_as_float(__builtin_amdgcn_readlane(__float_as_int(x), l)); }
__device__ __forceinline__ float wave_sum(float x) {
  x += dppf<0x128>(x); x += dppf<0x124>(x); x += dppf<0x122>(x); x += dppf<0x121>(x);
  return (rdlane(x, 0) + rdlane(x, 16)) + (rdlane(x, 32) + rdlane(x, 48));
}
__device__ __forceinline__ float wave_max(float x) {
  x = fmaxf(x, dppf<0x128>(x)); x = fmaxf(x, dppf<0x124>(x)); x = fmaxf(x, dppf<0x122>(x)); x = fmaxf(x, dppf<0x121>(x));
  return fmaxf(fmaxf(rdlane(x, 0), rdlane(x, 16)), fmaxf(rdlane(x, 32), rdlane(x, 48)));
}
typedef __amdgpu_buffer_rsrc_t rsrc_t;
__device__ __forceinline__ u32x2 ld64(rsrc_t r, unsigned off) { return __builtin_amdgcn_raw_buffer_load_b64(r, off, 0, 16); }
__device__ __forceinline__ u32x4 ld128(rsrc_t r, unsigned off) { return __builtin_amdgcn_raw_buffer_load_b128(r, off, 0, 16); }
__device__ __forceinline__ void st64(rsrc_t r, unsigned off, unsigned v, unsigned tag) { __builtin_amdgcn_raw_buffer_store_b64(u32x2{v, tag}, r, off, 0, 16); }
__device__ __forceinline__ void st128(rsrc_t r, unsigned off, unsigned a, unsigned b, unsigned tag) { __builtin_amdgcn_raw_buffer_store_b128(u32x4{a, tag, b, tag}, r, off, 0, 16); }

#define GV_ANDS(R0, R1, R2, R3, T) \
  "v_and_b32_dpp " R0 ", %1, %2 row_newbcast:" #T " row_mask:0xf bank_mask:0xf\n" \
  "v_and_b32_dpp " R1 ", %1, %3 row_newbcast:" #T " row_mask:0xf bank_mask:0xf\n" \
  "v_and_b32_dpp " R2 ", %1, %4 row_newbcast:" #T " row_mask:0xf bank_mask:0xf\n" \
  "v_and_b32_dpp " R3 ", %1, %5 row_newbcast:" #T " row_mask:0xf bank_mask:0xf\n"
#define GV_A(T) GV_ANDS("v248", "v249", "v250", "v251", T)
#define GV_B(T) GV_ANDS("v252", "v253", "v254", "v255", T)
#define GV_MA(W) "v_mfma_f32_16x16x32_bf16 %0, v[248:251], %" #W ", %0\n"
#define GV_MB(W) "v_mfma_f32_16x16x32_bf16 %0, v[252:255], %" #W ", %0\n"
__device__ __forceinline__ void gemv16(f32x4& acc, unsigned P, const u32x4& m, const u32x4* w) {
  asm volatile("s_nop 1\n"
    GV_A(0) GV_B(1) GV_MA(6) GV_A(2) GV_MB(7) GV_B(3) GV_MA(8) GV_A(4) GV_MB(9) GV_B(5) GV_MA(10) GV_A(6) GV_MB(11) GV_B(7) GV_MA(12) GV_A(8) GV_MB(13)
    GV_B(9) GV_MA(14) GV_A(10) GV_MB(15) GV_B(11) GV_MA(16) GV_A(12) GV_MB(17) GV_B(13) GV_MA(18) GV_A(14) GV_MB(19) GV_B(15) GV_MA(20) "s_nop 3\n" GV_MB(21) "s_nop 7\n"
    : "+v"(acc)
    : "v"(P), "v"(m[0]), "v"(m[1]), "v"(m[2]), "v"(m[3]),
      "a"(w[0]), "a"(w[1]), "a"(w[2]), "a"(w[3]), "a"(w[4]), "a"(w[5]), "a"(w[6]), "a"(w[7]),
      "a"(w[8]), "a"(w[9]), "a"(w[10]), "a"(w[11]), "a"(w[12]), "a"(w[13]), "a"(w[14]), "a"(w[15])
    : "v248", "v249", "v250", "v251", "v252", "v253", "v254", "v255");
}
__device__ __forceinline__ void mfma_v0(f32x4& d, const u32x4& a, const u32x4& b) { asm volatile("s_nop 1\nv_mfma_f32_16x16x32_bf16 %0, %1, %2, 0" : "=&v"(d) : "v"(a), "v"(b)); }
__device__ __forceinline__ void mfma_v(f32x4& d, const u32x4& a, const u32x4& b) { asm volatile("v_mfma_f32_16x16x32_bf16 %0, %1, %2, %0" : "+v"(d) : "v"(a), "v"(b)); }
__device__ __forceinline__ void mfma_a0(f32x4& d, const u32x4& a, const u32x4& b) { asm volatile("s_nop 1\nv_mfma_f32_16x16x32_bf16 %0, %1, %2, 0" : "=&v"(d) : "v"(a), "a"(b)); }
__device__ __forceinline__ void mfma_a(f32x4& d, const u32x4& a, const u32x4& b) { asm volatile("v_mfma_f32_16x16x32_bf16 %0, %1, %2, %0" : "+v"(d) : "v"(a), "a"(b)); }
__device__ __forceinline__ void mfma_vn(f32x4& d, const u32x4& a, const u32x4& b) { asm volatile("s_nop 1\nv_mfma_f32_16x16x32_bf16 %0, %1, %2, %0" : "+v"(d) : "v"(a), "v"(b)); }
__device__ __forceinline__ void ld_a(u32x4& d, const void* p) { asm volatile("global_load_dwordx4 %0, %1, off nt" : "=a"(d) : "v"(p) : "memory"); }
__device__ __forceinline__ void pin16(u32x4* w) {
  asm volatile("" : "+a"(w[0]), "+a"(w[1]), "+a"(w[2]), "+a"(w[3]), "+a"(w[4]), "+a"(w[5]), "+a"(w[6]), "+a"(w[7]),
                    "+a"(w[8]), "+a"(w[9]), "+a"(w[10]), "+a"(w[11]), "+a"(w[12]), "+a"(w[13]), "+a"(w[14]), "+a"(w[15]) :: "memory");
}

constexpr int L_KV = 0;
constexpr int L_OWN = 128 * 1024, OWN_B = 2048 + 32;
constexpr int L_ACT = 144 * 1024, ACT_W = 1536, L_RED = L_ACT + 4 * ACT_W, L_OH = L_RED + 2 * 2048, L_XR = L_OH + 1024, L_YT = L_XR + 256, L_QS = L_YT + 2048, L_SEND = L_QS + 512;
static_assert(L_SEND <= LDS_BYTES && L_OWN + 4 * OWN_B <= L_ACT, "service LDS");

struct Svc {
  rsrc_t ws; unsigned tag; int j, w, lane, tid; LAS unsigned char* L; u32x4 mask; bool dead; int rb;
  __device__ __forceinline__ bool spin(unsigned& n) {
    asm volatile("" ::: "memory");
    if (dead) return true;
    if (++n > SPIN_CAP) { dead = true; return true; }
    return false;
  }
  __device__ __forceinline__ LAS float* red() { rb ^= 1; return (LAS float*)(L + L_RED + rb * 2048); }
  __device__ __forceinline__ void ln_own(const float (&v)[4][2], float& n0, float& n1) {
    const float s = ((v[0][0] + v[0][1]) + (v[1][0] + v[1][1])) + ((v[2][0] + v[2][1]) + (v[3][0] + v[3][1]));
    const float mean = wave_sum(s) * (1.0f / DM);
    float q = 0.f;
#pragma unroll
    for (int i = 0; i < 4; ++i) { const float a = v[i][0] - mean, b = v[i][1] - mean; q = fmaf(a, a, q); q = fmaf(b, b, q); }
    const float rstd = __builtin_amdgcn_rsqf(wave_sum(q) * (1.0f / DM) + 1e-5f);
    n0 = (v[0][0] - mean) * rstd; n1 = (v[0][1] - mean) * rstd;
  }
  template <int N, class F> __device__ __forceinline__ void gather2(u32x4 (&g)[N], F off) {
    u32x4 h[N];
#pragma unroll
    for (int i = 0; i < N; ++i) g[i] = ld128(ws, off(i));
    __builtin_amdgcn_s_sleep(9);
    for (unsigned n = 0;;) {
      asm volatile("" ::: "memory");
#pragma unroll
      for (int i = 0; i < N; ++i) h[i] = ld128(ws, off(i));
      bool ok = true;
#pragma unroll
      for (int i = 0; i < N; ++i) ok &= (g[i][1] == tag) & (g[i][3] == tag);
      if (__all(ok)) break;
      asm volatile("" ::: "memory");
#pragma unroll
      for (int i = 0; i < N; ++i) g[i] = ld128(ws, off(i));
      ok = true;
#pragma unroll
      for (int i = 0; i < N; ++i) ok &= (h[i][1] == tag) & (h[i][3] == tag);
      if (__all(ok) || spin(n)) {
#pragma unroll
        for (int i = 0; i < N; ++i) g[i] = h[i];
        break;
      }
    }
  }
  __device__ __forceinline__ void gather512(unsigned off, float (&v)[4][2]) {
    u32x4 g[4];
    const unsigned o = off + lane * 16; const int w_ = w;
    gather2<4>(g, [&](int i) { return o + ((i + w_) & 3) * 1024; });
#pragma unroll
    for (int i = 0; i < 4; ++i) { v[i][0] = __uint_as_float(g[i][0]); v[i][1] = __uint_as_float(g[i][2]); }
  }
  __device__ __forceinline__ void qproj(float n0, float n1, const u32x4 (&wq)[32], unsigned qoff, int qb) {
    LAS unsigned* act = (LAS unsigned*)(L + L_ACT + w * ACT_W);
    act[2 * lane] = cvt_pk(n0, n0); act[2 * lane + 1] = cvt_pk(n1, n1);
    const int r = lane >> 4, n = lane & 15;
    const unsigned P0 = act[4 * n + r], P1 = act[64 + 4 * n + r];
    f32x4 acc = {0.f, 0.f, 0.f, 0.f};
    gemv16(acc, P0, mask, wq); gemv16(acc, P1, mask, wq + 16);
    LAS float* rd = red();
    if (lane < 32) *(LAS f32x4*)(rd + w * 128 + 8 * n + 4 * r) = acc;
    __syncthreads();
    if (tid < 64) {
      const float a = (rd[2 * tid] + rd[128 + 2 * tid]) + (rd[256 + 2 * tid] + rd[384 + 2 * tid]);
      const float b = (rd[2 * tid + 1] + rd[128 + 2 * tid + 1]) + (rd[256 + 2 * tid + 1] + rd[384 + 2 * tid + 1]);
      const unsigned pk = cvt_pk(a, b);
      st64(ws, qoff + tid * 8, pk, tag);
      ((LAS unsigned*)(L + L_QS))[qb * 64 + tid] = pk;
    }
    __syncthreads();
  }
  struct Att { f32x4 O; float nm, l; };
  __device__ __forceinline__ void att_unit(const u32x4 (&k)[4], const u32x4 (&v)[4], const u32x4 (&qa)[2], Att& a) {
    const int n = lane & 15, g = lane >> 4;
    f32x4 d0, d1;
    mfma_v0(d0, qa[0], k[0]); mfma_v0(d1, qa[0], k[2]); mfma_v(d0, qa[1], k[1]); mfma_v(d1, qa[1], k[3]);
    asm volatile("s_nop 7" : "+v"(d0), "+v"(d1));
    const float z0 = d0[0] + dppf<0xF5>(d0[1]), z1 = d1[0] + dppf<0xF5>(d1[1]);
    const bool used = ((n & 1) == 0) & ((n >> 3) == (g & 1));
    const float z = used ? (g >= 2 ? z1 : z0) : -INFINITY;
    const float nm = fminf(a.nm, -CS * wave_max(z));
    const float al = __builtin_amdgcn_exp2f(nm - a.nm);
    a.O *= al; a.l *= al; a.nm = nm;
    const float p = __builtin_amdgcn_exp2f(fmaf(z, CS, nm));
    a.l += p;
    unsigned pb = cvt_pk(p, p);
    pb = (unsigned)__builtin_amdgcn_update_dpp((int)pb, (int)pb, 0x128, 0xA, 0xf, false);
    const unsigned b0 = (unsigned)__builtin_amdgcn_update_dpp(0, (int)pb, 0x150, 0xf, 0xf, true), b1 = (unsigned)__builtin_amdgcn_update_dpp(0, (int)pb, 0x152, 0xf, 0xf, true);
    const unsigned b2 = (unsigned)__builtin_amdgcn_update_dpp(0, (int)pb, 0x154, 0xf, 0xf, true), b3 = (unsigned)__builtin_amdgcn_update_dpp(0, (int)pb, 0x156, 0xf, 0xf, true);
    asm volatile("s_nop 7" : "+v"(a.O));
    mfma_vn(a.O, u32x4{b0 & mask[0], b0 & mask[1], b0 & mask[2], b0 & mask[3]}, v[0]);
    mfma_vn(a.O, u32x4{b1 & mask[0], b1 & mask[1], b1 & mask[2], b1 & mask[3]}, v[1]);
    mfma_vn(a.O, u32x4{b2 & mask[0], b2 & mask[1], b2 & mask[2], b2 & mask[3]}, v[2]);
    mfma_vn(a.O, u32x4{b3 & mask[0], b3 & mask[1], b3 & mask[2], b3 & mask[3]}, v[3]);
    asm volatile("s_nop 7" : "+v"(a.O));
  }
  __device__ __forceinline__ void att_q(int qb, u32x4 (&qa)[2]) {
    const int n = lane & 15, g = lane >> 4;
    LAS unsigned* qs = (LAS unsigned*)(L + L_QS) + qb * 64 + 16 * (n & 1) + 4 * g;
    qa[0] = *(LAS u32x4*)qs; qa[1] = *(LAS u32x4*)(qs + 32);
  }
  __device__ __forceinline__ void att_out(const Att& a, int job) {
    LAS unsigned char* o = L + L_OWN + job * OWN_B;
    if (lane < 32) *(LAS f32x4*)(o + w * 512 + lane * 16) = a.O;
    const float l = wave_sum(a.l);
    if (lane == 0) { ((LAS float*)(o + 2048))[2 * w] = -a.nm; ((LAS float*)(o + 2048))[2 * w + 1] = l; }
  }
  template <int NU> __device__ __forceinline__ Att att_first(const Params& p, int b) {
    const int n = lane & 15, g = lane >> 4;
    const size_t off = (((size_t)b * NH + j) * SK + (size_t)(SK / 16 - SVC_UNITS + 4 * w) * 16) * 256;
    const unsigned char* kp = p.K + off + (n >> 1) * 256 + (n & 1) * 64 + g * 16;
    const unsigned char* vp = p.V + off + g * 1024 + n * 16;
    u32x4 qa[2]; att_q(b, qa);
    Att a; a.O = f32x4{0.f, 0.f, 0.f, 0.f}; a.nm = INFINITY; a.l = 0.f;
    LAS unsigned char* kv = L + L_KV + w * 32768 + lane * 16;
#pragma unroll 1
    for (int u0 = 0; u0 < 4; u0 += NU) {
      u32x4 k[NU][4], v[NU][4];
#pragma unroll
      for (int u = 0; u < NU; ++u) {
        const unsigned char* ku = kp + (u0 + u) * 4096; const unsigned char* vu = vp + (u0 + u) * 4096;
        k[u][0] = __builtin_nontemporal_load((const GAS u32x4*)ku); k[u][1] = __builtin_nontemporal_load((const GAS u32x4*)(ku + 128));
        k[u][2] = __builtin_nontemporal_load((const GAS u32x4*)(ku + 2048)); k[u][3] = __builtin_nontemporal_load((const GAS u32x4*)(ku + 2176));
#pragma unroll
        for (int i = 0; i < 4; ++i) v[u][i] = __builtin_nontemporal_load((const GAS u32x4*)(vu + 256 * i));
      }
      __builtin_amdgcn_sched_barrier(0);
#pragma unroll
      for (int u = 0; u < NU; ++u) {
#pragma unroll
        for (int i = 0; i < 4; ++i) { *(LAS u32x4*)(kv + (u0 + u) * 8192 + i * 1024) = k[u][i]; *(LAS u32x4*)(kv + (u0 + u) * 8192 + 4096 + i * 1024) = v[u][i]; }
        att_unit(k[u], v[u], qa, a);
      }
    }
    return a;
  }
  __device__ __forceinline__ void att_second(int b, int job) {
    u32x4 qa[2]; att_q(b, qa);
    Att a; a.O = f32x4{0.f, 0.f, 0.f, 0.f}; a.nm = INFINITY; a.l = 0.f;
    LAS unsigned char* kv = L + L_KV + w * 32768 + lane * 16;
#pragma unroll 1
    for (int u = 0; u < 4; ++u) {
      u32x4 k[4], v[4];
#pragma unroll
      for (int i = 0; i < 4; ++i) { k[i] = *(LAS u32x4*)(kv + u * 8192 + i * 1024); v[i] = *(LAS u32x4*)(kv + u * 8192 + 4096 + i * 1024); }
      att_unit(k, v, qa, a);
    }
    att_out(a, job);
  }
};
__device__ __forceinline__ u32x4 tr_frag(LAS unsigned char* t) {
  const u32x2 lo = __builtin_bit_cast(u32x2, __builtin_amdgcn_ds_read_tr16_b64_v4i16((LAS s16x4*)t));
  const u32x2 hi = __builtin_bit_cast(u32x2, __builtin_amdgcn_ds_read_tr16_b64_v4i16((LAS s16x4*)(t + 128)));
  return u32x4{lo[0], lo[1], hi[0], hi[1]};
}

__device__ __forceinline__ void service(const Params& p, LAS unsigned char* L, int j) {
  Svc s;
  s.tid = threadIdx.x; s.lane = s.tid & 63; s.w = __builtin_amdgcn_readfirstlane(s.tid >> 6); s.j = j; s.L = L; s.dead = false; s.rb = 0;
  s.ws = __builtin_amdgcn_make_buffer_rsrc(p.ws, 0, (int)WS_END, 0x00020000);
  const int w = s.w;
  int lane = s.lane, tid = s.tid, r = lane >> 4, n = lane & 15;
  u32x4 wq[32], wo[32];
  {
    const u16* src = p.wq + (size_t)(128 * w + r) * 4096 + 128 * j + 8 * n;
#pragma unroll
    for (int t = 0; t < 32; ++t) ld_a(wq[t], src + (size_t)t * (4 * 4096));
  }
  const unsigned epoch = __builtin_amdgcn_raw_buffer_load_b32(s.ws, WS_EPOCH, 0, 16);
  float x0[2][4][2];
#pragma unroll
  for (int b = 0; b < 2; ++b)
#pragma unroll
    for (int i = 0; i < 4; ++i) { const unsigned v = *(const GAS unsigned*)(p.x + b * DM + 128 * ((i + w) & 3) + 2 * lane); x0[b][i][0] = bf2f(v & 0xffffu); x0[b][i][1] = bf2f(v >> 16); }
  s.tag = __builtin_amdgcn_readfirstlane(epoch) + 1u;
#pragma unroll
  for (int d = 0; d < 4; ++d) s.mask[d] = (n >> 1) == d ? ((n & 1) ? 0xffff0000u : 0x0000ffffu) : 0u;
  float nq[2][2];
#pragma unroll
  for (int b = 0; b < 2; ++b) s.ln_own(x0[b], nq[b][0], nq[b][1]);
  asm volatile("s_waitcnt vmcnt(0)" ::: "memory");
  pin16(wq); pin16(wq + 16);
#pragma unroll
  for (int b = 0; b < 2; ++b) s.qproj(nq[b][0], nq[b][1], wq, WS_Q + (b * 32 + j) * 512, b);
  const int blk0 = j < 22 ? 3 * j : 66 + 2 * (j - 22), nblk = j < 22 ? 3 : 2;
  u32x4 gu[24], dn[11];
  {
    LAS unsigned char* stg = L + L_KV + w * 32768;
    const unsigned troff = (8 * r + (n >> 2)) * 32 + (n & 3) * 8;
    {
      u32x4 t[32];
#pragma unroll
      for (int i = 0; i < 32; ++i) t[i] = __builtin_nontemporal_load((const GAS u32x4*)(p.wo + (size_t)(128 * j + 4 * i + r) * DM + 128 * w + 8 * n));
      __builtin_amdgcn_sched_barrier(0);
#pragma unroll
      for (int i = 0; i < 32; ++i) *(LAS u32x4*)(stg + ((n >> 1) * 4 + (i >> 3)) * 1024 + (4 * (i & 7) + r) * 32 + (n & 1) * 16) = t[i];
#pragma unroll
      for (int i = 0; i < 32; ++i) { wo[i] = tr_frag(stg + i * 1024 + troff); asm volatile("" : "+a"(wo[i])); }
      asm volatile("" ::: "memory");
    }
    u32x4 t[32], d[11];
    {
      const int rw = lane >> 3, pc = lane & 7, pcc = min(pc, 2 * nblk - 1);
#pragma unroll
      for (int i = 0; i < 32; ++i) t[i] = __builtin_nontemporal_load((const GAS u32x4*)((i < 16 ? p.wg : p.wu) + (size_t)(128 * w + 8 * (i & 15) + rw) * FF + 16 * blk0 + 8 * pcc));
#pragma unroll
      for (int i = 0; i < 11; ++i) d[i] = __builtin_nontemporal_load((const GAS u32x4*)(p.wd + (size_t)(32 * min(11 * w + i, 42) + (lane >> 1)) * DM + 16 * j + 8 * (lane & 1)));
      __builtin_amdgcn_sched_barrier(0);
      if (pc < 6) {
#pragma unroll
        for (int i = 0; i < 32; ++i) *(LAS u32x4*)(stg + (((i >> 4) * 3 + (pc >> 1)) * 4 + ((i & 15) >> 2)) * 1024 + (8 * (i & 3) + rw) * 32 + (pc & 1) * 16) = t[i];
      }
    }
#pragma unroll
    for (int i = 0; i < 24; ++i) gu[i] = tr_frag(stg + i * 1024 + troff);
    asm volatile("" ::: "memory");
#pragma unroll
    for (int i = 0; i < 11; ++i) *(LAS u32x4*)(stg + i * 1024 + lane * 16) = d[i];
#pragma unroll
    for (int i = 0; i < 11; ++i) dn[i] = tr_frag(stg + i * 1024 + troff);
    asm volatile("" ::: "memory");
  }
  s.att_out(s.att_first<2>(p, 0), 0);
  LAS float* xr = (LAS float*)(L + L_XR);
  if (tid < 32) xr[tid] = bf2f(p.x[(tid >> 4) * DM + 16 * j + (tid & 15)]);
  __syncthreads();
  for (int job = 0; job < 4; ++job) {
    const int pass = job >> 1, b = job & 1;
    asm volatile("" : "+v"(tid));
    lane = tid & 63; r = lane >> 4; n = lane & 15; s.tid = tid; s.lane = lane;
    {
      const unsigned pb = WS_PART + (unsigned)(job * 32 + j) * PWAVES * PSTR;
      const unsigned mlo = pb + min(lane, NREC - 1) * PSTR + 1024;
      if (w == 0) {
        for (unsigned sp = 0;;) {
          const u32x4 ml = ld128(s.ws, mlo);
          const unsigned long long okm = __ballot((ml[1] == s.tag) & (ml[3] == s.tag));
          if (__builtin_popcountll(okm & ((1ull << NREC) - 1)) >= NREC - 2 || s.spin(sp)) break;
          __builtin_amdgcn_s_sleep(8);
        }
      }
      __syncthreads();
      const int half = w >> 1, d = (w & 1) * 64 + lane;
      const unsigned go = pb + 4 * half * PSTR + (((d & 3) >> 1) * 32 + (d >> 3) + 16 * ((d >> 2) & 1)) * 16 + (d & 1) * 8;
      const int cnt = half == 0 ? 4 : NREC - 4;
      u32x2 g[4]; u32x4 ml;
      for (unsigned sp = 0;;) {
        ml = ld128(s.ws, mlo);
        bool ok = (ml[1] == s.tag) & (ml[3] == s.tag);
#pragma unroll
        for (int i = 0; i < 4; ++i) g[i] = ld64(s.ws, go + min(i, cnt - 1) * PSTR);
#pragma unroll
        for (int i = 0; i < 4; ++i) ok &= g[i][1] == s.tag;
        if (__all(ok) || s.spin(sp)) break;
      }
      LAS unsigned char* own = L + L_OWN + job * OWN_B;
      const float omu = ((LAS float*)(own + 2048))[2 * (lane & 3)], ol = ((LAS float*)(own + 2048))[2 * (lane & 3) + 1];
      const bool isown = (lane >> 2) == 2;
      const float m = lane < NREC ? __uint_as_float(ml[0]) : isown ? omu : -INFINITY;
      const float M = wave_max(m);
      const float e = ((lane < NREC) | isown) ? __builtin_amdgcn_exp2f(m - M) : 0.f;
      const float inv = __builtin_amdgcn_rcpf(wave_sum(e * (isown ? ol : __uint_as_float(ml[2]))));
      float a0 = 0.f;
#pragma unroll
      for (int i = 0; i < 4; ++i) a0 = fmaf(i < cnt ? rdlane(e, 4 * half + i) : 0.f, __uint_as_float(g[i][0]), a0);
      {
        LAS float* oo = (LAS float*)own + 2 * half * 128 + ((d >> 3) + 16 * ((d >> 2) & 1)) * 4 + (d & 3);
        a0 = fmaf(rdlane(e, 8 + 2 * half), oo[0], a0); a0 = fmaf(rdlane(e, 9 + 2 * half), oo[128], a0);
      }
      LAS float* oh = (LAS float*)(L + L_OH);
      oh[half * 128 + d] = a0 * inv;
      __syncthreads();
      {
        LAS unsigned* act = (LAS unsigned*)(L + L_ACT + w * ACT_W);
        const float o0 = oh[2 * lane] + oh[128 + 2 * lane], o1 = oh[2 * lane + 1] + oh[128 + 2 * lane + 1];
        const unsigned hi = cvt_pk(o0, o1);
        act[lane] = hi; act[64 + lane] = cvt_pk(o0 - bf2f(hi & 0xffffu), o1 - bf2f(hi >> 16));
      }
      u32x4 af[4];
#pragma unroll
      for (int k = 0; k < 4; ++k) af[k] = *(LAS u32x4*)(L + L_ACT + w * ACT_W + (n == 1 ? 256 : 0) + 64 * k + 16 * r);
      f32x4 y[8];
#pragma unroll
      for (int nb = 0; nb < 8; ++nb) mfma_a0(y[nb], af[0], wo[nb * 4]);
#pragma unroll
      for (int k = 1; k < 4; ++k)
#pragma unroll
        for (int nb = 0; nb < 8; ++nb) mfma_a(y[nb], af[k], wo[nb * 4 + k]);
      asm volatile("s_nop 7" : "+v"(y[0]), "+v"(y[1]), "+v"(y[2]), "+v"(y[3]), "+v"(y[4]), "+v"(y[5]), "+v"(y[6]), "+v"(y[7]));
      LAS float* yt = (LAS float*)(L + L_YT + w * 512);
      if (lane < 16) {
#pragma unroll
        for (int nb = 0; nb < 8; ++nb) yt[16 * nb + lane] = y[nb][0] + y[nb][1];
      }
      const float y0 = yt[2 * lane], y1 = yt[2 * lane + 1];
      st128(s.ws, WS_Y + (unsigned)(job * 32 + j) * 4096 + (128 * w + 2 * lane) * 8, __float_as_uint(y0), __float_as_uint(y1), s.tag);
    }
    {
      u32x4 g[1];
      { const unsigned yo = WS_Y + (unsigned)(job * 32 + (tid >> 3)) * 4096 + (16 * j + 2 * (tid & 7)) * 8; s.gather2<1>(g, [&](int) { return yo; }); }
      float a = __uint_as_float(g[0][0]), c = __uint_as_float(g[0][2]);
      a += dppf<0x128>(a); c += dppf<0x128>(c);
      {
        auto t16 = __builtin_amdgcn_permlane16_swap(__float_as_uint(a), __float_as_uint(a), false, false);
        a = __uint_as_float(t16[0]) + __uint_as_float(t16[1]);
        auto u16_ = __builtin_amdgcn_permlane16_swap(__float_as_uint(c), __float_as_uint(c), false, false);
        c = __uint_as_float(u16_[0]) + __uint_as_float(u16_[1]);
        auto s32 = __builtin_amdgcn_permlane32_swap(__float_as_uint(a), __float_as_uint(a), false, false);
        a = __uint_as_float(s32[0]) + __uint_as_float(s32[1]);
        auto u32_ = __builtin_amdgcn_permlane32_swap(__float_as_uint(c), __float_as_uint(c), false, false);
        c = __uint_as_float(u32_[0]) + __uint_as_float(u32_[1]);
      }
      LAS float* rd = s.red();
      if (lane < 8) { rd[w * 16 + 2 * lane] = a; rd[w * 16 + 2 * lane + 1] = c; }
      __syncthreads();
      if (tid < 16) {
        const float v = xr[b * 16 + tid] + ((rd[tid] + rd[16 + tid]) + (rd[32 + tid] + rd[48 + tid]));
        xr[b * 16 + tid] = v;
        st64(s.ws, WS_X1 + job * 4096 + (16 * j + tid) * 8, __float_as_uint(v), s.tag);
      }
    }
    {
      float v[4][2]; s.gather512(WS_X1 + job * 4096, v);
      float n0, n1; s.ln_own(v, n0, n1);
      LAS unsigned* act = (LAS unsigned*)(L + L_ACT + w * ACT_W);
      const unsigned hi = cvt_pk(n0, n1);
      act[lane] = hi; act[64 + lane] = cvt_pk(n0 - bf2f(hi & 0xffffu), n1 - bf2f(hi >> 16));
      u32x4 a[4];
#pragma unroll
      for (int k = 0; k < 4; ++k) a[k] = *(LAS u32x4*)(L + L_ACT + w * ACT_W + (n == 1 ? 256 : 0) + 64 * k + 16 * r);
      f32x4 acc[6];
#pragma unroll
      for (int nb = 0; nb < 6; ++nb) mfma_v0(acc[nb], a[0], gu[nb * 4]);
#pragma unroll
      for (int k = 1; k < 4; ++k)
#pragma unroll
        for (int nb = 0; nb < 6; ++nb) mfma_v(acc[nb], a[k], gu[nb * 4 + k]);
      asm volatile("s_nop 7" : "+v"(acc[0]), "+v"(acc[1]), "+v"(acc[2]), "+v"(acc[3]), "+v"(acc[4]), "+v"(acc[5]));
      LAS float* rd = s.red();
      if (lane < 16) {
#pragma unroll
        for (int nb = 0; nb < 6; ++nb) rd[w * 96 + nb * 16 + lane] = acc[nb][0] + acc[nb][1];
      }
      __syncthreads();
      if (tid < 16 * nblk) {
        const float G = (rd[tid] + rd[96 + tid]) + (rd[192 + tid] + rd[288 + tid]);
        const float U = (rd[48 + tid] + rd[144 + tid]) + (rd[240 + tid] + rd[336 + tid]);
        st64(s.ws, WS_H + job * 11264 + (16 * blk0 + tid) * 8, __float_as_uint(G * U), s.tag);
      }
    }
    {
      const int np = w < 3 ? 176 : 160;
      u32x4 g[3];
      { const unsigned hb = WS_H + job * 11264 + 176 * w * 16; const int l_ = lane; s.gather2<3>(g, [&](int i) { return hb + min(l_ + 64 * i, np - 1) * 16; }); }
      LAS unsigned* act = (LAS unsigned*)(L + L_ACT + w * ACT_W);
#pragma unroll
      for (int i = 0; i < 3; ++i) if (lane + 64 * i < 176) {
        const bool in = lane + 64 * i < np;
        const float a = in ? __uint_as_float(g[i][0]) : 0.f, c = in ? __uint_as_float(g[i][2]) : 0.f;
        const unsigned hi = cvt_pk(a, c);
        act[lane + 64 * i] = hi; act[176 + lane + 64 * i] = cvt_pk(a - bf2f(hi & 0xffffu), c - bf2f(hi >> 16));
      }
      u32x4 a[11];
#pragma unroll
      for (int k = 0; k < 11; ++k) a[k] = *(LAS u32x4*)(L + L_ACT + w * ACT_W + (n == 1 ? 704 : 0) + 64 * k + 16 * r);
      f32x4 acc0, acc1;
      mfma_v0(acc0, a[0], dn[0]); mfma_v0(acc1, a[1], dn[1]);
#pragma unroll
      for (int k = 2; k < 11; ++k) { if (k & 1) mfma_v(acc1, a[k], dn[k]); else mfma_v(acc0, a[k], dn[k]); }
      asm volatile("s_nop 7" : "+v"(acc0), "+v"(acc1));
      LAS float* rd = s.red();
      if (lane < 16) rd[w * 16 + lane] = (acc0[0] + acc0[1]) + (acc1[0] + acc1[1]);
      __syncthreads();
      if (tid < 16) {
        const float v = xr[b * 16 + tid] + ((rd[tid] + rd[16 + tid]) + (rd[32 + tid] + rd[48 + tid]));
        xr[b * 16 + tid] = v;
        if (pass == 0) st64(s.ws, WS_X2 + b * 4096 + (16 * j + tid) * 8, __float_as_uint(v), s.tag);
        else p.out[b * DM + 16 * j + tid] = (u16)(cvt_pk(v, v) & 0xffffu);
      }
    }
    if (pass == 0) {
      float v[4][2]; s.gather512(WS_X2 + b * 4096, v);
      float n0, n1; s.ln_own(v, n0, n1);
      s.qproj(n0, n1, wq, WS_Q + ((2 + b) * 32 + j) * 512, b);
      s.att_second(b, 2 + b);
      if (b == 0) s.att_out(s.att_first<1>(p, 1), 1);
    }
  }
  if (j == 0 && tid == 0) __builtin_amdgcn_raw_buffer_store_b32(s.tag, s.ws, WS_EPOCH, 0, 16);
}

__global__ void __launch_bounds__(256, 1) k_main(Params p) {
  extern __shared__ __attribute__((aligned(16))) unsigned char smem[];
  if (blockIdx.x >= NSTREAM) { if (blockIdx.x < NSTREAM + NSERV) service(p, (LAS unsigned char*)smem, blockIdx.x - NSTREAM); return; }
  const int w = __builtin_amdgcn_readfirstlane(threadIdx.x >> 6);
  const int gw = blockIdx.x * 4 + w, h = gw / WPH, wi = gw % WPH;
  const int units = wi < N_HI ? U_HI : U_LO;
  const size_t kvoff = ((size_t)h * SK + (size_t)wi * 16) * 256;
  const unsigned char* kb = p.K + kvoff; const unsigned char* vb = p.V + kvoff;
  const unsigned char* qp = p.ws + WS_Q + h * 512;
  const unsigned char* pp = p.ws + WS_PART + (size_t)(h * PWAVES + (wi >> 2)) * PSTR;
  const unsigned char* rp = p.ws + WS_REP; const int rm = h * 1024 + (wi >> 2) * 128;
  const int nn = units - R_RET; const unsigned l0 = (unsigned)(size_t)(LAS unsigned char*)smem, lb = l0 + w * WAVE_LDS, xb = l0 + LDS_XCH;
  asm volatile(R"ASM(
s_mov_b64 s[20:21], %0
s_mov_b64 s[22:23], %1
s_mov_b64 s[64:65], %2
s_mov_b64 s[66:67], %3
s_mov_b32 s36, %4
s_mov_b64 s[62:63], %5
s_mov_b32 s57, %6
s_mov_b64 s[60:61], %7
s_mov_b32 s74, %8
s_lshl_b32 s75, %9, 9
s_add_u32 s75, s75, s74
s_mov_b64 s[78:79], %10
s_add_u32 s80, s78, %11
s_addc_u32 s81, s79, 0
s_memrealtime s[70:71]
s_waitcnt lgkmcnt(0)
s_mov_b32 s77, s70
v_mov_b32 v45, 0
global_load_dword v47, v45, s[62:63] offset:0 sc1
v_mbcnt_lo_u32_b32 v28, -1, 0
v_mbcnt_hi_u32_b32 v28, -1, v28
v_and_b32 v29, 15, v28
v_lshrrev_b32 v30, 4, v28
v_lshrrev_b32 v31, 1, v29
v_lshlrev_b32 v0, 8, v31
v_and_b32 v31, 1, v29
v_lshlrev_b32 v32, 6, v31
v_add_u32 v0, v0, v32
v_lshlrev_b32 v32, 4, v30
v_add_u32 v0, v0, v32
v_lshlrev_b32 v1, 10, v30
v_lshlrev_b32 v32, 4, v29
v_add_u32 v1, v1, v32
s_nop 1
s_add_u32 s24, s20, 134217728
s_addc_u32 s25, s21, 0
s_add_u32 s26, s22, 134217728
s_addc_u32 s27, s23, 0
s_add_u32 s58, s20, 0
s_addc_u32 s59, s21, 0
global_load_dwordx4 v[96:99], v0, s[58:59] offset:0 nt
global_load_dwordx4 v[100:103], v0, s[58:59] offset:128 nt
global_load_dwordx4 v[104:107], v0, s[58:59] offset:2048 nt
global_load_dwordx4 v[108:111], v0, s[58:59] offset:2176 nt
s_add_u32 s58, s22, 0
s_addc_u32 s59, s23, 0
global_load_dwordx4 v[112:115], v1, s[58:59] offset:0 nt
global_load_dwordx4 v[116:119], v1, s[58:59] offset:256 nt
global_load_dwordx4 v[120:123], v1, s[58:59] offset:512 nt
global_load_dwordx4 v[124:127], v1, s[58:59] offset:768 nt
s_add_u32 s58, s20, 114688
s_addc_u32 s59, s21, 0
global_load_dwordx4 v[128:131], v0, s[58:59] offset:0 nt
global_load_dwordx4 v[132:135], v0, s[58:59] offset:128 nt
global_load_dwordx4 v[136:139], v0, s[58:59] offset:2048 nt
global_load_dwordx4 v[140:143], v0, s[58:59] offset:2176 nt
s_add_u32 s58, s22, 114688
s_addc_u32 s59, s23, 0
global_load_dwordx4 v[144:147], v1, s[58:59] offset:0 nt
global_load_dwordx4 v[148:151], v1, s[58:59] offset:256 nt
global_load_dwordx4 v[152:155], v1, s[58:59] offset:512 nt
global_load_dwordx4 v[156:159], v1, s[58:59] offset:768 nt
v_and_b32 v31, 1, v29
v_lshlrev_b32 v31, 4, v31
v_mov_b32 v32, 0xffff
v_lshlrev_b32 v32, v31, v32
v_lshrrev_b32 v31, 1, v29
v_cmp_eq_u32 vcc, 0, v31
s_nop 1
v_cndmask_b32 v4, 0, v32, vcc
v_cmp_eq_u32 vcc, 1, v31
s_nop 1
v_cndmask_b32 v5, 0, v32, vcc
v_cmp_eq_u32 vcc, 2, v31
s_nop 1
v_cndmask_b32 v6, 0, v32, vcc
v_cmp_eq_u32 vcc, 3, v31
s_nop 1
v_cndmask_b32 v7, 0, v32, vcc
v_lshrrev_b32 v31, 3, v29
v_and_b32 v33, 1, v30
v_and_b32 v34, 1, v29
v_xor_b32 v31, v31, v33
v_or_b32 v31, v31, v34
v_cmp_eq_u32 s[40:41], 0, v31
s_mov_b32 s38, 0x3e0293ee
s_mov_b32 s56, 0x4000
s_mov_b32 s69, 92
s_memrealtime s[70:71]
s_waitcnt lgkmcnt(0)
s_add_u32 s68, s70, 92
v_mov_b32 v34, s74
v_mov_b32 v32, 0
v_mov_b32 v33, 0
ds_write_b64 v34, v[32:33] offset:2112
ds_write_b64 v34, v[32:33] offset:2120
s_waitcnt lgkmcnt(0)
s_barrier
s_getpc_b64 s[44:45]
Lpc0_%=:
s_add_u32 s46, s44, LrareB_%=-Lpc0_%=
s_addc_u32 s47, s45, 0
s_add_u32 s44, s44, LrareA_%=-Lpc0_%=
s_addc_u32 s45, s45, 0
s_nop 5
s_waitcnt vmcnt(16)
v_readfirstlane_b32 s37, v47
s_nop 0
s_add_u32 s37, s37, 1
v_mov_b32_e32 v36, 0
v_mov_b32_e32 v37, 0
v_mov_b32_e32 v38, 0
v_mov_b32_e32 v39, 0
v_mov_b32_e32 v46, 0
v_mov_b32_e32 v2, 0x7f800000
v_mov_b32_e32 v40, 0
v_mov_b32_e32 v41, 0
v_mov_b32_e32 v42, 0
v_mov_b32_e32 v43, 0
v_mov_b32_e32 v47, 0
v_mov_b32_e32 v3, 0x7f800000
s_waitcnt vmcnt(8)
s_add_u32 s58, s20, 229376
s_addc_u32 s59, s21, 0
Lp1_%=:
s_memrealtime s[70:71]
s_waitcnt lgkmcnt(0)
s_sub_u32 s72, s68, s70
s_cmp_gt_i32 s72, 0
s_cbranch_scc0 Lg2_%=
s_sleep 1
s_branch Lp1_%=
Lg2_%=:
s_add_u32 s68, s68, s69
global_load_dwordx4 v[160:163], v0, s[58:59] offset:0 nt
global_load_dwordx4 v[164:167], v0, s[58:59] offset:128 nt
global_load_dwordx4 v[168:171], v0, s[58:59] offset:2048 nt
global_load_dwordx4 v[172:175], v0, s[58:59] offset:2176 nt
s_add_u32 s58, s22, 229376
s_addc_u32 s59, s23, 0
global_load_dwordx4 v[176:179], v1, s[58:59] offset:0 nt
global_load_dwordx4 v[180:183], v1, s[58:59] offset:256 nt
global_load_dwordx4 v[184:187], v1, s[58:59] offset:512 nt
global_load_dwordx4 v[188:191], v1, s[58:59] offset:768 nt
s_waitcnt vmcnt(8)
s_add_u32 s58, s20, 344064
s_addc_u32 s59, s21, 0
Lp3_%=:
s_memrealtime s[70:71]
s_waitcnt lgkmcnt(0)
s_sub_u32 s72, s68, s70
s_cmp_gt_i32 s72, 0
s_cbranch_scc0 Lg4_%=
s_sleep 1
s_branch Lp3_%=
Lg4_%=:
s_add_u32 s68, s68, s69
global_load_dwordx4 v[192:195], v0, s[58:59] offset:0 nt
global_load_dwordx4 v[196:199], v0, s[58:59] offset:128 nt
global_load_dwordx4 v[200:203], v0, s[58:59] offset:2048 nt
global_load_dwordx4 v[204:207], v0, s[58:59] offset:2176 nt
s_add_u32 s58, s22, 344064
s_addc_u32 s59, s23, 0
global_load_dwordx4 v[208:211], v1, s[58:59] offset:0 nt
global_load_dwordx4 v[212:215], v1, s[58:59] offset:256 nt
global_load_dwordx4 v[216:219], v1, s[58:59] offset:512 nt
global_load_dwordx4 v[220:223], v1, s[58:59] offset:768 nt
s_waitcnt vmcnt(8)
s_memrealtime s[70:71]
s_waitcnt lgkmcnt(0)
s_mov_b32 s76, s70
s_add_u32 s58, s20, 458752
s_addc_u32 s59, s21, 0
Lp5_%=:
s_memrealtime s[70:71]
s_waitcnt lgkmcnt(0)
s_sub_u32 s72, s68, s70
s_cmp_gt_i32 s72, 0
s_cbranch_scc0 Lg6_%=
s_sleep 1
s_branch Lp5_%=
Lg6_%=:
s_add_u32 s68, s68, s69
global_load_dwordx4 v[224:227], v0, s[58:59] offset:0 nt
global_load_dwordx4 v[228:231], v0, s[58:59] offset:128 nt
global_load_dwordx4 v[232:235], v0, s[58:59] offset:2048 nt
global_load_dwordx4 v[236:239], v0, s[58:59] offset:2176 nt
s_add_u32 s58, s22, 458752
s_addc_u32 s59, s23, 0
global_load_dwordx4 v[240:243], v1, s[58:59] offset:0 nt
global_load_dwordx4 v[244:247], v1, s[58:59] offset:256 nt
global_load_dwordx4 v[248:251], v1, s[58:59] offset:512 nt
global_load_dwordx4 v[252:255], v1, s[58:59] offset:768 nt
s_waitcnt vmcnt(8)
s_add_u32 s58, s20, 573440
s_addc_u32 s59, s21, 0
Lp7_%=:
s_memrealtime s[70:71]
s_waitcnt lgkmcnt(0)
s_sub_u32 s72, s68, s70
s_cmp_gt_i32 s72, 0
s_cbranch_scc0 Lg8_%=
s_sleep 1
s_branch Lp7_%=
Lg8_%=:
s_add_u32 s68, s68, s69
global_load_dwordx4 a[0:3], v0, s[58:59] offset:0 nt
global_load_dwordx4 a[4:7], v0, s[58:59] offset:128 nt
global_load_dwordx4 a[8:11], v0, s[58:59] offset:2048 nt
global_load_dwordx4 a[12:15], v0, s[58:59] offset:2176 nt
s_add_u32 s58, s22, 573440
s_addc_u32 s59, s23, 0
global_load_dwordx4 a[16:19], v1, s[58:59] offset:0 nt
global_load_dwordx4 a[20:23], v1, s[58:59] offset:256 nt
global_load_dwordx4 a[24:27], v1, s[58:59] offset:512 nt
global_load_dwordx4 a[28:31], v1, s[58:59] offset:768 nt
s_waitcnt vmcnt(8)
s_add_u32 s58, s20, 688128
s_addc_u32 s59, s21, 0
Lp9_%=:
s_memrealtime s[70:71]
s_waitcnt lgkmcnt(0)
s_sub_u32 s72, s68, s70
s_cmp_gt_i32 s72, 0
s_cbranch_scc0 Lg10_%=
s_sleep 1
s_branch Lp9_%=
Lg10_%=:
s_add_u32 s68, s68, s69
global_load_dwordx4 a[32:35], v0, s[58:59] offset:0 nt
global_load_dwordx4 a[36:39], v0, s[58:59] offset:128 nt
global_load_dwordx4 a[40:43], v0, s[58:59] offset:2048 nt
global_load_dwordx4 a[44:47], v0, s[58:59] offset:2176 nt
s_add_u32 s58, s22, 688128
s_addc_u32 s59, s23, 0
global_load_dwordx4 a[48:51], v1, s[58:59] offset:0 nt
global_load_dwordx4 a[52:55], v1, s[58:59] offset:256 nt
global_load_dwordx4 a[56:59], v1, s[58:59] offset:512 nt
global_load_dwordx4 a[60:63], v1, s[58:59] offset:768 nt
s_waitcnt vmcnt(8)
s_add_u32 s58, s20, 802816
s_addc_u32 s59, s21, 0
Lp11_%=:
s_memrealtime s[70:71]
s_waitcnt lgkmcnt(0)
s_sub_u32 s72, s68, s70
s_cmp_gt_i32 s72, 0
s_cbranch_scc0 Lg12_%=
s_sleep 1
s_branch Lp11_%=
Lg12_%=:
s_add_u32 s68, s68, s69
global_load_dwordx4 a[64:67], v0, s[58:59] offset:0 nt
global_load_dwordx4 a[68:71], v0, s[58:59] offset:128 nt
global_load_dwordx4 a[72:75], v0, s[58:59] offset:2048 nt
global_load_dwordx4 a[76:79], v0, s[58:59] offset:2176 nt
s_add_u32 s58, s22, 802816
s_addc_u32 s59, s23, 0
global_load_dwordx4 a[80:83], v1, s[58:59] offset:0 nt
global_load_dwordx4 a[84:87], v1, s[58:59] offset:256 nt
global_load_dwordx4 a[88:91], v1, s[58:59] offset:512 nt
global_load_dwordx4 a[92:95], v1, s[58:59] offset:768 nt
s_waitcnt vmcnt(8)
s_add_u32 s58, s20, 917504
s_addc_u32 s59, s21, 0
Lp13_%=:
s_memrealtime s[70:71]
s_waitcnt lgkmcnt(0)
s_sub_u32 s72, s68, s70
s_cmp_gt_i32 s72, 0
s_cbranch_scc0 Lg14_%=
s_sleep 1
s_branch Lp13_%=
Lg14_%=:
s_add_u32 s68, s68, s69
global_load_dwordx4 a[96:99], v0, s[58:59] offset:0 nt
global_load_dwordx4 a[100:103], v0, s[58:59] offset:128 nt
global_load_dwordx4 a[104:107], v0, s[58:59] offset:2048 nt
global_load_dwordx4 a[108:111], v0, s[58:59] offset:2176 nt
s_add_u32 s58, s22, 917504
s_addc_u32 s59, s23, 0
global_load_dwordx4 a[112:115], v1, s[58:59] offset:0 nt
global_load_dwordx4 a[116:119], v1, s[58:59] offset:256 nt
global_load_dwordx4 a[120:123], v1, s[58:59] offset:512 nt
global_load_dwordx4 a[124:127], v1, s[58:59] offset:768 nt
s_waitcnt vmcnt(8)
s_add_u32 s58, s20, 1032192
s_addc_u32 s59, s21, 0
Lp15_%=:
s_memrealtime s[70:71]
s_waitcnt lgkmcnt(0)
s_sub_u32 s72, s68, s70
s_cmp_gt_i32 s72, 0
s_cbranch_scc0 Lg16_%=
s_sleep 1
s_branch Lp15_%=
Lg16_%=:
s_add_u32 s68, s68, s69
global_load_dwordx4 a[128:131], v0, s[58:59] offset:0 nt
global_load_dwordx4 a[132:135], v0, s[58:59] offset:128 nt
global_load_dwordx4 a[136:139], v0, s[58:59] offset:2048 nt
global_load_dwordx4 a[140:143], v0, s[58:59] offset:2176 nt
s_add_u32 s58, s22, 1032192
s_addc_u32 s59, s23, 0
global_load_dwordx4 a[144:147], v1, s[58:59] offset:0 nt
global_load_dwordx4 a[148:151], v1, s[58:59] offset:256 nt
global_load_dwordx4 a[152:155], v1, s[58:59] offset:512 nt
global_load_dwordx4 a[156:159], v1, s[58:59] offset:768 nt
s_add_u32 s58, s64, 0
s_addc_u32 s59, s65, 0
v_mbcnt_lo_u32_b32 v10, -1, 0
v_mbcnt_hi_u32_b32 v10, -1, v10
v_lshlrev_b32_e32 v10, 3, v10
global_load_dwordx2 v[8:9], v10, s[58:59] offset:0 sc1
s_add_u32 s58, s64, 16384
s_addc_u32 s59, s65, 0
v_mbcnt_lo_u32_b32 v18, -1, 0
v_mbcnt_hi_u32_b32 v18, -1, v18
v_lshlrev_b32_e32 v18, 3, v18
global_load_dwordx2 v[16:17], v18, s[58:59] offset:0 sc1
s_waitcnt vmcnt(10)
s_add_u32 s58, s20, 1146880
s_addc_u32 s59, s21, 0
Lp17_%=:
s_memrealtime s[70:71]
s_waitcnt lgkmcnt(0)
s_sub_u32 s72, s68, s70
s_cmp_gt_i32 s72, 0
s_cbranch_scc0 Lg18_%=
s_sleep 1
s_branch Lp17_%=
Lg18_%=:
s_add_u32 s68, s68, s69
global_load_dwordx4 a[160:163], v0, s[58:59] offset:0 nt
global_load_dwordx4 a[164:167], v0, s[58:59] offset:128 nt
global_load_dwordx4 a[168:171], v0, s[58:59] offset:2048 nt
global_load_dwordx4 a[172:175], v0, s[58:59] offset:2176 nt
s_add_u32 s58, s22, 1146880
s_addc_u32 s59, s23, 0
global_load_dwordx4 a[176:179], v1, s[58:59] offset:0 nt
global_load_dwordx4 a[180:183], v1, s[58:59] offset:256 nt
global_load_dwordx4 a[184:187], v1, s[58:59] offset:512 nt
global_load_dwordx4 a[188:191], v1, s[58:59] offset:768 nt
s_waitcnt vmcnt(10)
s_add_u32 s58, s20, 1261568
s_addc_u32 s59, s21, 0
Lp19_%=:
s_memrealtime s[70:71]
s_waitcnt lgkmcnt(0)
s_sub_u32 s72, s68, s70
s_cmp_gt_i32 s72, 0
s_cbranch_scc0 Lg20_%=
s_sleep 1
s_branch Lp19_%=
Lg20_%=:
s_add_u32 s68, s68, s69
global_load_dwordx4 a[192:195], v0, s[58:59] offset:0 nt
global_load_dwordx4 a[196:199], v0, s[58:59] offset:128 nt
global_load_dwordx4 a[200:203], v0, s[58:59] offset:2048 nt
global_load_dwordx4 a[204:207], v0, s[58:59] offset:2176 nt
s_add_u32 s58, s22, 1261568
s_addc_u32 s59, s23, 0
global_load_dwordx4 a[208:211], v1, s[58:59] offset:0 nt
global_load_dwordx4 a[212:215], v1, s[58:59] offset:256 nt
global_load_dwordx4 a[216:219], v1, s[58:59] offset:512 nt
global_load_dwordx4 a[220:223], v1, s[58:59] offset:768 nt
s_waitcnt vmcnt(8)
s_memrealtime s[70:71]
s_waitcnt lgkmcnt(0)
s_sub_u32 s70, s70, s76
v_mov_b32_e32 v45, s74
v_mov_b32_e32 v44, s70
s_mov_b64 exec, 1
ds_add_u32 v45, v44 offset:2120
s_waitcnt lgkmcnt(0)
v_mov_b32_e32 v44, 1
ds_add_rtn_u32 v44, v45, v44 offset:2124
s_waitcnt lgkmcnt(0)
v_readfirstlane_b32 s48, v44
s_mov_b64 exec, -1
s_cmp_eq_u32 s48, 3
s_nop 1
s_cbranch_scc0 Lnr21_%=
v_mov_b32_e32 v45, s74
ds_read_b32 v28, v45 offset:2120
v_mov_b32_e32 v29, s37
v_mov_b32_e32 v30, 0
v_mov_b32_e32 v31, s37
s_waitcnt lgkmcnt(0)
v_mbcnt_lo_u32_b32 v45, -1, 0
v_mbcnt_hi_u32_b32 v45, -1, v45
v_lshlrev_b32_e32 v45, 4, v45
s_mov_b64 exec, 0xff
global_store_dwordx4 v45, v[28:31], s[80:81] offset:0 sc1
s_mov_b64 exec, -1
s_nop 3
Lnr21_%=:
s_nop 1
v_cmp_ne_u32_e32 vcc, s37, v9
s_nop 0
s_cbranch_vccz Lok22_%=
s_add_u32 s58, s64, 0
s_addc_u32 s59, s65, 0
Lag23_%=:
s_sleep 2
global_load_dwordx2 v[8:9], v10, s[58:59] offset:0 sc1
s_waitcnt vmcnt(0)
v_cmp_ne_u32_e32 vcc, s37, v9
s_cbranch_vccz Lok22_%=
s_sub_u32 s56, s56, 1
s_cbranch_scc0 Lag23_%=
s_mov_b32 s56, 0
Lok22_%=:
s_nop 1
v_mov_b32_e32 v45, v8
v_mbcnt_lo_u32_b32 v44, -1, 0
v_mbcnt_hi_u32_b32 v44, -1, v44
v_and_b32_e32 v8, 1, v44
v_lshlrev_b32_e32 v8, 6, v8
v_and_b32_e32 v44, 48, v44
v_add_u32_e32 v44, v8, v44
ds_bpermute_b32 v8, v44, v45
ds_bpermute_b32 v9, v44, v45 offset:4
ds_bpermute_b32 v10, v44, v45 offset:8
ds_bpermute_b32 v11, v44, v45 offset:12
ds_bpermute_b32 v12, v44, v45 offset:128
ds_bpermute_b32 v13, v44, v45 offset:132
ds_bpermute_b32 v14, v44, v45 offset:136
ds_bpermute_b32 v15, v44, v45 offset:140
s_waitcnt lgkmcnt(0)
v_cmp_ne_u32_e32 vcc, s37, v17
s_nop 0
s_cbranch_vccz Lok24_%=
s_add_u32 s58, s64, 16384
s_addc_u32 s59, s65, 0
Lag25_%=:
s_sleep 2
global_load_dwordx2 v[16:17], v18, s[58:59] offset:0 sc1
s_waitcnt vmcnt(0)
v_cmp_ne_u32_e32 vcc, s37, v17
s_cbranch_vccz Lok24_%=
s_sub_u32 s56, s56, 1
s_cbranch_scc0 Lag25_%=
s_mov_b32 s56, 0
Lok24_%=:
s_nop 1
v_mov_b32_e32 v45, v16
v_mbcnt_lo_u32_b32 v44, -1, 0
v_mbcnt_hi_u32_b32 v44, -1, v44
v_and_b32_e32 v16, 1, v44
v_lshlrev_b32_e32 v16, 6, v16
v_and_b32_e32 v44, 48, v44
v_add_u32_e32 v44, v16, v44
ds_bpermute_b32 v16, v44, v45
ds_bpermute_b32 v17, v44, v45 offset:4
ds_bpermute_b32 v18, v44, v45 offset:8
ds_bpermute_b32 v19, v44, v45 offset:12
ds_bpermute_b32 v20, v44, v45 offset:128
ds_bpermute_b32 v21, v44, v45 offset:132
ds_bpermute_b32 v22, v44, v45 offset:136
ds_bpermute_b32 v23, v44, v45 offset:140
s_waitcnt lgkmcnt(0)
v_mfma_f32_16x16x32_bf16 v[28:31], v[8:11], v[96:99], 0
v_mfma_f32_16x16x32_bf16 v[32:35], v[8:11], v[104:107], 0
v_mfma_f32_16x16x32_bf16 v[28:31], v[12:15], v[100:103], v[28:31]
v_mfma_f32_16x16x32_bf16 v[32:35], v[12:15], v[108:111], v[32:35]
s_nop 6
v_add_f32_dpp v44, v29, v28 quad_perm:[1,1,3,3] row_mask:0x3 bank_mask:0xf
s_nop 1
v_add_f32_dpp v44, v33, v32 quad_perm:[1,1,3,3] row_mask:0xc bank_mask:0xf
v_fma_f32 v45, v44, s38, v2
v_cmp_lt_f32_e32 vcc, 0, v45
s_nop 0
s_and_b64 s[48:49], vcc, s[40:41]
s_cbranch_scc0 Lc26_%=
s_swappc_b64 s[42:43], s[44:45]
Lc26_%=:
v_exp_f32_e32 v45, v45
s_nop 0
v_cndmask_b32_e64 v45, 0, v45, s[40:41]
v_add_f32_e32 v46, v46, v45
v_cvt_pk_bf16_f32 v44, v45, v45
s_nop 1
v_mov_b32_dpp v44, v44 row_ror:8 row_mask:0xa bank_mask:0xf
s_nop 1
v_and_b32_dpp v28, v44, v4 row_newbcast:0 row_mask:0xf bank_mask:0xf
v_and_b32_dpp v29, v44, v5 row_newbcast:0 row_mask:0xf bank_mask:0xf
v_and_b32_dpp v30, v44, v6 row_newbcast:0 row_mask:0xf bank_mask:0xf
v_and_b32_dpp v31, v44, v7 row_newbcast:0 row_mask:0xf bank_mask:0xf
v_and_b32_dpp v32, v44, v4 row_newbcast:2 row_mask:0xf bank_mask:0xf
v_and_b32_dpp v33, v44, v5 row_newbcast:2 row_mask:0xf bank_mask:0xf
v_and_b32_dpp v34, v44, v6 row_newbcast:2 row_mask:0xf bank_mask:0xf
v_and_b32_dpp v35, v44, v7 row_newbcast:2 row_mask:0xf bank_mask:0xf
v_mfma_f32_16x16x32_bf16 v[36:39], v[28:31], v[112:115], v[36:39]
v_and_b32_dpp v28, v44, v4 row_newbcast:4 row_mask:0xf bank_mask:0xf
v_and_b32_dpp v29, v44, v5 row_newbcast:4 row_mask:0xf bank_mask:0xf
v_and_b32_dpp v30, v44, v6 row_newbcast:4 row_mask:0xf bank_mask:0xf
v_and_b32_dpp v31, v44, v7 row_newbcast:4 row_mask:0xf bank_mask:0xf
v_mfma_f32_16x16x32_bf16 v[36:39], v[32:35], v[116:119], v[36:39]
v_and_b32_dpp v32, v44, v4 row_newbcast:6 row_mask:0xf bank_mask:0xf
v_and_b32_dpp v33, v44, v5 row_newbcast:6 row_mask:0xf bank_mask:0xf
v_and_b32_dpp v34, v44, v6 row_newbcast:6 row_mask:0xf bank_mask:0xf
v_and_b32_dpp v35, v44, v7 row_newbcast:6 row_mask:0xf bank_mask:0xf
v_mfma_f32_16x16x32_bf16 v[36:39], v[28:31], v[120:123], v[36:39]
s_nop 0
v_mfma_f32_16x16x32_bf16 v[36:39], v[32:35], v[124:127], v[36:39]
s_add_u32 s58, s20, 1376256
s_addc_u32 s59, s21, 0
Lp27_%=:
s_memrealtime s[70:71]
s_waitcnt lgkmcnt(0)
s_sub_u32 s72, s68, s70
s_cmp_gt_i32 s72, 0
s_cbranch_scc0 Lg28_%=
s_sleep 1
s_branch Lp27_%=
Lg28_%=:
s_add_u32 s68, s68, s69
global_load_dwordx4 a[224:227], v0, s[58:59] offset:0 nt
global_load_dwordx4 a[228:231], v0, s[58:59] offset:128 nt
global_load_dwordx4 a[232:235], v0, s[58:59] offset:2048 nt
global_load_dwordx4 a[236:239], v0, s[58:59] offset:2176 nt
s_add_u32 s58, s22, 1376256
s_addc_u32 s59, s23, 0
global_load_dwordx4 a[240:243], v1, s[58:59] offset:0 nt
global_load_dwordx4 a[244:247], v1, s[58:59] offset:256 nt
global_load_dwordx4 a[248:251], v1, s[58:59] offset:512 nt
global_load_dwordx4 a[252:255], v1, s[58:59] offset:768 nt
v_mfma_f32_16x16x32_bf16 v[28:31], v[8:11], v[128:131], 0
v_mfma_f32_16x16x32_bf16 v[32:35], v[8:11], v[136:139], 0
v_mfma_f32_16x16x32_bf16 v[28:31], v[12:15], v[132:135], v[28:31]
v_mfma_f32_16x16x32_bf16 v[32:35], v[12:15], v[140:143], v[32:35]
s_nop 6
v_add_f32_dpp v44, v29, v28 quad_perm:[1,1,3,3] row_mask:0x3 bank_mask:0xf
s_nop 1
v_add_f32_dpp v44, v33, v32 quad_perm:[1,1,3,3] row_mask:0xc bank_mask:0xf
v_fma_f32 v45, v44, s38, v2
v_cmp_lt_f32_e32 vcc, 0, v45
s_nop 0
s_and_b64 s[48:49], vcc, s[40:41]
s_cbranch_scc0 Lc29_%=
s_swappc_b64 s[42:43], s[44:45]
Lc29_%=:
v_exp_f32_e32 v45, v45
s_nop 0
v_cndmask_b32_e64 v45, 0, v45, s[40:41]
v_add_f32_e32 v46, v46, v45
v_cvt_pk_bf16_f32 v44, v45, v45
s_nop 1
v_mov_b32_dpp v44, v44 row_ror:8 row_mask:0xa bank_mask:0xf
s_nop 1
v_and_b32_dpp v28, v44, v4 row_newbcast:0 row_mask:0xf bank_mask:0xf
v_and_b32_dpp v29, v44, v5 row_newbcast:0 row_mask:0xf bank_mask:0xf
v_and_b32_dpp v30, v44, v6 row_newbcast:0 row_mask:0xf bank_mask:0xf
v_and_b32_dpp v31, v44, v7 row_newbcast:0 row_mask:0xf bank_mask:0xf
v_and_b32_dpp v32, v44, v4 row_newbcast:2 row_mask:0xf bank_mask:0xf
v_and_b32_dpp v33, v44, v5 row_newbcast:2 row_mask:0xf bank_mask:0xf
v_and_b32_dpp v34, v44, v6 row_newbcast:2 row_mask:0xf bank_mask:0xf
v_and_b32_dpp v35, v44, v7 row_newbcast:2 row_mask:0xf bank_mask:0xf
v_mfma_f32_16x16x32_bf16 v[36:39], v[28:31], v[144:147], v[36:39]
v_and_b32_dpp v28, v44, v4 row_newbcast:4 row_mask:0xf bank_mask:0xf
v_and_b32_dpp v29, v44, v5 row_newbcast:4 row_mask:0xf bank_mask:0xf
v_and_b32_dpp v30, v44, v6 row_newbcast:4 row_mask:0xf bank_mask:0xf
v_and_b32_dpp v31, v44, v7 row_newbcast:4 row_mask:0xf bank_mask:0xf
v_mfma_f32_16x16x32_bf16 v[36:39], v[32:35], v[148:151], v[36:39]
v_and_b32_dpp v32, v44, v4 row_newbcast:6 row_mask:0xf bank_mask:0xf
v_and_b32_dpp v33, v44, v5 row_newbcast:6 row_mask:0xf bank_mask:0xf
v_and_b32_dpp v34, v44, v6 row_newbcast:6 row_mask:0xf bank_mask:0xf
v_and_b32_dpp v35, v44, v7 row_newbcast:6 row_mask:0xf bank_mask:0xf
v_mfma_f32_16x16x32_bf16 v[36:39], v[28:31], v[152:155], v[36:39]
s_nop 0
v_mfma_f32_16x16x32_bf16 v[36:39], v[32:35], v[156:159], v[36:39]
s_add_u32 s58, s20, 1490944
s_addc_u32 s59, s21, 0
Lp30_%=:
s_memrealtime s[70:71]
s_waitcnt lgkmcnt(0)
s_sub_u32 s72, s68, s70
s_cmp_gt_i32 s72, 0
s_cbranch_scc0 Lg31_%=
s_sleep 1
s_branch Lp30_%=
Lg31_%=:
s_add_u32 s68, s68, s69
global_load_dwordx4 v[48:51], v0, s[58:59] offset:0 nt
global_load_dwordx4 v[52:55], v0, s[58:59] offset:128 nt
global_load_dwordx4 v[56:59], v0, s[58:59] offset:2048 nt
global_load_dwordx4 v[60:63], v0, s[58:59] offset:2176 nt
s_add_u32 s58, s22, 1490944
s_addc_u32 s59, s23, 0
global_load_dwordx4 v[64:67], v1, s[58:59] offset:0 nt
global_load_dwordx4 v[68:71], v1, s[58:59] offset:256 nt
global_load_dwordx4 v[72:75], v1, s[58:59] offset:512 nt
global_load_dwordx4 v[76:79], v1, s[58:59] offset:768 nt
v_mfma_f32_16x16x32_bf16 v[28:31], v[8:11], v[160:163], 0
v_mfma_f32_16x16x32_bf16 v[32:35], v[8:11], v[168:171], 0
v_mfma_f32_16x16x32_bf16 v[28:31], v[12:15], v[164:167], v[28:31]
v_mfma_f32_16x16x32_bf16 v[32:35], v[12:15], v[172:175], v[32:35]
s_nop 6
v_add_f32_dpp v44, v29, v28 quad_perm:[1,1,3,3] row_mask:0x3 bank_mask:0xf
s_nop 1
v_add_f32_dpp v44, v33, v32 quad_perm:[1,1,3,3] row_mask:0xc bank_mask:0xf
v_fma_f32 v45, v44, s38, v2
v_cmp_lt_f32_e32 vcc, 0, v45
s_nop 0
s_and_b64 s[48:49], vcc, s[40:41]
s_cbranch_scc0 Lc32_%=
s_swappc_b64 s[42:43], s[44:45]
Lc32_%=:
v_exp_f32_e32 v45, v45
s_nop 0
v_cndmask_b32_e64 v45, 0, v45, s[40:41]
v_add_f32_e32 v46, v46, v45
v_cvt_pk_bf16_f32 v44, v45, v45
s_nop 1
v_mov_b32_dpp v44, v44 row_ror:8 row_mask:0xa bank_mask:0xf
s_nop 1
v_and_b32_dpp v28, v44, v4 row_newbcast:0 row_mask:0xf bank_mask:0xf
v_and_b32_dpp v29, v44, v5 row_newbcast:0 row_mask:0xf bank_mask:0xf
v_and_b32_dpp v30, v44, v6 row_newbcast:0 row_mask:0xf bank_mask:0xf
v_and_b32_dpp v31, v44, v7 row_newbcast:0 row_mask:0xf bank_mask:0xf
v_and_b32_dpp v32, v44, v4 row_newbcast:2 row_mask:0xf bank_mask:0xf
v_and_b32_dpp v33, v44, v5 row_newbcast:2 row_mask:0xf bank_mask:0xf
v_and_b32_dpp v34, v44, v6 row_newbcast:2 row_mask:0xf bank_mask:0xf
v_and_b32_dpp v35, v44, v7 row_newbcast:2 row_mask:0xf bank_mask:0xf
v_mfma_f32_16x16x32_bf16 v[36:39], v[28:31], v[176:179], v[36:39]
v_and_b32_dpp v28, v44, v4 row_newbcast:4 row_mask:0xf bank_mask:0xf
v_and_b32_dpp v29, v44, v5 row_newbcast:4 row_mask:0xf bank_mask:0xf
v_and_b32_dpp v30, v44, v6 row_newbcast:4 row_mask:0xf bank_mask:0xf
v_and_b32_dpp v31, v44, v7 row_newbcast:4 row_mask:0xf bank_mask:0xf
v_mfma_f32_16x16x32_bf16 v[36:39], v[32:35], v[180:183], v[36:39]
v_and_b32_dpp v32, v44, v4 row_newbcast:6 row_mask:0xf bank_mask:0xf
v_and_b32_dpp v33, v44, v5 row_newbcast:6 row_mask:0xf bank_mask:0xf
v_and_b32_dpp v34, v44, v6 row_newbcast:6 row_mask:0xf bank_mask:0xf
v_and_b32_dpp v35, v44, v7 row_newbcast:6 row_mask:0xf bank_mask:0xf
v_mfma_f32_16x16x32_bf16 v[36:39], v[28:31], v[184:187], v[36:39]
s_nop 0
v_mfma_f32_16x16x32_bf16 v[36:39], v[32:35], v[188:191], v[36:39]
s_add_u32 s58, s20, 1605632
s_addc_u32 s59, s21, 0
Lp33_%=:
s_memrealtime s[70:71]
s_waitcnt lgkmcnt(0)
s_sub_u32 s72, s68, s70
s_cmp_gt_i32 s72, 0
s_cbranch_scc0 Lg34_%=
s_sleep 1
s_branch Lp33_%=
Lg34_%=:
s_add_u32 s68, s68, s69
global_load_dwordx4 v[80:83], v0, s[58:59] offset:0 nt
global_load_dwordx4 v[84:87], v0, s[58:59] offset:128 nt
global_load_dwordx4 v[88:91], v0, s[58:59] offset:2048 nt
global_load_dwordx4 v[92:95], v0, s[58:59] offset:2176 nt
v_mfma_f32_16x16x32_bf16 v[28:31], v[8:11], v[192:195], 0
v_mfma_f32_16x16x32_bf16 v[32:35], v[8:11], v[200:203], 0
v_mfma_f32_16x16x32_bf16 v[28:31], v[12:15], v[196:199], v[28:31]
v_mfma_f32_16x16x32_bf16 v[32:35], v[12:15], v[204:207], v[32:35]
s_nop 6
v_add_f32_dpp v44, v29, v28 quad_perm:[1,1,3,3] row_mask:0x3 bank_mask:0xf
s_nop 1
v_add_f32_dpp v44, v33, v32 quad_perm:[1,1,3,3] row_mask:0xc bank_mask:0xf
v_fma_f32 v45, v44, s38, v2
v_cmp_lt_f32_e32 vcc, 0, v45
s_nop 0
s_and_b64 s[48:49], vcc, s[40:41]
s_cbranch_scc0 Lc35_%=
s_swappc_b64 s[42:43], s[44:45]
Lc35_%=:
v_exp_f32_e32 v45, v45
s_nop 0
v_cndmask_b32_e64 v45, 0, v45, s[40:41]
v_add_f32_e32 v46, v46, v45
v_cvt_pk_bf16_f32 v44, v45, v45
s_nop 1
v_mov_b32_dpp v44, v44 row_ror:8 row_mask:0xa bank_mask:0xf
s_nop 1
v_and_b32_dpp v28, v44, v4 row_newbcast:0 row_mask:0xf bank_mask:0xf
v_and_b32_dpp v29, v44, v5 row_newbcast:0 row_mask:0xf bank_mask:0xf
v_and_b32_dpp v30, v44, v6 row_newbcast:0 row_mask:0xf bank_mask:0xf
v_and_b32_dpp v31, v44, v7 row_newbcast:0 row_mask:0xf bank_mask:0xf
v_and_b32_dpp v32, v44, v4 row_newbcast:2 row_mask:0xf bank_mask:0xf
v_and_b32_dpp v33, v44, v5 row_newbcast:2 row_mask:0xf bank_mask:0xf
v_and_b32_dpp v34, v44, v6 row_newbcast:2 row_mask:0xf bank_mask:0xf
v_and_b32_dpp v35, v44, v7 row_newbcast:2 row_mask:0xf bank_mask:0xf
v_mfma_f32_16x16x32_bf16 v[36:39], v[28:31], v[208:211], v[36:39]
v_and_b32_dpp v28, v44, v4 row_newbcast:4 row_mask:0xf bank_mask:0xf
v_and_b32_dpp v29, v44, v5 row_newbcast:4 row_mask:0xf bank_mask:0xf
v_and_b32_dpp v30, v44, v6 row_newbcast:4 row_mask:0xf bank_mask:0xf
v_and_b32_dpp v31, v44, v7 row_newbcast:4 row_mask:0xf bank_mask:0xf
v_mfma_f32_16x16x32_bf16 v[36:39], v[32:35], v[212:215], v[36:39]
v_and_b32_dpp v32, v44, v4 row_newbcast:6 row_mask:0xf bank_mask:0xf
v_and_b32_dpp v33, v44, v5 row_newbcast:6 row_mask:0xf bank_mask:0xf
v_and_b32_dpp v34, v44, v6 row_newbcast:6 row_mask:0xf bank_mask:0xf
v_and_b32_dpp v35, v44, v7 row_newbcast:6 row_mask:0xf bank_mask:0xf
v_mfma_f32_16x16x32_bf16 v[36:39], v[28:31], v[216:219], v[36:39]
s_nop 0
v_mfma_f32_16x16x32_bf16 v[36:39], v[32:35], v[220:223], v[36:39]
v_mfma_f32_16x16x32_bf16 v[28:31], v[8:11], v[224:227], 0
v_mfma_f32_16x16x32_bf16 v[32:35], v[8:11], v[232:235], 0
v_mfma_f32_16x16x32_bf16 v[28:31], v[12:15], v[228:231], v[28:31]
v_mfma_f32_16x16x32_bf16 v[32:35], v[12:15], v[236:239], v[32:35]
s_nop 6
v_add_f32_dpp v44, v29, v28 quad_perm:[1,1,3,3] row_mask:0x3 bank_mask:0xf
s_nop 1
v_add_f32_dpp v44, v33, v32 quad_perm:[1,1,3,3] row_mask:0xc bank_mask:0xf
v_fma_f32 v45, v44, s38, v2
v_cmp_lt_f32_e32 vcc, 0, v45
s_nop 0
s_and_b64 s[48:49], vcc, s[40:41]
s_cbranch_scc0 Lc36_%=
s_swappc_b64 s[42:43], s[44:45]
Lc36_%=:
v_exp_f32_e32 v45, v45
s_nop 0
v_cndmask_b32_e64 v45, 0, v45, s[40:41]
v_add_f32_e32 v46, v46, v45
v_cvt_pk_bf16_f32 v44, v45, v45
s_nop 1
v_mov_b32_dpp v44, v44 row_ror:8 row_mask:0xa bank_mask:0xf
s_nop 1
v_and_b32_dpp v28, v44, v4 row_newbcast:0 row_mask:0xf bank_mask:0xf
v_and_b32_dpp v29, v44, v5 row_newbcast:0 row_mask:0xf bank_mask:0xf
v_and_b32_dpp v30, v44, v6 row_newbcast:0 row_mask:0xf bank_mask:0xf
v_and_b32_dpp v31, v44, v7 row_newbcast:0 row_mask:0xf bank_mask:0xf
v_and_b32_dpp v32, v44, v4 row_newbcast:2 row_mask:0xf bank_mask:0xf
v_and_b32_dpp v33, v44, v5 row_newbcast:2 row_mask:0xf bank_mask:0xf
v_and_b32_dpp v34, v44, v6 row_newbcast:2 row_mask:0xf bank_mask:0xf
v_and_b32_dpp v35, v44, v7 row_newbcast:2 row_mask:0xf bank_mask:0xf
v_mfma_f32_16x16x32_bf16 v[36:39], v[28:31], v[240:243], v[36:39]
v_and_b32_dpp v28, v44, v4 row_newbcast:4 row_mask:0xf bank_mask:0xf
v_and_b32_dpp v29, v44, v5 row_newbcast:4 row_mask:0xf bank_mask:0xf
v_and_b32_dpp v30, v44, v6 row_newbcast:4 row_mask:0xf bank_mask:0xf
v_and_b32_dpp v31, v44, v7 row_newbcast:4 row_mask:0xf bank_mask:0xf
v_mfma_f32_16x16x32_bf16 v[36:39], v[32:35], v[244:247], v[36:39]
v_and_b32_dpp v32, v44, v4 row_newbcast:6 row_mask:0xf bank_mask:0xf
v_and_b32_dpp v33, v44, v5 row_newbcast:6 row_mask:0xf bank_mask:0xf
v_and_b32_dpp v34, v44, v6 row_newbcast:6 row_mask:0xf bank_mask:0xf
v_and_b32_dpp v35, v44, v7 row_newbcast:6 row_mask:0xf bank_mask:0xf
v_mfma_f32_16x16x32_bf16 v[36:39], v[28:31], v[248:251], v[36:39]
s_nop 0
v_mfma_f32_16x16x32_bf16 v[36:39], v[32:35], v[252:255], v[36:39]
v_mfma_f32_16x16x32_bf16 v[28:31], v[8:11], a[0:3], 0
v_mfma_f32_16x16x32_bf16 v[32:35], v[8:11], a[8:11], 0
v_mfma_f32_16x16x32_bf16 v[28:31], v[12:15], a[4:7], v[28:31]
v_mfma_f32_16x16x32_bf16 v[32:35], v[12:15], a[12:15], v[32:35]
s_nop 6
v_add_f32_dpp v44, v29, v28 quad_perm:[1,1,3,3] row_mask:0x3 bank_mask:0xf
s_nop 1
v_add_f32_dpp v44, v33, v32 quad_perm:[1,1,3,3] row_mask:0xc bank_mask:0xf
v_fma_f32 v45, v44, s38, v2
v_cmp_lt_f32_e32 vcc, 0, v45
s_nop 0
s_and_b64 s[48:49], vcc, s[40:41]
s_cbranch_scc0 Lc37_%=
s_swappc_b64 s[42:43], s[44:45]
Lc37_%=:
v_exp_f32_e32 v45, v45
s_nop 0
v_cndmask_b32_e64 v45, 0, v45, s[40:41]
v_add_f32_e32 v46, v46, v45
v_cvt_pk_bf16_f32 v44, v45, v45
s_nop 1
v_mov_b32_dpp v44, v44 row_ror:8 row_mask:0xa bank_mask:0xf
s_nop 1
v_and_b32_dpp v28, v44, v4 row_newbcast:0 row_mask:0xf bank_mask:0xf
v_and_b32_dpp v29, v44, v5 row_newbcast:0 row_mask:0xf bank_mask:0xf
v_and_b32_dpp v30, v44, v6 row_newbcast:0 row_mask:0xf bank_mask:0xf
v_and_b32_dpp v31, v44, v7 row_newbcast:0 row_mask:0xf bank_mask:0xf
v_and_b32_dpp v32, v44, v4 row_newbcast:2 row_mask:0xf bank_mask:0xf
v_and_b32_dpp v33, v44, v5 row_newbcast:2 row_mask:0xf bank_mask:0xf
v_and_b32_dpp v34, v44, v6 row_newbcast:2 row_mask:0xf bank_mask:0xf
v_and_b32_dpp v35, v44, v7 row_newbcast:2 row_mask:0xf bank_mask:0xf
v_mfma_f32_16x16x32_bf16 v[36:39], v[28:31], a[16:19], v[36:39]
v_and_b32_dpp v28, v44, v4 row_newbcast:4 row_mask:0xf bank_mask:0xf
v_and_b32_dpp v29, v44, v5 row_newbcast:4 row_mask:0xf bank_mask:0xf
v_and_b32_dpp v30, v44, v6 row_newbcast:4 row_mask:0xf bank_mask:0xf
v_and_b32_dpp v31, v44, v7 row_newbcast:4 row_mask:0xf bank_mask:0xf
v_mfma_f32_16x16x32_bf16 v[36:39], v[32:35], a[20:23], v[36:39]
v_and_b32_dpp v32, v44, v4 row_newbcast:6 row_mask:0xf bank_mask:0xf
v_and_b32_dpp v33, v44, v5 row_newbcast:6 row_mask:0xf bank_mask:0xf
v_and_b32_dpp v34, v44, v6 row_newbcast:6 row_mask:0xf bank_mask:0xf
v_and_b32_dpp v35, v44, v7 row_newbcast:6 row_mask:0xf bank_mask:0xf
v_mfma_f32_16x16x32_bf16 v[36:39], v[28:31], a[24:27], v[36:39]
s_nop 0
v_mfma_f32_16x16x32_bf16 v[36:39], v[32:35], a[28:31], v[36:39]
v_mfma_f32_16x16x32_bf16 v[28:31], v[8:11], a[32:35], 0
v_mfma_f32_16x16x32_bf16 v[32:35], v[8:11], a[40:43], 0
v_mfma_f32_16x16x32_bf16 v[28:31], v[12:15], a[36:39], v[28:31]
v_mfma_f32_16x16x32_bf16 v[32:35], v[12:15], a[44:47], v[32:35]
s_nop 6
v_add_f32_dpp v44, v29, v28 quad_perm:[1,1,3,3] row_mask:0x3 bank_mask:0xf
s_nop 1
v_add_f32_dpp v44, v33, v32 quad_perm:[1,1,3,3] row_mask:0xc bank_mask:0xf
v_fma_f32 v45, v44, s38, v2
v_cmp_lt_f32_e32 vcc, 0, v45
s_nop 0
s_and_b64 s[48:49], vcc, s[40:41]
s_cbranch_scc0 Lc38_%=
s_swappc_b64 s[42:43], s[44:45]
Lc38_%=:
v_exp_f32_e32 v45, v45
s_nop 0
v_cndmask_b32_e64 v45, 0, v45, s[40:41]
v_add_f32_e32 v46, v46, v45
v_cvt_pk_bf16_f32 v44, v45, v45
s_nop 1
v_mov_b32_dpp v44, v44 row_ror:8 row_mask:0xa bank_mask:0xf
s_nop 1
v_and_b32_dpp v28, v44, v4 row_newbcast:0 row_mask:0xf bank_mask:0xf
v_and_b32_dpp v29, v44, v5 row_newbcast:0 row_mask:0xf bank_mask:0xf
v_and_b32_dpp v30, v44, v6 row_newbcast:0 row_mask:0xf bank_mask:0xf
v_and_b32_dpp v31, v44, v7 row_newbcast:0 row_mask:0xf bank_mask:0xf
v_and_b32_dpp v32, v44, v4 row_newbcast:2 row_mask:0xf bank_mask:0xf
v_and_b32_dpp v33, v44, v5 row_newbcast:2 row_mask:0xf bank_mask:0xf
v_and_b32_dpp v34, v44, v6 row_newbcast:2 row_mask:0xf bank_mask:0xf
v_and_b32_dpp v35, v44, v7 row_newbcast:2 row_mask:0xf bank_mask:0xf
v_mfma_f32_16x16x32_bf16 v[36:39], v[28:31], a[48:51], v[36:39]
v_and_b32_dpp v28, v44, v4 row_newbcast:4 row_mask:0xf bank_mask:0xf
v_and_b32_dpp v29, v44, v5 row_newbcast:4 row_mask:0xf bank_mask:0xf
v_and_b32_dpp v30, v44, v6 row_newbcast:4 row_mask:0xf bank_mask:0xf
v_and_b32_dpp v31, v44, v7 row_newbcast:4 row_mask:0xf bank_mask:0xf
v_mfma_f32_16x16x32_bf16 v[36:39], v[32:35], a[52:55], v[36:39]
v_and_b32_dpp v32, v44, v4 row_newbcast:6 row_mask:0xf bank_mask:0xf
v_and_b32_dpp v33, v44, v5 row_newbcast:6 row_mask:0xf bank_mask:0xf
v_and_b32_dpp v34, v44, v6 row_newbcast:6 row_mask:0xf bank_mask:0xf
v_and_b32_dpp v35, v44, v7 row_newbcast:6 row_mask:0xf bank_mask:0xf
v_mfma_f32_16x16x32_bf16 v[36:39], v[28:31], a[56:59], v[36:39]
s_nop 0
v_mfma_f32_16x16x32_bf16 v[36:39], v[32:35], a[60:63], v[36:39]
v_mfma_f32_16x16x32_bf16 v[28:31], v[8:11], a[64:67], 0
v_mfma_f32_16x16x32_bf16 v[32:35], v[8:11], a[72:75], 0
v_mfma_f32_16x16x32_bf16 v[28:31], v[12:15], a[68:71], v[28:31]
v_mfma_f32_16x16x32_bf16 v[32:35], v[12:15], a[76:79], v[32:35]
s_nop 6
v_add_f32_dpp v44, v29, v28 quad_perm:[1,1,3,3] row_mask:0x3 bank_mask:0xf
s_nop 1
v_add_f32_dpp v44, v33, v32 quad_perm:[1,1,3,3] row_mask:0xc bank_mask:0xf
v_fma_f32 v45, v44, s38, v2
v_cmp_lt_f32_e32 vcc, 0, v45
s_nop 0
s_and_b64 s[48:49], vcc, s[40:41]
s_cbranch_scc0 Lc39_%=
s_swappc_b64 s[42:43], s[44:45]
Lc39_%=:
v_exp_f32_e32 v45, v45
s_nop 0
v_cndmask_b32_e64 v45, 0, v45, s[40:41]
v_add_f32_e32 v46, v46, v45
v_cvt_pk_bf16_f32 v44, v45, v45
s_nop 1
v_mov_b32_dpp v44, v44 row_ror:8 row_mask:0xa bank_mask:0xf
s_nop 1
v_and_b32_dpp v28, v44, v4 row_newbcast:0 row_mask:0xf bank_mask:0xf
v_and_b32_dpp v29, v44, v5 row_newbcast:0 row_mask:0xf bank_mask:0xf
v_and_b32_dpp v30, v44, v6 row_newbcast:0 row_mask:0xf bank_mask:0xf
v_and_b32_dpp v31, v44, v7 row_newbcast:0 row_mask:0xf bank_mask:0xf
v_and_b32_dpp v32, v44, v4 row_newbcast:2 row_mask:0xf bank_mask:0xf
v_and_b32_dpp v33, v44, v5 row_newbcast:2 row_mask:0xf bank_mask:0xf
v_and_b32_dpp v34, v44, v6 row_newbcast:2 row_mask:0xf bank_mask:0xf
v_and_b32_dpp v35, v44, v7 row_newbcast:2 row_mask:0xf bank_mask:0xf
v_mfma_f32_16x16x32_bf16 v[36:39], v[28:31], a[80:83], v[36:39]
v_and_b32_dpp v28, v44, v4 row_newbcast:4 row_mask:0xf bank_mask:0xf
v_and_b32_dpp v29, v44, v5 row_newbcast:4 row_mask:0xf bank_mask:0xf
v_and_b32_dpp v30, v44, v6 row_newbcast:4 row_mask:0xf bank_mask:0xf
v_and_b32_dpp v31, v44, v7 row_newbcast:4 row_mask:0xf bank_mask:0xf
v_mfma_f32_16x16x32_bf16 v[36:39], v[32:35], a[84:87], v[36:39]
v_and_b32_dpp v32, v44, v4 row_newbcast:6 row_mask:0xf bank_mask:0xf
v_and_b32_dpp v33, v44, v5 row_newbcast:6 row_mask:0xf bank_mask:0xf
v_and_b32_dpp v34, v44, v6 row_newbcast:6 row_mask:0xf bank_mask:0xf
v_and_b32_dpp v35, v44, v7 row_newbcast:6 row_mask:0xf bank_mask:0xf
v_mfma_f32_16x16x32_bf16 v[36:39], v[28:31], a[88:91], v[36:39]
s_nop 0
v_mfma_f32_16x16x32_bf16 v[36:39], v[32:35], a[92:95], v[36:39]
v_mfma_f32_16x16x32_bf16 v[28:31], v[8:11], a[96:99], 0
v_mfma_f32_16x16x32_bf16 v[32:35], v[8:11], a[104:107], 0
v_mfma_f32_16x16x32_bf16 v[28:31], v[12:15], a[100:103], v[28:31]
v_mfma_f32_16x16x32_bf16 v[32:35], v[12:15], a[108:111], v[32:35]
s_nop 6
v_add_f32_dpp v44, v29, v28 quad_perm:[1,1,3,3] row_mask:0x3 bank_mask:0xf
s_nop 1
v_add_f32_dpp v44, v33, v32 quad_perm:[1,1,3,3] row_mask:0xc bank_mask:0xf
v_fma_f32 v45, v44, s38, v2
v_cmp_lt_f32_e32 vcc, 0, v45
s_nop 0
s_and_b64 s[48:49], vcc, s[40:41]
s_cbranch_scc0 Lc40_%=
s_swappc_b64 s[42:43], s[44:45]
Lc40_%=:
v_exp_f32_e32 v45, v45
s_nop 0
v_cndmask_b32_e64 v45, 0, v45, s[40:41]
v_add_f32_e32 v46, v46, v45
v_cvt_pk_bf16_f32 v44, v45, v45
s_nop 1
v_mov_b32_dpp v44, v44 row_ror:8 row_mask:0xa bank_mask:0xf
s_nop 1
v_and_b32_dpp v28, v44, v4 row_newbcast:0 row_mask:0xf bank_mask:0xf
v_and_b32_dpp v29, v44, v5 row_newbcast:0 row_mask:0xf bank_mask:0xf
v_and_b32_dpp v30, v44, v6 row_newbcast:0 row_mask:0xf bank_mask:0xf
v_and_b32_dpp v31, v44, v7 row_newbcast:0 row_mask:0xf bank_mask:0xf
v_and_b32_dpp v32, v44, v4 row_newbcast:2 row_mask:0xf bank_mask:0xf
v_and_b32_dpp v33, v44, v5 row_newbcast:2 row_mask:0xf bank_mask:0xf
v_and_b32_dpp v34, v44, v6 row_newbcast:2 row_mask:0xf bank_mask:0xf
v_and_b32_dpp v35, v44, v7 row_newbcast:2 row_mask:0xf bank_mask:0xf
v_mfma_f32_16x16x32_bf16 v[36:39], v[28:31], a[112:115], v[36:39]
v_and_b32_dpp v28, v44, v4 row_newbcast:4 row_mask:0xf bank_mask:0xf
v_and_b32_dpp v29, v44, v5 row_newbcast:4 row_mask:0xf bank_mask:0xf
v_and_b32_dpp v30, v44, v6 row_newbcast:4 row_mask:0xf bank_mask:0xf
v_and_b32_dpp v31, v44, v7 row_newbcast:4 row_mask:0xf bank_mask:0xf
v_mfma_f32_16x16x32_bf16 v[36:39], v[32:35], a[116:119], v[36:39]
v_and_b32_dpp v32, v44, v4 row_newbcast:6 row_mask:0xf bank_mask:0xf
v_and_b32_dpp v33, v44, v5 row_newbcast:6 row_mask:0xf bank_mask:0xf
v_and_b32_dpp v34, v44, v6 row_newbcast:6 row_mask:0xf bank_mask:0xf
v_and_b32_dpp v35, v44, v7 row_newbcast:6 row_mask:0xf bank_mask:0xf
v_mfma_f32_16x16x32_bf16 v[36:39], v[28:31], a[120:123], v[36:39]
s_nop 0
v_mfma_f32_16x16x32_bf16 v[36:39], v[32:35], a[124:127], v[36:39]
v_mfma_f32_16x16x32_bf16 v[28:31], v[8:11], a[128:131], 0
v_mfma_f32_16x16x32_bf16 v[32:35], v[8:11], a[136:139], 0
v_mfma_f32_16x16x32_bf16 v[28:31], v[12:15], a[132:135], v[28:31]
v_mfma_f32_16x16x32_bf16 v[32:35], v[12:15], a[140:143], v[32:35]
s_nop 6
v_add_f32_dpp v44, v29, v28 quad_perm:[1,1,3,3] row_mask:0x3 bank_mask:0xf
s_nop 1
v_add_f32_dpp v44, v33, v32 quad_perm:[1,1,3,3] row_mask:0xc bank_mask:0xf
v_fma_f32 v45, v44, s38, v2
v_cmp_lt_f32_e32 vcc, 0, v45
s_nop 0
s_and_b64 s[48:49], vcc, s[40:41]
s_cbranch_scc0 Lc41_%=
s_swappc_b64 s[42:43], s[44:45]
Lc41_%=:
v_exp_f32_e32 v45, v45
s_nop 0
v_cndmask_b32_e64 v45, 0, v45, s[40:41]
v_add_f32_e32 v46, v46, v45
v_cvt_pk_bf16_f32 v44, v45, v45
s_nop 1
v_mov_b32_dpp v44, v44 row_ror:8 row_mask:0xa bank_mask:0xf
s_nop 1
v_and_b32_dpp v28, v44, v4 row_newbcast:0 row_mask:0xf bank_mask:0xf
v_and_b32_dpp v29, v44, v5 row_newbcast:0 row_mask:0xf bank_mask:0xf
v_and_b32_dpp v30, v44, v6 row_newbcast:0 row_mask:0xf bank_mask:0xf
v_and_b32_dpp v31, v44, v7 row_newbcast:0 row_mask:0xf bank_mask:0xf
v_and_b32_dpp v32, v44, v4 row_newbcast:2 row_mask:0xf bank_mask:0xf
v_and_b32_dpp v33, v44, v5 row_newbcast:2 row_mask:0xf bank_mask:0xf
v_and_b32_dpp v34, v44, v6 row_newbcast:2 row_mask:0xf bank_mask:0xf
v_and_b32_dpp v35, v44, v7 row_newbcast:2 row_mask:0xf bank_mask:0xf
v_mfma_f32_16x16x32_bf16 v[36:39], v[28:31], a[144:147], v[36:39]
v_and_b32_dpp v28, v44, v4 row_newbcast:4 row_mask:0xf bank_mask:0xf
v_and_b32_dpp v29, v44, v5 row_newbcast:4 row_mask:0xf bank_mask:0xf
v_and_b32_dpp v30, v44, v6 row_newbcast:4 row_mask:0xf bank_mask:0xf
v_and_b32_dpp v31, v44, v7 row_newbcast:4 row_mask:0xf bank_mask:0xf
v_mfma_f32_16x16x32_bf16 v[36:39], v[32:35], a[148:151], v[36:39]
v_and_b32_dpp v32, v44, v4 row_newbcast:6 row_mask:0xf bank_mask:0xf
v_and_b32_dpp v33, v44, v5 row_newbcast:6 row_mask:0xf bank_mask:0xf
v_and_b32_dpp v34, v44, v6 row_newbcast:6 row_mask:0xf bank_mask:0xf
v_and_b32_dpp v35, v44, v7 row_newbcast:6 row_mask:0xf bank_mask:0xf
v_mfma_f32_16x16x32_bf16 v[36:39], v[28:31], a[152:155], v[36:39]
s_nop 0
v_mfma_f32_16x16x32_bf16 v[36:39], v[32:35], a[156:159], v[36:39]
v_mfma_f32_16x16x32_bf16 v[28:31], v[8:11], a[160:163], 0
v_mfma_f32_16x16x32_bf16 v[32:35], v[8:11], a[168:171], 0
v_mfma_f32_16x16x32_bf16 v[28:31], v[12:15], a[164:167], v[28:31]
v_mfma_f32_16x16x32_bf16 v[32:35], v[12:15], a[172:175], v[32:35]
s_nop 6
v_add_f32_dpp v44, v29, v28 quad_perm:[1,1,3,3] row_mask:0x3 bank_mask:0xf
s_nop 1
v_add_f32_dpp v44, v33, v32 quad_perm:[1,1,3,3] row_mask:0xc bank_mask:0xf
v_fma_f32 v45, v44, s38, v2
v_cmp_lt_f32_e32 vcc, 0, v45
s_nop 0
s_and_b64 s[48:49], vcc, s[40:41]
s_cbranch_scc0 Lc42_%=
s_swappc_b64 s[42:43], s[44:45]
Lc42_%=:
v_exp_f32_e32 v45, v45
s_nop 0
v_cndmask_b32_e64 v45, 0, v45, s[40:41]
v_add_f32_e32 v46, v46, v45
v_cvt_pk_bf16_f32 v44, v45, v45
s_nop 1
v_mov_b32_dpp v44, v44 row_ror:8 row_mask:0xa bank_mask:0xf
s_nop 1
v_and_b32_dpp v28, v44, v4 row_newbcast:0 row_mask:0xf bank_mask:0xf
v_and_b32_dpp v29, v44, v5 row_newbcast:0 row_mask:0xf bank_mask:0xf
v_and_b32_dpp v30, v44, v6 row_newbcast:0 row_mask:0xf bank_mask:0xf
v_and_b32_dpp v31, v44, v7 row_newbcast:0 row_mask:0xf bank_mask:0xf
v_and_b32_dpp v32, v44, v4 row_newbcast:2 row_mask:0xf bank_mask:0xf
v_and_b32_dpp v33, v44, v5 row_newbcast:2 row_mask:0xf bank_mask:0xf
v_and_b32_dpp v34, v44, v6 row_newbcast:2 row_mask:0xf bank_mask:0xf
v_and_b32_dpp v35, v44, v7 row_newbcast:2 row_mask:0xf bank_mask:0xf
v_mfma_f32_16x16x32_bf16 v[36:39], v[28:31], a[176:179], v[36:39]
v_and_b32_dpp v28, v44, v4 row_newbcast:4 row_mask:0xf bank_mask:0xf
v_and_b32_dpp v29, v44, v5 row_newbcast:4 row_mask:0xf bank_mask:0xf
v_and_b32_dpp v30, v44, v6 row_newbcast:4 row_mask:0xf bank_mask:0xf
v_and_b32_dpp v31, v44, v7 row_newbcast:4 row_mask:0xf bank_mask:0xf
v_mfma_f32_16x16x32_bf16 v[36:39], v[32:35], a[180:183], v[36:39]
v_and_b32_dpp v32, v44, v4 row_newbcast:6 row_mask:0xf bank_mask:0xf
v_and_b32_dpp v33, v44, v5 row_newbcast:6 row_mask:0xf bank_mask:0xf
v_and_b32_dpp v34, v44, v6 row_newbcast:6 row_mask:0xf bank_mask:0xf
v_and_b32_dpp v35, v44, v7 row_newbcast:6 row_mask:0xf bank_mask:0xf
v_mfma_f32_16x16x32_bf16 v[36:39], v[28:31], a[184:187], v[36:39]
s_nop 0
v_mfma_f32_16x16x32_bf16 v[36:39], v[32:35], a[188:191], v[36:39]
s_waitcnt vmcnt(24)
v_mfma_f32_16x16x32_bf16 v[28:31], v[8:11], a[192:195], 0
v_mfma_f32_16x16x32_bf16 v[32:35], v[8:11], a[200:203], 0
v_mfma_f32_16x16x32_bf16 v[28:31], v[12:15], a[196:199], v[28:31]
v_mfma_f32_16x16x32_bf16 v[32:35], v[12:15], a[204:207], v[32:35]
s_nop 6
v_add_f32_dpp v44, v29, v28 quad_perm:[1,1,3,3] row_mask:0x3 bank_mask:0xf
s_nop 1
v_add_f32_dpp v44, v33, v32 quad_perm:[1,1,3,3] row_mask:0xc bank_mask:0xf
v_fma_f32 v45, v44, s38, v2
v_cmp_lt_f32_e32 vcc, 0, v45
s_nop 0
s_and_b64 s[48:49], vcc, s[40:41]
s_cbranch_scc0 Lc43_%=
s_swappc_b64 s[42:43], s[44:45]
Lc43_%=:
v_exp_f32_e32 v45, v45
s_nop 0
v_cndmask_b32_e64 v45, 0, v45, s[40:41]
v_add_f32_e32 v46, v46, v45
v_cvt_pk_bf16_f32 v44, v45, v45
s_nop 1
v_mov_b32_dpp v44, v44 row_ror:8 row_mask:0xa bank_mask:0xf
s_waitcnt vmcnt(20)
s_nop 0
v_and_b32_dpp v28, v44, v4 row_newbcast:0 row_mask:0xf bank_mask:0xf
v_and_b32_dpp v29, v44, v5 row_newbcast:0 row_mask:0xf bank_mask:0xf
v_and_b32_dpp v30, v44, v6 row_newbcast:0 row_mask:0xf bank_mask:0xf
v_and_b32_dpp v31, v44, v7 row_newbcast:0 row_mask:0xf bank_mask:0xf
v_and_b32_dpp v32, v44, v4 row_newbcast:2 row_mask:0xf bank_mask:0xf
v_and_b32_dpp v33, v44, v5 row_newbcast:2 row_mask:0xf bank_mask:0xf
v_and_b32_dpp v34, v44, v6 row_newbcast:2 row_mask:0xf bank_mask:0xf
v_and_b32_dpp v35, v44, v7 row_newbcast:2 row_mask:0xf bank_mask:0xf
v_mfma_f32_16x16x32_bf16 v[36:39], v[28:31], a[208:211], v[36:39]
v_and_b32_dpp v28, v44, v4 row_newbcast:4 row_mask:0xf bank_mask:0xf
v_and_b32_dpp v29, v44, v5 row_newbcast:4 row_mask:0xf bank_mask:0xf
v_and_b32_dpp v30, v44, v6 row_newbcast:4 row_mask:0xf bank_mask:0xf
v_and_b32_dpp v31, v44, v7 row_newbcast:4 row_mask:0xf bank_mask:0xf
v_mfma_f32_16x16x32_bf16 v[36:39], v[32:35], a[212:215], v[36:39]
v_and_b32_dpp v32, v44, v4 row_newbcast:6 row_mask:0xf bank_mask:0xf
v_and_b32_dpp v33, v44, v5 row_newbcast:6 row_mask:0xf bank_mask:0xf
v_and_b32_dpp v34, v44, v6 row_newbcast:6 row_mask:0xf bank_mask:0xf
v_and_b32_dpp v35, v44, v7 row_newbcast:6 row_mask:0xf bank_mask:0xf
v_mfma_f32_16x16x32_bf16 v[36:39], v[28:31], a[216:219], v[36:39]
s_nop 0
v_mfma_f32_16x16x32_bf16 v[36:39], v[32:35], a[220:223], v[36:39]
s_waitcnt vmcnt(16)
v_mfma_f32_16x16x32_bf16 v[28:31], v[8:11], a[224:227], 0
v_mfma_f32_16x16x32_bf16 v[32:35], v[8:11], a[232:235], 0
v_mfma_f32_16x16x32_bf16 v[28:31], v[12:15], a[228:231], v[28:31]
v_mfma_f32_16x16x32_bf16 v[32:35], v[12:15], a[236:239], v[32:35]
s_nop 6
v_add_f32_dpp v44, v29, v28 quad_perm:[1,1,3,3] row_mask:0x3 bank_mask:0xf
s_nop 1
v_add_f32_dpp v44, v33, v32 quad_perm:[1,1,3,3] row_mask:0xc bank_mask:0xf
v_fma_f32 v45, v44, s38, v2
v_cmp_lt_f32_e32 vcc, 0, v45
s_nop 0
s_and_b64 s[48:49], vcc, s[40:41]
s_cbranch_scc0 Lc44_%=
s_swappc_b64 s[42:43], s[44:45]
Lc44_%=:
v_exp_f32_e32 v45, v45
s_nop 0
v_cndmask_b32_e64 v45, 0, v45, s[40:41]
v_add_f32_e32 v46, v46, v45
v_cvt_pk_bf16_f32 v44, v45, v45
s_nop 1
v_mov_b32_dpp v44, v44 row_ror:8 row_mask:0xa bank_mask:0xf
s_waitcnt vmcnt(12)
s_nop 0
v_and_b32_dpp v28, v44, v4 row_newbcast:0 row_mask:0xf bank_mask:0xf
v_and_b32_dpp v29, v44, v5 row_newbcast:0 row_mask:0xf bank_mask:0xf
v_and_b32_dpp v30, v44, v6 row_newbcast:0 row_mask:0xf bank_mask:0xf
v_and_b32_dpp v31, v44, v7 row_newbcast:0 row_mask:0xf bank_mask:0xf
v_and_b32_dpp v32, v44, v4 row_newbcast:2 row_mask:0xf bank_mask:0xf
v_and_b32_dpp v33, v44, v5 row_newbcast:2 row_mask:0xf bank_mask:0xf
v_and_b32_dpp v34, v44, v6 row_newbcast:2 row_mask:0xf bank_mask:0xf
v_and_b32_dpp v35, v44, v7 row_newbcast:2 row_mask:0xf bank_mask:0xf
v_mfma_f32_16x16x32_bf16 v[36:39], v[28:31], a[240:243], v[36:39]
v_and_b32_dpp v28, v44, v4 row_newbcast:4 row_mask:0xf bank_mask:0xf
v_and_b32_dpp v29, v44, v5 row_newbcast:4 row_mask:0xf bank_mask:0xf
v_and_b32_dpp v30, v44, v6 row_newbcast:4 row_mask:0xf bank_mask:0xf
v_and_b32_dpp v31, v44, v7 row_newbcast:4 row_mask:0xf bank_mask:0xf
v_mfma_f32_16x16x32_bf16 v[36:39], v[32:35], a[244:247], v[36:39]
v_and_b32_dpp v32, v44, v4 row_newbcast:6 row_mask:0xf bank_mask:0xf
v_and_b32_dpp v33, v44, v5 row_newbcast:6 row_mask:0xf bank_mask:0xf
v_and_b32_dpp v34, v44, v6 row_newbcast:6 row_mask:0xf bank_mask:0xf
v_and_b32_dpp v35, v44, v7 row_newbcast:6 row_mask:0xf bank_mask:0xf
v_mfma_f32_16x16x32_bf16 v[36:39], v[28:31], a[248:251], v[36:39]
s_nop 0
v_mfma_f32_16x16x32_bf16 v[36:39], v[32:35], a[252:255], v[36:39]
s_waitcnt vmcnt(8)
v_mfma_f32_16x16x32_bf16 v[28:31], v[8:11], v[48:51], 0
v_mfma_f32_16x16x32_bf16 v[32:35], v[8:11], v[56:59], 0
v_mfma_f32_16x16x32_bf16 v[28:31], v[12:15], v[52:55], v[28:31]
v_mfma_f32_16x16x32_bf16 v[32:35], v[12:15], v[60:63], v[32:35]
v_mbcnt_lo_u32_b32 v45, -1, 0
v_mbcnt_hi_u32_b32 v45, -1, v45
v_lshlrev_b32_e32 v45, 4, v45
v_add_u32_e32 v45, s57, v45
ds_write_b128 v45, v[48:51]
ds_write_b128 v45, v[52:55] offset:1024
ds_write_b128 v45, v[56:59] offset:2048
ds_write_b128 v45, v[60:63] offset:3072
s_waitcnt lgkmcnt(0)
s_add_u32 s58, s22, 1605632
s_addc_u32 s59, s23, 0
global_load_dwordx4 v[48:51], v1, s[58:59] offset:0 nt
global_load_dwordx4 v[52:55], v1, s[58:59] offset:256 nt
global_load_dwordx4 v[56:59], v1, s[58:59] offset:512 nt
global_load_dwordx4 v[60:63], v1, s[58:59] offset:768 nt
v_add_f32_dpp v44, v29, v28 quad_perm:[1,1,3,3] row_mask:0x3 bank_mask:0xf
s_nop 1
v_add_f32_dpp v44, v33, v32 quad_perm:[1,1,3,3] row_mask:0xc bank_mask:0xf
v_fma_f32 v45, v44, s38, v2
v_cmp_lt_f32_e32 vcc, 0, v45
s_nop 0
s_and_b64 s[48:49], vcc, s[40:41]
s_cbranch_scc0 Lc45_%=
s_swappc_b64 s[42:43], s[44:45]
Lc45_%=:
v_exp_f32_e32 v45, v45
s_nop 0
v_cndmask_b32_e64 v45, 0, v45, s[40:41]
v_add_f32_e32 v46, v46, v45
v_cvt_pk_bf16_f32 v44, v45, v45
s_nop 1
v_mov_b32_dpp v44, v44 row_ror:8 row_mask:0xa bank_mask:0xf
s_waitcnt vmcnt(8)
s_nop 0
v_and_b32_dpp v28, v44, v4 row_newbcast:0 row_mask:0xf bank_mask:0xf
v_and_b32_dpp v29, v44, v5 row_newbcast:0 row_mask:0xf bank_mask:0xf
v_and_b32_dpp v30, v44, v6 row_newbcast:0 row_mask:0xf bank_mask:0xf
v_and_b32_dpp v31, v44, v7 row_newbcast:0 row_mask:0xf bank_mask:0xf
v_and_b32_dpp v32, v44, v4 row_newbcast:2 row_mask:0xf bank_mask:0xf
v_and_b32_dpp v33, v44, v5 row_newbcast:2 row_mask:0xf bank_mask:0xf
v_and_b32_dpp v34, v44, v6 row_newbcast:2 row_mask:0xf bank_mask:0xf
v_and_b32_dpp v35, v44, v7 row_newbcast:2 row_mask:0xf bank_mask:0xf
v_mfma_f32_16x16x32_bf16 v[36:39], v[28:31], v[64:67], v[36:39]
v_and_b32_dpp v28, v44, v4 row_newbcast:4 row_mask:0xf bank_mask:0xf
v_and_b32_dpp v29, v44, v5 row_newbcast:4 row_mask:0xf bank_mask:0xf
v_and_b32_dpp v30, v44, v6 row_newbcast:4 row_mask:0xf bank_mask:0xf
v_and_b32_dpp v31, v44, v7 row_newbcast:4 row_mask:0xf bank_mask:0xf
v_mfma_f32_16x16x32_bf16 v[36:39], v[32:35], v[68:71], v[36:39]
v_and_b32_dpp v32, v44, v4 row_newbcast:6 row_mask:0xf bank_mask:0xf
v_and_b32_dpp v33, v44, v5 row_newbcast:6 row_mask:0xf bank_mask:0xf
v_and_b32_dpp v34, v44, v6 row_newbcast:6 row_mask:0xf bank_mask:0xf
v_and_b32_dpp v35, v44, v7 row_newbcast:6 row_mask:0xf bank_mask:0xf
v_mfma_f32_16x16x32_bf16 v[36:39], v[28:31], v[72:75], v[36:39]
s_nop 0
v_mfma_f32_16x16x32_bf16 v[36:39], v[32:35], v[76:79], v[36:39]
v_mbcnt_lo_u32_b32 v45, -1, 0
v_mbcnt_hi_u32_b32 v45, -1, v45
v_lshlrev_b32_e32 v45, 4, v45
v_add_u32_e32 v45, s57, v45
ds_write_b128 v45, v[64:67] offset:4096
ds_write_b128 v45, v[68:71] offset:5120
ds_write_b128 v45, v[72:75] offset:6144
ds_write_b128 v45, v[76:79] offset:7168
s_waitcnt lgkmcnt(0)
s_add_u32 s58, s20, 1720320
s_addc_u32 s59, s21, 0
Lp46_%=:
s_memrealtime s[70:71]
s_waitcnt lgkmcnt(0)
s_sub_u32 s72, s68, s70
s_cmp_gt_i32 s72, 0
s_cbranch_scc0 Lg47_%=
s_sleep 1
s_branch Lp46_%=
Lg47_%=:
s_add_u32 s68, s68, s69
global_load_dwordx4 v[64:67], v0, s[58:59] offset:0 nt
global_load_dwordx4 v[68:71], v0, s[58:59] offset:128 nt
global_load_dwordx4 v[72:75], v0, s[58:59] offset:2048 nt
global_load_dwordx4 v[76:79], v0, s[58:59] offset:2176 nt
v_mbcnt_lo_u32_b32 v44, -1, 0
v_mbcnt_hi_u32_b32 v44, -1, v44
v_lshrrev_b32_e32 v45, 1, v44
v_lshlrev_b32_e32 v45, 10, v45
v_and_b32_e32 v44, 1, v44
v_mul_u32_u24_e32 v44, 384, v44
v_add3_u32 v45, v45, v44, 64
v_add_u32_e32 v45, 64, v45
global_load_dwordx4 v[24:27], v45, s[78:79] offset:0 sc1
s_waitcnt vmcnt(9)
v_mfma_f32_16x16x32_bf16 v[28:31], v[8:11], v[80:83], 0
v_mfma_f32_16x16x32_bf16 v[32:35], v[8:11], v[88:91], 0
v_mfma_f32_16x16x32_bf16 v[28:31], v[12:15], v[84:87], v[28:31]
v_mfma_f32_16x16x32_bf16 v[32:35], v[12:15], v[92:95], v[32:35]
v_mbcnt_lo_u32_b32 v45, -1, 0
v_mbcnt_hi_u32_b32 v45, -1, v45
v_lshlrev_b32_e32 v45, 4, v45
v_add_u32_e32 v45, s57, v45
ds_write_b128 v45, v[80:83] offset:8192
ds_write_b128 v45, v[84:87] offset:9216
ds_write_b128 v45, v[88:91] offset:10240
ds_write_b128 v45, v[92:95] offset:11264
s_waitcnt lgkmcnt(0)
s_add_u32 s58, s22, 1720320
s_addc_u32 s59, s23, 0
global_load_dwordx4 v[80:83], v1, s[58:59] offset:0 nt
global_load_dwordx4 v[84:87], v1, s[58:59] offset:256 nt
global_load_dwordx4 v[88:91], v1, s[58:59] offset:512 nt
global_load_dwordx4 v[92:95], v1, s[58:59] offset:768 nt
v_add_f32_dpp v44, v29, v28 quad_perm:[1,1,3,3] row_mask:0x3 bank_mask:0xf
s_nop 1
v_add_f32_dpp v44, v33, v32 quad_perm:[1,1,3,3] row_mask:0xc bank_mask:0xf
v_fma_f32 v45, v44, s38, v2
v_cmp_lt_f32_e32 vcc, 0, v45
s_nop 0
s_and_b64 s[48:49], vcc, s[40:41]
s_cbranch_scc0 Lc48_%=
s_swappc_b64 s[42:43], s[44:45]
Lc48_%=:
v_exp_f32_e32 v45, v45
s_nop 0
v_cndmask_b32_e64 v45, 0, v45, s[40:41]
v_add_f32_e32 v46, v46, v45
v_cvt_pk_bf16_f32 v44, v45, v45
s_nop 1
v_mov_b32_dpp v44, v44 row_ror:8 row_mask:0xa bank_mask:0xf
s_waitcnt vmcnt(9)
s_nop 0
v_and_b32_dpp v28, v44, v4 row_newbcast:0 row_mask:0xf bank_mask:0xf
v_and_b32_dpp v29, v44, v5 row_newbcast:0 row_mask:0xf bank_mask:0xf
v_and_b32_dpp v30, v44, v6 row_newbcast:0 row_mask:0xf bank_mask:0xf
v_and_b32_dpp v31, v44, v7 row_newbcast:0 row_mask:0xf bank_mask:0xf
v_and_b32_dpp v32, v44, v4 row_newbcast:2 row_mask:0xf bank_mask:0xf
v_and_b32_dpp v33, v44, v5 row_newbcast:2 row_mask:0xf bank_mask:0xf
v_and_b32_dpp v34, v44, v6 row_newbcast:2 row_mask:0xf bank_mask:0xf
v_and_b32_dpp v35, v44, v7 row_newbcast:2 row_mask:0xf bank_mask:0xf
v_mfma_f32_16x16x32_bf16 v[36:39], v[28:31], v[48:51], v[36:39]
v_and_b32_dpp v28, v44, v4 row_newbcast:4 row_mask:0xf bank_mask:0xf
v_and_b32_dpp v29, v44, v5 row_newbcast:4 row_mask:0xf bank_mask:0xf
v_and_b32_dpp v30, v44, v6 row_newbcast:4 row_mask:0xf bank_mask:0xf
v_and_b32_dpp v31, v44, v7 row_newbcast:4 row_mask:0xf bank_mask:0xf
v_mfma_f32_16x16x32_bf16 v[36:39], v[32:35], v[52:55], v[36:39]
v_and_b32_dpp v32, v44, v4 row_newbcast:6 row_mask:0xf bank_mask:0xf
v_and_b32_dpp v33, v44, v5 row_newbcast:6 row_mask:0xf bank_mask:0xf
v_and_b32_dpp v34, v44, v6 row_newbcast:6 row_mask:0xf bank_mask:0xf
v_and_b32_dpp v35, v44, v7 row_newbcast:6 row_mask:0xf bank_mask:0xf
v_mfma_f32_16x16x32_bf16 v[36:39], v[28:31], v[56:59], v[36:39]
s_nop 0
v_mfma_f32_16x16x32_bf16 v[36:39], v[32:35], v[60:63], v[36:39]
v_mbcnt_lo_u32_b32 v45, -1, 0
v_mbcnt_hi_u32_b32 v45, -1, v45
v_lshlrev_b32_e32 v45, 4, v45
v_add_u32_e32 v45, s57, v45
ds_write_b128 v45, v[48:51] offset:12288
ds_write_b128 v45, v[52:55] offset:13312
ds_write_b128 v45, v[56:59] offset:14336
ds_write_b128 v45, v[60:63] offset:15360
s_waitcnt lgkmcnt(0)
s_add_u32 s58, s20, 1835008
s_addc_u32 s59, s21, 0
Lp49_%=:
s_memrealtime s[70:71]
s_waitcnt lgkmcnt(0)
s_sub_u32 s72, s68, s70
s_cmp_gt_i32 s72, 0
s_cbranch_scc0 Lg50_%=
s_sleep 1
s_branch Lp49_%=
Lg50_%=:
s_add_u32 s68, s68, s69
global_load_dwordx4 v[48:51], v0, s[58:59] offset:0 nt
global_load_dwordx4 v[52:55], v0, s[58:59] offset:128 nt
global_load_dwordx4 v[56:59], v0, s[58:59] offset:2048 nt
global_load_dwordx4 v[60:63], v0, s[58:59] offset:2176 nt
s_waitcnt vmcnt(9)
v_mfma_f32_16x16x32_bf16 v[28:31], v[8:11], v[64:67], 0
v_mfma_f32_16x16x32_bf16 v[32:35], v[8:11], v[72:75], 0
v_mfma_f32_16x16x32_bf16 v[28:31], v[12:15], v[68:71], v[28:31]
v_mfma_f32_16x16x32_bf16 v[32:35], v[12:15], v[76:79], v[32:35]
v_mbcnt_lo_u32_b32 v45, -1, 0
v_mbcnt_hi_u32_b32 v45, -1, v45
v_lshlrev_b32_e32 v45, 4, v45
v_add_u32_e32 v45, s57, v45
ds_write_b128 v45, v[64:67] offset:16384
ds_write_b128 v45, v[68:71] offset:17408
ds_write_b128 v45, v[72:75] offset:18432
ds_write_b128 v45, v[76:79] offset:19456
s_waitcnt lgkmcnt(0)
s_add_u32 s58, s22, 1835008
s_addc_u32 s59, s23, 0
global_load_dwordx4 v[64:67], v1, s[58:59] offset:0 nt
global_load_dwordx4 v[68:71], v1, s[58:59] offset:256 nt
global_load_dwordx4 v[72:75], v1, s[58:59] offset:512 nt
global_load_dwordx4 v[76:79], v1, s[58:59] offset:768 nt
v_add_f32_dpp v44, v29, v28 quad_perm:[1,1,3,3] row_mask:0x3 bank_mask:0xf
s_nop 1
v_add_f32_dpp v44, v33, v32 quad_perm:[1,1,3,3] row_mask:0xc bank_mask:0xf
v_fma_f32 v45, v44, s38, v2
v_cmp_lt_f32_e32 vcc, 0, v45
s_nop 0
s_and_b64 s[48:49], vcc, s[40:41]
s_cbranch_scc0 Lc51_%=
s_swappc_b64 s[42:43], s[44:45]
Lc51_%=:
v_exp_f32_e32 v45, v45
s_nop 0
v_cndmask_b32_e64 v45, 0, v45, s[40:41]
v_add_f32_e32 v46, v46, v45
v_cvt_pk_bf16_f32 v44, v45, v45
s_nop 1
v_mov_b32_dpp v44, v44 row_ror:8 row_mask:0xa bank_mask:0xf
s_waitcnt vmcnt(8)
s_nop 0
v_and_b32_dpp v28, v44, v4 row_newbcast:0 row_mask:0xf bank_mask:0xf
v_and_b32_dpp v29, v44, v5 row_newbcast:0 row_mask:0xf bank_mask:0xf
v_and_b32_dpp v30, v44, v6 row_newbcast:0 row_mask:0xf bank_mask:0xf
v_and_b32_dpp v31, v44, v7 row_newbcast:0 row_mask:0xf bank_mask:0xf
v_and_b32_dpp v32, v44, v4 row_newbcast:2 row_mask:0xf bank_mask:0xf
v_and_b32_dpp v33, v44, v5 row_newbcast:2 row_mask:0xf bank_mask:0xf
v_and_b32_dpp v34, v44, v6 row_newbcast:2 row_mask:0xf bank_mask:0xf
v_and_b32_dpp v35, v44, v7 row_newbcast:2 row_mask:0xf bank_mask:0xf
v_mfma_f32_16x16x32_bf16 v[36:39], v[28:31], v[80:83], v[36:39]
v_and_b32_dpp v28, v44, v4 row_newbcast:4 row_mask:0xf bank_mask:0xf
v_and_b32_dpp v29, v44, v5 row_newbcast:4 row_mask:0xf bank_mask:0xf
v_and_b32_dpp v30, v44, v6 row_newbcast:4 row_mask:0xf bank_mask:0xf
v_and_b32_dpp v31, v44, v7 row_newbcast:4 row_mask:0xf bank_mask:0xf
v_mfma_f32_16x16x32_bf16 v[36:39], v[32:35], v[84:87], v[36:39]
v_and_b32_dpp v32, v44, v4 row_newbcast:6 row_mask:0xf bank_mask:0xf
v_and_b32_dpp v33, v44, v5 row_newbcast:6 row_mask:0xf bank_mask:0xf
v_and_b32_dpp v34, v44, v6 row_newbcast:6 row_mask:0xf bank_mask:0xf
v_and_b32_dpp v35, v44, v7 row_newbcast:6 row_mask:0xf bank_mask:0xf
v_mfma_f32_16x16x32_bf16 v[36:39], v[28:31], v[88:91], v[36:39]
s_nop 0
v_mfma_f32_16x16x32_bf16 v[36:39], v[32:35], v[92:95], v[36:39]
v_mbcnt_lo_u32_b32 v45, -1, 0
v_mbcnt_hi_u32_b32 v45, -1, v45
v_lshlrev_b32_e32 v45, 4, v45
v_add_u32_e32 v45, s57, v45
ds_write_b128 v45, v[80:83] offset:20480
ds_write_b128 v45, v[84:87] offset:21504
ds_write_b128 v45, v[88:91] offset:22528
ds_write_b128 v45, v[92:95] offset:23552
s_waitcnt lgkmcnt(0)
s_add_u32 s58, s20, 1949696
s_addc_u32 s59, s21, 0
Lp52_%=:
s_memrealtime s[70:71]
s_waitcnt lgkmcnt(0)
s_sub_u32 s72, s68, s70
s_cmp_gt_i32 s72, 0
s_cbranch_scc0 Lg53_%=
s_sleep 1
s_branch Lp52_%=
Lg53_%=:
s_add_u32 s68, s68, s69
global_load_dwordx4 v[80:83], v0, s[58:59] offset:0 nt
global_load_dwordx4 v[84:87], v0, s[58:59] offset:128 nt
global_load_dwordx4 v[88:91], v0, s[58:59] offset:2048 nt
global_load_dwordx4 v[92:95], v0, s[58:59] offset:2176 nt
v_xor_b32_e32 v25, s37, v25
v_xor_b32_e32 v27, s37, v27
v_or_b32_e32 v25, v25, v27
v_cmp_eq_u32_e32 vcc, 0, v25
v_cvt_f32_u32_e32 v45, v24
s_nop 0
v_cndmask_b32_e32 v45, 0, v45, vcc
v_cndmask_b32_e64 v44, 0, 1.0, vcc
s_nop 0
v_add_f32_dpp v24, v45, v45 row_ror:8 row_mask:0xf bank_mask:0xf
s_nop 1
v_add_f32_dpp v24, v24, v24 row_ror:4 row_mask:0xf bank_mask:0xf
s_nop 1
v_add_f32_dpp v24, v24, v24 row_ror:2 row_mask:0xf bank_mask:0xf
s_nop 1
v_add_f32_dpp v24, v24, v24 row_ror:1 row_mask:0xf bank_mask:0xf
s_nop 0
v_readlane_b32 s48, v24, 0
v_readlane_b32 s49, v24, 16
v_readlane_b32 s50, v24, 32
v_readlane_b32 s51, v24, 48
v_mov_b32_e32 v24, s48
v_add_f32_e32 v24, s49, v24
v_add_f32_e32 v24, s50, v24
v_add_f32_e32 v24, s51, v24
v_add_f32_dpp v25, v44, v44 row_ror:8 row_mask:0xf bank_mask:0xf
s_nop 1
v_add_f32_dpp v25, v25, v25 row_ror:4 row_mask:0xf bank_mask:0xf
s_nop 1
v_add_f32_dpp v25, v25, v25 row_ror:2 row_mask:0xf bank_mask:0xf
s_nop 1
v_add_f32_dpp v25, v25, v25 row_ror:1 row_mask:0xf bank_mask:0xf
s_nop 0
v_readlane_b32 s48, v25, 0
v_readlane_b32 s49, v25, 16
v_readlane_b32 s50, v25, 32
v_readlane_b32 s51, v25, 48
v_mov_b32_e32 v25, s48
v_add_f32_e32 v25, s49, v25
v_add_f32_e32 v25, s50, v25
v_add_f32_e32 v25, s51, v25
v_max_f32_e32 v25, 1.0, v25
v_rcp_f32_e32 v25, v25
s_nop 0
v_mul_f32_e32 v45, v24, v25
v_mul_f32_e32 v45, 0x3cfd70a4, v45
v_cvt_u32_f32_e32 v45, v45
s_nop 0
v_readfirstlane_b32 s48, v45
s_nop 0
s_max_u32 s48, s48, 92
s_min_u32 s69, s48, 118
s_mul_i32 s48, s69, 16
s_add_u32 s48, s48, 130
s_add_u32 s68, s77, s48
s_mul_i32 s73, s69, 233
s_lshr_b32 s73, s73, 8
s_waitcnt vmcnt(8)
v_mfma_f32_16x16x32_bf16 v[28:31], v[8:11], v[48:51], 0
v_mfma_f32_16x16x32_bf16 v[32:35], v[8:11], v[56:59], 0
v_mfma_f32_16x16x32_bf16 v[28:31], v[12:15], v[52:55], v[28:31]
v_mfma_f32_16x16x32_bf16 v[32:35], v[12:15], v[60:63], v[32:35]
v_mbcnt_lo_u32_b32 v45, -1, 0
v_mbcnt_hi_u32_b32 v45, -1, v45
v_lshlrev_b32_e32 v45, 4, v45
v_add_u32_e32 v45, s57, v45
ds_write_b128 v45, v[48:51] offset:24576
ds_write_b128 v45, v[52:55] offset:25600
ds_write_b128 v45, v[56:59] offset:26624
ds_write_b128 v45, v[60:63] offset:27648
s_waitcnt lgkmcnt(0)
s_add_u32 s58, s22, 1949696
s_addc_u32 s59, s23, 0
global_load_dwordx4 v[48:51], v1, s[58:59] offset:0 nt
global_load_dwordx4 v[52:55], v1, s[58:59] offset:256 nt
global_load_dwordx4 v[56:59], v1, s[58:59] offset:512 nt
global_load_dwordx4 v[60:63], v1, s[58:59] offset:768 nt
v_add_f32_dpp v44, v29, v28 quad_perm:[1,1,3,3] row_mask:0x3 bank_mask:0xf
s_nop 1
v_add_f32_dpp v44, v33, v32 quad_perm:[1,1,3,3] row_mask:0xc bank_mask:0xf
v_fma_f32 v45, v44, s38, v2
v_cmp_lt_f32_e32 vcc, 0, v45
s_nop 0
s_and_b64 s[48:49], vcc, s[40:41]
s_cbranch_scc0 Lc54_%=
s_swappc_b64 s[42:43], s[44:45]
Lc54_%=:
v_exp_f32_e32 v45, v45
s_nop 0
v_cndmask_b32_e64 v45, 0, v45, s[40:41]
v_add_f32_e32 v46, v46, v45
v_cvt_pk_bf16_f32 v44, v45, v45
s_nop 1
v_mov_b32_dpp v44, v44 row_ror:8 row_mask:0xa bank_mask:0xf
s_waitcnt vmcnt(8)
s_nop 0
v_and_b32_dpp v28, v44, v4 row_newbcast:0 row_mask:0xf bank_mask:0xf
v_and_b32_dpp v29, v44, v5 row_newbcast:0 row_mask:0xf bank_mask:0xf
v_and_b32_dpp v30, v44, v6 row_newbcast:0 row_mask:0xf bank_mask:0xf
v_and_b32_dpp v31, v44, v7 row_newbcast:0 row_mask:0xf bank_mask:0xf
v_and_b32_dpp v32, v44, v4 row_newbcast:2 row_mask:0xf bank_mask:0xf
v_and_b32_dpp v33, v44, v5 row_newbcast:2 row_mask:0xf bank_mask:0xf
v_and_b32_dpp v34, v44, v6 row_newbcast:2 row_mask:0xf bank_mask:0xf
v_and_b32_dpp v35, v44, v7 row_newbcast:2 row_mask:0xf bank_mask:0xf
v_mfma_f32_16x16x32_bf16 v[36:39], v[28:31], v[64:67], v[36:39]
v_and_b32_dpp v28, v44, v4 row_newbcast:4 row_mask:0xf bank_mask:0xf
v_and_b32_dpp v29, v44, v5 row_newbcast:4 row_mask:0xf bank_mask:0xf
v_and_b32_dpp v30, v44, v6 row_newbcast:4 row_mask:0xf bank_mask:0xf
v_and_b32_dpp v31, v44, v7 row_newbcast:4 row_mask:0xf bank_mask:0xf
v_mfma_f32_16x16x32_bf16 v[36:39], v[32:35], v[68:71], v[36:39]
v_and_b32_dpp v32, v44, v4 row_newbcast:6 row_mask:0xf bank_mask:0xf
v_and_b32_dpp v33, v44, v5 row_newbcast:6 row_mask:0xf bank_mask:0xf
v_and_b32_dpp v34, v44, v6 row_newbcast:6 row_mask:0xf bank_mask:0xf
v_and_b32_dpp v35, v44, v7 row_newbcast:6 row_mask:0xf bank_mask:0xf
v_mfma_f32_16x16x32_bf16 v[36:39], v[28:31], v[72:75], v[36:39]
s_nop 0
v_mfma_f32_16x16x32_bf16 v[36:39], v[32:35], v[76:79], v[36:39]
v_mbcnt_lo_u32_b32 v45, -1, 0
v_mbcnt_hi_u32_b32 v45, -1, v45
v_lshlrev_b32_e32 v45, 4, v45
v_add_u32_e32 v45, s57, v45
ds_write_b128 v45, v[64:67] offset:28672
ds_write_b128 v45, v[68:71] offset:29696
ds_write_b128 v45, v[72:75] offset:30720
ds_write_b128 v45, v[76:79] offset:31744
s_waitcnt lgkmcnt(0)
s_add_u32 s58, s20, 2064384
s_addc_u32 s59, s21, 0
Lp55_%=:
s_memrealtime s[70:71]
s_waitcnt lgkmcnt(0)
s_sub_u32 s72, s68, s70
s_cmp_gt_i32 s72, 0
s_cbranch_scc0 Lg56_%=
s_sleep 1
s_branch Lp55_%=
Lg56_%=:
s_add_u32 s68, s68, s69
global_load_dwordx4 v[64:67], v0, s[58:59] offset:0
global_load_dwordx4 v[68:71], v0, s[58:59] offset:128
global_load_dwordx4 v[72:75], v0, s[58:59] offset:2048
global_load_dwordx4 v[76:79], v0, s[58:59] offset:2176
s_waitcnt vmcnt(8)
v_mfma_f32_16x16x32_bf16 v[28:31], v[8:11], v[80:83], 0
v_mfma_f32_16x16x32_bf16 v[32:35], v[8:11], v[88:91], 0
v_mfma_f32_16x16x32_bf16 v[28:31], v[12:15], v[84:87], v[28:31]
v_mfma_f32_16x16x32_bf16 v[32:35], v[12:15], v[92:95], v[32:35]
v_mbcnt_lo_u32_b32 v45, -1, 0
v_mbcnt_hi_u32_b32 v45, -1, v45
v_lshlrev_b32_e32 v45, 4, v45
v_add_u32_e32 v45, s57, v45
ds_write_b128 v45, v[80:83] offset:32768
ds_write_b128 v45, v[84:87] offset:33792
ds_write_b128 v45, v[88:91] offset:34816
ds_write_b128 v45, v[92:95] offset:35840
s_waitcnt lgkmcnt(0)
s_add_u32 s58, s22, 2064384
s_addc_u32 s59, s23, 0
global_load_dwordx4 v[80:83], v1, s[58:59] offset:0
global_load_dwordx4 v[84:87], v1, s[58:59] offset:256
global_load_dwordx4 v[88:91], v1, s[58:59] offset:512
global_load_dwordx4 v[92:95], v1, s[58:59] offset:768
v_add_f32_dpp v44, v29, v28 quad_perm:[1,1,3,3] row_mask:0x3 bank_mask:0xf
s_nop 1
v_add_f32_dpp v44, v33, v32 quad_perm:[1,1,3,3] row_mask:0xc bank_mask:0xf
v_fma_f32 v45, v44, s38, v2
v_cmp_lt_f32_e32 vcc, 0, v45
s_nop 0
s_and_b64 s[48:49], vcc, s[40:41]
s_cbranch_scc0 Lc57_%=
s_swappc_b64 s[42:43], s[44:45]
Lc57_%=:
v_exp_f32_e32 v45, v45
s_nop 0
v_cndmask_b32_e64 v45, 0, v45, s[40:41]
v_add_f32_e32 v46, v46, v45
v_cvt_pk_bf16_f32 v44, v45, v45
s_nop 1
v_mov_b32_dpp v44, v44 row_ror:8 row_mask:0xa bank_mask:0xf
s_waitcnt vmcnt(8)
s_nop 0
v_and_b32_dpp v28, v44, v4 row_newbcast:0 row_mask:0xf bank_mask:0xf
v_and_b32_dpp v29, v44, v5 row_newbcast:0 row_mask:0xf bank_mask:0xf
v_and_b32_dpp v30, v44, v6 row_newbcast:0 row_mask:0xf bank_mask:0xf
v_and_b32_dpp v31, v44, v7 row_newbcast:0 row_mask:0xf bank_mask:0xf
v_and_b32_dpp v32, v44, v4 row_newbcast:2 row_mask:0xf bank_mask:0xf
v_and_b32_dpp v33, v44, v5 row_newbcast:2 row_mask:0xf bank_mask:0xf
v_and_b32_dpp v34, v44, v6 row_newbcast:2 row_mask:0xf bank_mask:0xf
v_and_b32_dpp v35, v44, v7 row_newbcast:2 row_mask:0xf bank_mask:0xf
v_mfma_f32_16x16x32_bf16 v[36:39], v[28:31], v[48:51], v[36:39]
v_and_b32_dpp v28, v44, v4 row_newbcast:4 row_mask:0xf bank_mask:0xf
v_and_b32_dpp v29, v44, v5 row_newbcast:4 row_mask:0xf bank_mask:0xf
v_and_b32_dpp v30, v44, v6 row_newbcast:4 row_mask:0xf bank_mask:0xf
v_and_b32_dpp v31, v44, v7 row_newbcast:4 row_mask:0xf bank_mask:0xf
v_mfma_f32_16x16x32_bf16 v[36:39], v[32:35], v[52:55], v[36:39]
v_and_b32_dpp v32, v44, v4 row_newbcast:6 row_mask:0xf bank_mask:0xf
v_and_b32_dpp v33, v44, v5 row_newbcast:6 row_mask:0xf bank_mask:0xf
v_and_b32_dpp v34, v44, v6 row_newbcast:6 row_mask:0xf bank_mask:0xf
v_and_b32_dpp v35, v44, v7 row_newbcast:6 row_mask:0xf bank_mask:0xf
v_mfma_f32_16x16x32_bf16 v[36:39], v[28:31], v[56:59], v[36:39]
s_nop 0
v_mfma_f32_16x16x32_bf16 v[36:39], v[32:35], v[60:63], v[36:39]
v_mbcnt_lo_u32_b32 v45, -1, 0
v_mbcnt_hi_u32_b32 v45, -1, v45
v_lshlrev_b32_e32 v45, 4, v45
v_add_u32_e32 v45, s57, v45
ds_write_b128 v45, v[48:51] offset:36864
ds_write_b128 v45, v[52:55] offset:37888
ds_write_b128 v45, v[56:59] offset:38912
v_mov_b32_e32 v24, v60
v_mov_b32_e32 v25, v61
v_mov_b32_e32 v26, v62
v_mov_b32_e32 v27, v63
s_waitcnt lgkmcnt(0)
s_add_u32 s58, s20, 2179072
s_addc_u32 s59, s21, 0
Lp58_%=:
s_memrealtime s[70:71]
s_waitcnt lgkmcnt(0)
s_sub_u32 s72, s68, s70
s_cmp_gt_i32 s72, 0
s_cbranch_scc0 Lg59_%=
s_sleep 1
s_branch Lp58_%=
Lg59_%=:
s_add_u32 s68, s68, s69
global_load_dwordx4 v[48:51], v0, s[58:59] offset:0 nt
global_load_dwordx4 v[52:55], v0, s[58:59] offset:128 nt
global_load_dwordx4 v[56:59], v0, s[58:59] offset:2048 nt
global_load_dwordx4 v[60:63], v0, s[58:59] offset:2176 nt
s_add_u32 s30, s22, 2179072
s_addc_u32 s31, s23, 0
s_add_u32 s28, s20, 2293760
s_addc_u32 s29, s21, 0
s_waitcnt vmcnt(8)
v_mfma_f32_16x16x32_bf16 v[28:31], v[8:11], v[64:67], 0
v_mfma_f32_16x16x32_bf16 v[32:35], v[8:11], v[72:75], 0
v_mfma_f32_16x16x32_bf16 v[28:31], v[12:15], v[68:71], v[28:31]
v_mfma_f32_16x16x32_bf16 v[32:35], v[12:15], v[76:79], v[32:35]
global_load_dwordx4 v[64:67], v1, s[30:31] offset:0 nt
global_load_dwordx4 v[68:71], v1, s[30:31] offset:256 nt
global_load_dwordx4 v[72:75], v1, s[30:31] offset:512 nt
global_load_dwordx4 v[76:79], v1, s[30:31] offset:768 nt
s_add_u32 s30, s30, 114688
s_addc_u32 s31, s31, 0
s_nop 0
v_add_f32_dpp v44, v29, v28 quad_perm:[1,1,3,3] row_mask:0x3 bank_mask:0xf
s_nop 1
v_add_f32_dpp v44, v33, v32 quad_perm:[1,1,3,3] row_mask:0xc bank_mask:0xf
v_fma_f32 v45, v44, s38, v2
v_cmp_lt_f32_e32 vcc, 0, v45
s_nop 0
s_and_b64 s[48:49], vcc, s[40:41]
s_cbranch_scc0 Lc60_%=
s_swappc_b64 s[42:43], s[44:45]
Lc60_%=:
v_exp_f32_e32 v45, v45
s_nop 0
v_cndmask_b32_e64 v45, 0, v45, s[40:41]
v_add_f32_e32 v46, v46, v45
v_cvt_pk_bf16_f32 v44, v45, v45
s_nop 1
v_mov_b32_dpp v44, v44 row_ror:8 row_mask:0xa bank_mask:0xf
s_waitcnt vmcnt(8)
s_nop 0
v_and_b32_dpp v28, v44, v4 row_newbcast:0 row_mask:0xf bank_mask:0xf
v_and_b32_dpp v29, v44, v5 row_newbcast:0 row_mask:0xf bank_mask:0xf
v_and_b32_dpp v30, v44, v6 row_newbcast:0 row_mask:0xf bank_mask:0xf
v_and_b32_dpp v31, v44, v7 row_newbcast:0 row_mask:0xf bank_mask:0xf
v_and_b32_dpp v32, v44, v4 row_newbcast:2 row_mask:0xf bank_mask:0xf
v_and_b32_dpp v33, v44, v5 row_newbcast:2 row_mask:0xf bank_mask:0xf
v_and_b32_dpp v34, v44, v6 row_newbcast:2 row_mask:0xf bank_mask:0xf
v_and_b32_dpp v35, v44, v7 row_newbcast:2 row_mask:0xf bank_mask:0xf
v_mfma_f32_16x16x32_bf16 v[36:39], v[28:31], v[80:83], v[36:39]
v_and_b32_dpp v28, v44, v4 row_newbcast:4 row_mask:0xf bank_mask:0xf
v_and_b32_dpp v29, v44, v5 row_newbcast:4 row_mask:0xf bank_mask:0xf
v_and_b32_dpp v30, v44, v6 row_newbcast:4 row_mask:0xf bank_mask:0xf
v_and_b32_dpp v31, v44, v7 row_newbcast:4 row_mask:0xf bank_mask:0xf
v_mfma_f32_16x16x32_bf16 v[36:39], v[32:35], v[84:87], v[36:39]
v_and_b32_dpp v32, v44, v4 row_newbcast:6 row_mask:0xf bank_mask:0xf
v_and_b32_dpp v33, v44, v5 row_newbcast:6 row_mask:0xf bank_mask:0xf
v_and_b32_dpp v34, v44, v6 row_newbcast:6 row_mask:0xf bank_mask:0xf
v_and_b32_dpp v35, v44, v7 row_newbcast:6 row_mask:0xf bank_mask:0xf
v_mfma_f32_16x16x32_bf16 v[36:39], v[28:31], v[88:91], v[36:39]
s_nop 0
v_mfma_f32_16x16x32_bf16 v[36:39], v[32:35], v[92:95], v[36:39]
Lp61_%=:
s_memrealtime s[70:71]
s_waitcnt lgkmcnt(0)
s_sub_u32 s72, s68, s70
s_cmp_gt_i32 s72, 0
s_cbranch_scc0 Lg62_%=
s_sleep 1
s_branch Lp61_%=
Lg62_%=:
s_add_u32 s68, s68, s69
global_load_dwordx4 v[80:83], v0, s[28:29] offset:0 nt
global_load_dwordx4 v[84:87], v0, s[28:29] offset:128 nt
global_load_dwordx4 v[88:91], v0, s[28:29] offset:2048 nt
global_load_dwordx4 v[92:95], v0, s[28:29] offset:2176 nt
s_add_u32 s28, s28, 114688
s_addc_u32 s29, s29, 0
s_mov_b32 s39, 4
s_nop 3
Lloop63_%=:
s_waitcnt vmcnt(8)
v_mfma_f32_16x16x32_bf16 v[28:31], v[8:11], v[48:51], 0
v_mfma_f32_16x16x32_bf16 v[32:35], v[8:11], v[56:59], 0
v_mfma_f32_16x16x32_bf16 v[28:31], v[12:15], v[52:55], v[28:31]
v_mfma_f32_16x16x32_bf16 v[32:35], v[12:15], v[60:63], v[32:35]
global_load_dwordx4 v[48:51], v1, s[30:31] offset:0 nt
global_load_dwordx4 v[52:55], v1, s[30:31] offset:256 nt
global_load_dwordx4 v[56:59], v1, s[30:31] offset:512 nt
global_load_dwordx4 v[60:63], v1, s[30:31] offset:768 nt
s_add_u32 s30, s30, 114688
s_addc_u32 s31, s31, 0
s_nop 0
v_add_f32_dpp v44, v29, v28 quad_perm:[1,1,3,3] row_mask:0x3 bank_mask:0xf
s_nop 1
v_add_f32_dpp v44, v33, v32 quad_perm:[1,1,3,3] row_mask:0xc bank_mask:0xf
v_fma_f32 v45, v44, s38, v2
v_cmp_lt_f32_e32 vcc, 0, v45
s_nop 0
s_and_b64 s[48:49], vcc, s[40:41]
s_cbranch_scc0 Lc64_%=
s_swappc_b64 s[42:43], s[44:45]
Lc64_%=:
v_exp_f32_e32 v45, v45
s_nop 0
v_cndmask_b32_e64 v45, 0, v45, s[40:41]
v_add_f32_e32 v46, v46, v45
v_cvt_pk_bf16_f32 v44, v45, v45
s_nop 1
v_mov_b32_dpp v44, v44 row_ror:8 row_mask:0xa bank_mask:0xf
s_waitcnt vmcnt(8)
s_nop 0
v_and_b32_dpp v28, v44, v4 row_newbcast:0 row_mask:0xf bank_mask:0xf
v_and_b32_dpp v29, v44, v5 row_newbcast:0 row_mask:0xf bank_mask:0xf
v_and_b32_dpp v30, v44, v6 row_newbcast:0 row_mask:0xf bank_mask:0xf
v_and_b32_dpp v31, v44, v7 row_newbcast:0 row_mask:0xf bank_mask:0xf
v_and_b32_dpp v32, v44, v4 row_newbcast:2 row_mask:0xf bank_mask:0xf
v_and_b32_dpp v33, v44, v5 row_newbcast:2 row_mask:0xf bank_mask:0xf
v_and_b32_dpp v34, v44, v6 row_newbcast:2 row_mask:0xf bank_mask:0xf
v_and_b32_dpp v35, v44, v7 row_newbcast:2 row_mask:0xf bank_mask:0xf
v_mfma_f32_16x16x32_bf16 v[36:39], v[28:31], v[64:67], v[36:39]
v_and_b32_dpp v28, v44, v4 row_newbcast:4 row_mask:0xf bank_mask:0xf
v_and_b32_dpp v29, v44, v5 row_newbcast:4 row_mask:0xf bank_mask:0xf
v_and_b32_dpp v30, v44, v6 row_newbcast:4 row_mask:0xf bank_mask:0xf
v_and_b32_dpp v31, v44, v7 row_newbcast:4 row_mask:0xf bank_mask:0xf
v_mfma_f32_16x16x32_bf16 v[36:39], v[32:35], v[68:71], v[36:39]
v_and_b32_dpp v32, v44, v4 row_newbcast:6 row_mask:0xf bank_mask:0xf
v_and_b32_dpp v33, v44, v5 row_newbcast:6 row_mask:0xf bank_mask:0xf
v_and_b32_dpp v34, v44, v6 row_newbcast:6 row_mask:0xf bank_mask:0xf
v_and_b32_dpp v35, v44, v7 row_newbcast:6 row_mask:0xf bank_mask:0xf
v_mfma_f32_16x16x32_bf16 v[36:39], v[28:31], v[72:75], v[36:39]
s_nop 0
v_mfma_f32_16x16x32_bf16 v[36:39], v[32:35], v[76:79], v[36:39]
Lp65_%=:
s_memrealtime s[70:71]
s_waitcnt lgkmcnt(0)
s_sub_u32 s72, s68, s70
s_cmp_gt_i32 s72, 0
s_cbranch_scc0 Lg66_%=
s_sleep 1
s_branch Lp65_%=
Lg66_%=:
s_add_u32 s68, s68, s69
global_load_dwordx4 v[64:67], v0, s[28:29] offset:0
global_load_dwordx4 v[68:71], v0, s[28:29] offset:128
global_load_dwordx4 v[72:75], v0, s[28:29] offset:2048
global_load_dwordx4 v[76:79], v0, s[28:29] offset:2176
s_add_u32 s28, s28, 114688
s_addc_u32 s29, s29, 0
s_waitcnt vmcnt(8)
v_mfma_f32_16x16x32_bf16 v[28:31], v[8:11], v[80:83], 0
v_mfma_f32_16x16x32_bf16 v[32:35], v[8:11], v[88:91], 0
v_mfma_f32_16x16x32_bf16 v[28:31], v[12:15], v[84:87], v[28:31]
v_mfma_f32_16x16x32_bf16 v[32:35], v[12:15], v[92:95], v[32:35]
global_load_dwordx4 v[80:83], v1, s[30:31] offset:0
global_load_dwordx4 v[84:87], v1, s[30:31] offset:256
global_load_dwordx4 v[88:91], v1, s[30:31] offset:512
global_load_dwordx4 v[92:95], v1, s[30:31] offset:768
s_add_u32 s30, s30, 114688
s_addc_u32 s31, s31, 0
s_nop 0
v_add_f32_dpp v44, v29, v28 quad_perm:[1,1,3,3] row_mask:0x3 bank_mask:0xf
s_nop 1
v_add_f32_dpp v44, v33, v32 quad_perm:[1,1,3,3] row_mask:0xc bank_mask:0xf
v_fma_f32 v45, v44, s38, v2
v_cmp_lt_f32_e32 vcc, 0, v45
s_nop 0
s_and_b64 s[48:49], vcc, s[40:41]
s_cbranch_scc0 Lc67_%=
s_swappc_b64 s[42:43], s[44:45]
Lc67_%=:
v_exp_f32_e32 v45, v45
s_nop 0
v_cndmask_b32_e64 v45, 0, v45, s[40:41]
v_add_f32_e32 v46, v46, v45
v_cvt_pk_bf16_f32 v44, v45, v45
s_nop 1
v_mov_b32_dpp v44, v44 row_ror:8 row_mask:0xa bank_mask:0xf
s_waitcnt vmcnt(8)
s_nop 0
v_and_b32_dpp v28, v44, v4 row_newbcast:0 row_mask:0xf bank_mask:0xf
v_and_b32_dpp v29, v44, v5 row_newbcast:0 row_mask:0xf bank_mask:0xf
v_and_b32_dpp v30, v44, v6 row_newbcast:0 row_mask:0xf bank_mask:0xf
v_and_b32_dpp v31, v44, v7 row_newbcast:0 row_mask:0xf bank_mask:0xf
v_and_b32_dpp v32, v44, v4 row_newbcast:2 row_mask:0xf bank_mask:0xf
v_and_b32_dpp v33, v44, v5 row_newbcast:2 row_mask:0xf bank_mask:0xf
v_and_b32_dpp v34, v44, v6 row_newbcast:2 row_mask:0xf bank_mask:0xf
v_and_b32_dpp v35, v44, v7 row_newbcast:2 row_mask:0xf bank_mask:0xf
v_mfma_f32_16x16x32_bf16 v[36:39], v[28:31], v[48:51], v[36:39]
v_and_b32_dpp v28, v44, v4 row_newbcast:4 row_mask:0xf bank_mask:0xf
v_and_b32_dpp v29, v44, v5 row_newbcast:4 row_mask:0xf bank_mask:0xf
v_and_b32_dpp v30, v44, v6 row_newbcast:4 row_mask:0xf bank_mask:0xf
v_and_b32_dpp v31, v44, v7 row_newbcast:4 row_mask:0xf bank_mask:0xf
v_mfma_f32_16x16x32_bf16 v[36:39], v[32:35], v[52:55], v[36:39]
v_and_b32_dpp v32, v44, v4 row_newbcast:6 row_mask:0xf bank_mask:0xf
v_and_b32_dpp v33, v44, v5 row_newbcast:6 row_mask:0xf bank_mask:0xf
v_and_b32_dpp v34, v44, v6 row_newbcast:6 row_mask:0xf bank_mask:0xf
v_and_b32_dpp v35, v44, v7 row_newbcast:6 row_mask:0xf bank_mask:0xf
v_mfma_f32_16x16x32_bf16 v[36:39], v[28:31], v[56:59], v[36:39]
s_nop 0
v_mfma_f32_16x16x32_bf16 v[36:39], v[32:35], v[60:63], v[36:39]
Lp68_%=:
s_memrealtime s[70:71]
s_waitcnt lgkmcnt(0)
s_sub_u32 s72, s68, s70
s_cmp_gt_i32 s72, 0
s_cbranch_scc0 Lg69_%=
s_sleep 1
s_branch Lp68_%=
Lg69_%=:
s_add_u32 s68, s68, s69
global_load_dwordx4 v[48:51], v0, s[28:29] offset:0 nt
global_load_dwordx4 v[52:55], v0, s[28:29] offset:128 nt
global_load_dwordx4 v[56:59], v0, s[28:29] offset:2048 nt
global_load_dwordx4 v[60:63], v0, s[28:29] offset:2176 nt
s_add_u32 s28, s28, 114688
s_addc_u32 s29, s29, 0
s_waitcnt vmcnt(8)
v_mfma_f32_16x16x32_bf16 v[28:31], v[8:11], v[64:67], 0
v_mfma_f32_16x16x32_bf16 v[32:35], v[8:11], v[72:75], 0
v_mfma_f32_16x16x32_bf16 v[28:31], v[12:15], v[68:71], v[28:31]
v_mfma_f32_16x16x32_bf16 v[32:35], v[12:15], v[76:79], v[32:35]
global_load_dwordx4 v[64:67], v1, s[30:31] offset:0 nt
global_load_dwordx4 v[68:71], v1, s[30:31] offset:256 nt
global_load_dwordx4 v[72:75], v1, s[30:31] offset:512 nt
global_load_dwordx4 v[76:79], v1, s[30:31] offset:768 nt
s_add_u32 s30, s30, 114688
s_addc_u32 s31, s31, 0
s_nop 0
v_add_f32_dpp v44, v29, v28 quad_perm:[1,1,3,3] row_mask:0x3 bank_mask:0xf
s_nop 1
v_add_f32_dpp v44, v33, v32 quad_perm:[1,1,3,3] row_mask:0xc bank_mask:0xf
v_fma_f32 v45, v44, s38, v2
v_cmp_lt_f32_e32 vcc, 0, v45
s_nop 0
s_and_b64 s[48:49], vcc, s[40:41]
s_cbranch_scc0 Lc70_%=
s_swappc_b64 s[42:43], s[44:45]
Lc70_%=:
v_exp_f32_e32 v45, v45
s_nop 0
v_cndmask_b32_e64 v45, 0, v45, s[40:41]
v_add_f32_e32 v46, v46, v45
v_cvt_pk_bf16_f32 v44, v45, v45
s_nop 1
v_mov_b32_dpp v44, v44 row_ror:8 row_mask:0xa bank_mask:0xf
s_waitcnt vmcnt(8)
s_nop 0
v_and_b32_dpp v28, v44, v4 row_newbcast:0 row_mask:0xf bank_mask:0xf
v_and_b32_dpp v29, v44, v5 row_newbcast:0 row_mask:0xf bank_mask:0xf
v_and_b32_dpp v30, v44, v6 row_newbcast:0 row_mask:0xf bank_mask:0xf
v_and_b32_dpp v31, v44, v7 row_newbcast:0 row_mask:0xf bank_mask:0xf
v_and_b32_dpp v32, v44, v4 row_newbcast:2 row_mask:0xf bank_mask:0xf
v_and_b32_dpp v33, v44, v5 row_newbcast:2 row_mask:0xf bank_mask:0xf
v_and_b32_dpp v34, v44, v6 row_newbcast:2 row_mask:0xf bank_mask:0xf
v_and_b32_dpp v35, v44, v7 row_newbcast:2 row_mask:0xf bank_mask:0xf
v_mfma_f32_16x16x32_bf16 v[36:39], v[28:31], v[80:83], v[36:39]
v_and_b32_dpp v28, v44, v4 row_newbcast:4 row_mask:0xf bank_mask:0xf
v_and_b32_dpp v29, v44, v5 row_newbcast:4 row_mask:0xf bank_mask:0xf
v_and_b32_dpp v30, v44, v6 row_newbcast:4 row_mask:0xf bank_mask:0xf
v_and_b32_dpp v31, v44, v7 row_newbcast:4 row_mask:0xf bank_mask:0xf
v_mfma_f32_16x16x32_bf16 v[36:39], v[32:35], v[84:87], v[36:39]
v_and_b32_dpp v32, v44, v4 row_newbcast:6 row_mask:0xf bank_mask:0xf
v_and_b32_dpp v33, v44, v5 row_newbcast:6 row_mask:0xf bank_mask:0xf
v_and_b32_dpp v34, v44, v6 row_newbcast:6 row_mask:0xf bank_mask:0xf
v_and_b32_dpp v35, v44, v7 row_newbcast:6 row_mask:0xf bank_mask:0xf
v_mfma_f32_16x16x32_bf16 v[36:39], v[28:31], v[88:91], v[36:39]
s_nop 0
v_mfma_f32_16x16x32_bf16 v[36:39], v[32:35], v[92:95], v[36:39]
Lp71_%=:
s_memrealtime s[70:71]
s_waitcnt lgkmcnt(0)
s_sub_u32 s72, s68, s70
s_cmp_gt_i32 s72, 0
s_cbranch_scc0 Lg72_%=
s_sleep 1
s_branch Lp71_%=
Lg72_%=:
s_add_u32 s68, s68, s69
global_load_dwordx4 v[80:83], v0, s[28:29] offset:0 nt
global_load_dwordx4 v[84:87], v0, s[28:29] offset:128 nt
global_load_dwordx4 v[88:91], v0, s[28:29] offset:2048 nt
global_load_dwordx4 v[92:95], v0, s[28:29] offset:2176 nt
s_add_u32 s28, s28, 114688
s_addc_u32 s29, s29, 0
s_sub_u32 s39, s39, 1
s_cmp_lg_u32 s39, 0
s_nop 3
s_cbranch_scc1 Lloop63_%=
s_waitcnt vmcnt(8)
v_mfma_f32_16x16x32_bf16 v[28:31], v[8:11], v[48:51], 0
v_mfma_f32_16x16x32_bf16 v[32:35], v[8:11], v[56:59], 0
v_mfma_f32_16x16x32_bf16 v[28:31], v[12:15], v[52:55], v[28:31]
v_mfma_f32_16x16x32_bf16 v[32:35], v[12:15], v[60:63], v[32:35]
s_add_u32 s58, s22, 3670016
s_addc_u32 s59, s23, 0
global_load_dwordx4 v[48:51], v1, s[58:59] offset:0 nt
global_load_dwordx4 v[52:55], v1, s[58:59] offset:256 nt
global_load_dwordx4 v[56:59], v1, s[58:59] offset:512 nt
global_load_dwordx4 v[60:63], v1, s[58:59] offset:768 nt
s_nop 0
v_add_f32_dpp v44, v29, v28 quad_perm:[1,1,3,3] row_mask:0x3 bank_mask:0xf
s_nop 1
v_add_f32_dpp v44, v33, v32 quad_perm:[1,1,3,3] row_mask:0xc bank_mask:0xf
v_fma_f32 v45, v44, s38, v2
v_cmp_lt_f32_e32 vcc, 0, v45
s_nop 0
s_and_b64 s[48:49], vcc, s[40:41]
s_cbranch_scc0 Lc73_%=
s_swappc_b64 s[42:43], s[44:45]
Lc73_%=:
v_exp_f32_e32 v45, v45
s_nop 0
v_cndmask_b32_e64 v45, 0, v45, s[40:41]
v_add_f32_e32 v46, v46, v45
v_cvt_pk_bf16_f32 v44, v45, v45
s_nop 1
v_mov_b32_dpp v44, v44 row_ror:8 row_mask:0xa bank_mask:0xf
s_waitcnt vmcnt(8)
s_nop 0
v_and_b32_dpp v28, v44, v4 row_newbcast:0 row_mask:0xf bank_mask:0xf
v_and_b32_dpp v29, v44, v5 row_newbcast:0 row_mask:0xf bank_mask:0xf
v_and_b32_dpp v30, v44, v6 row_newbcast:0 row_mask:0xf bank_mask:0xf
v_and_b32_dpp v31, v44, v7 row_newbcast:0 row_mask:0xf bank_mask:0xf
v_and_b32_dpp v32, v44, v4 row_newbcast:2 row_mask:0xf bank_mask:0xf
v_and_b32_dpp v33, v44, v5 row_newbcast:2 row_mask:0xf bank_mask:0xf
v_and_b32_dpp v34, v44, v6 row_newbcast:2 row_mask:0xf bank_mask:0xf
v_and_b32_dpp v35, v44, v7 row_newbcast:2 row_mask:0xf bank_mask:0xf
v_mfma_f32_16x16x32_bf16 v[36:39], v[28:31], v[64:67], v[36:39]
v_and_b32_dpp v28, v44, v4 row_newbcast:4 row_mask:0xf bank_mask:0xf
v_and_b32_dpp v29, v44, v5 row_newbcast:4 row_mask:0xf bank_mask:0xf
v_and_b32_dpp v30, v44, v6 row_newbcast:4 row_mask:0xf bank_mask:0xf
v_and_b32_dpp v31, v44, v7 row_newbcast:4 row_mask:0xf bank_mask:0xf
v_mfma_f32_16x16x32_bf16 v[36:39], v[32:35], v[68:71], v[36:39]
v_and_b32_dpp v32, v44, v4 row_newbcast:6 row_mask:0xf bank_mask:0xf
v_and_b32_dpp v33, v44, v5 row_newbcast:6 row_mask:0xf bank_mask:0xf
v_and_b32_dpp v34, v44, v6 row_newbcast:6 row_mask:0xf bank_mask:0xf
v_and_b32_dpp v35, v44, v7 row_newbcast:6 row_mask:0xf bank_mask:0xf
v_mfma_f32_16x16x32_bf16 v[36:39], v[28:31], v[72:75], v[36:39]
s_nop 0
v_mfma_f32_16x16x32_bf16 v[36:39], v[32:35], v[76:79], v[36:39]
s_add_u32 s58, s20, 3784704
s_addc_u32 s59, s21, 0
Lp74_%=:
s_memrealtime s[70:71]
s_waitcnt lgkmcnt(0)
s_sub_u32 s72, s68, s70
s_cmp_gt_i32 s72, 0
s_cbranch_scc0 Lg75_%=
s_sleep 1
s_branch Lp74_%=
Lg75_%=:
s_add_u32 s68, s68, s69
global_load_dwordx4 v[64:67], v0, s[58:59] offset:0
global_load_dwordx4 v[68:71], v0, s[58:59] offset:128
global_load_dwordx4 v[72:75], v0, s[58:59] offset:2048
global_load_dwordx4 v[76:79], v0, s[58:59] offset:2176
s_waitcnt vmcnt(8)
v_mfma_f32_16x16x32_bf16 v[28:31], v[8:11], v[80:83], 0
v_mfma_f32_16x16x32_bf16 v[32:35], v[8:11], v[88:91], 0
v_mfma_f32_16x16x32_bf16 v[28:31], v[12:15], v[84:87], v[28:31]
v_mfma_f32_16x16x32_bf16 v[32:35], v[12:15], v[92:95], v[32:35]
s_add_u32 s58, s22, 3784704
s_addc_u32 s59, s23, 0
global_load_dwordx4 v[80:83], v1, s[58:59] offset:0
global_load_dwordx4 v[84:87], v1, s[58:59] offset:256
global_load_dwordx4 v[88:91], v1, s[58:59] offset:512
global_load_dwordx4 v[92:95], v1, s[58:59] offset:768
s_nop 0
v_add_f32_dpp v44, v29, v28 quad_perm:[1,1,3,3] row_mask:0x3 bank_mask:0xf
s_nop 1
v_add_f32_dpp v44, v33, v32 quad_perm:[1,1,3,3] row_mask:0xc bank_mask:0xf
v_fma_f32 v45, v44, s38, v2
v_cmp_lt_f32_e32 vcc, 0, v45
s_nop 0
s_and_b64 s[48:49], vcc, s[40:41]
s_cbranch_scc0 Lc76_%=
s_swappc_b64 s[42:43], s[44:45]
Lc76_%=:
v_exp_f32_e32 v45, v45
s_nop 0
v_cndmask_b32_e64 v45, 0, v45, s[40:41]
v_add_f32_e32 v46, v46, v45
v_cvt_pk_bf16_f32 v44, v45, v45
s_nop 1
v_mov_b32_dpp v44, v44 row_ror:8 row_mask:0xa bank_mask:0xf
s_waitcnt vmcnt(8)
s_nop 0
v_and_b32_dpp v28, v44, v4 row_newbcast:0 row_mask:0xf bank_mask:0xf
v_and_b32_dpp v29, v44, v5 row_newbcast:0 row_mask:0xf bank_mask:0xf
v_and_b32_dpp v30, v44, v6 row_newbcast:0 row_mask:0xf bank_mask:0xf
v_and_b32_dpp v31, v44, v7 row_newbcast:0 row_mask:0xf bank_mask:0xf
v_and_b32_dpp v32, v44, v4 row_newbcast:2 row_mask:0xf bank_mask:0xf
v_and_b32_dpp v33, v44, v5 row_newbcast:2 row_mask:0xf bank_mask:0xf
v_and_b32_dpp v34, v44, v6 row_newbcast:2 row_mask:0xf bank_mask:0xf
v_and_b32_dpp v35, v44, v7 row_newbcast:2 row_mask:0xf bank_mask:0xf
v_mfma_f32_16x16x32_bf16 v[36:39], v[28:31], v[48:51], v[36:39]
v_and_b32_dpp v28, v44, v4 row_newbcast:4 row_mask:0xf bank_mask:0xf
v_and_b32_dpp v29, v44, v5 row_newbcast:4 row_mask:0xf bank_mask:0xf
v_and_b32_dpp v30, v44, v6 row_newbcast:4 row_mask:0xf bank_mask:0xf
v_and_b32_dpp v31, v44, v7 row_newbcast:4 row_mask:0xf bank_mask:0xf
v_mfma_f32_16x16x32_bf16 v[36:39], v[32:35], v[52:55], v[36:39]
v_and_b32_dpp v32, v44, v4 row_newbcast:6 row_mask:0xf bank_mask:0xf
v_and_b32_dpp v33, v44, v5 row_newbcast:6 row_mask:0xf bank_mask:0xf
v_and_b32_dpp v34, v44, v6 row_newbcast:6 row_mask:0xf bank_mask:0xf
v_and_b32_dpp v35, v44, v7 row_newbcast:6 row_mask:0xf bank_mask:0xf
v_mfma_f32_16x16x32_bf16 v[36:39], v[28:31], v[56:59], v[36:39]
s_nop 0
v_mfma_f32_16x16x32_bf16 v[36:39], v[32:35], v[60:63], v[36:39]
s_add_u32 s58, s20, 3899392
s_addc_u32 s59, s21, 0
Lp77_%=:
s_memrealtime s[70:71]
s_waitcnt lgkmcnt(0)
s_sub_u32 s72, s68, s70
s_cmp_gt_i32 s72, 0
s_cbranch_scc0 Lg78_%=
s_sleep 1
s_branch Lp77_%=
Lg78_%=:
s_add_u32 s68, s68, s69
global_load_dwordx4 v[48:51], v0, s[58:59] offset:0 nt
global_load_dwordx4 v[52:55], v0, s[58:59] offset:128 nt
global_load_dwordx4 v[56:59], v0, s[58:59] offset:2048 nt
global_load_dwordx4 v[60:63], v0, s[58:59] offset:2176 nt
s_waitcnt vmcnt(8)
v_mfma_f32_16x16x32_bf16 v[28:31], v[8:11], v[64:67], 0
v_mfma_f32_16x16x32_bf16 v[32:35], v[8:11], v[72:75], 0
v_mfma_f32_16x16x32_bf16 v[28:31], v[12:15], v[68:71], v[28:31]
v_mfma_f32_16x16x32_bf16 v[32:35], v[12:15], v[76:79], v[32:35]
s_add_u32 s58, s22, 3899392
s_addc_u32 s59, s23, 0
global_load_dwordx4 v[64:67], v1, s[58:59] offset:0 nt
global_load_dwordx4 v[68:71], v1, s[58:59] offset:256 nt
global_load_dwordx4 v[72:75], v1, s[58:59] offset:512 nt
global_load_dwordx4 v[76:79], v1, s[58:59] offset:768 nt
s_nop 0
v_add_f32_dpp v44, v29, v28 quad_perm:[1,1,3,3] row_mask:0x3 bank_mask:0xf
s_nop 1
v_add_f32_dpp v44, v33, v32 quad_perm:[1,1,3,3] row_mask:0xc bank_mask:0xf
v_fma_f32 v45, v44, s38, v2
v_cmp_lt_f32_e32 vcc, 0, v45
s_nop 0
s_and_b64 s[48:49], vcc, s[40:41]
s_cbranch_scc0 Lc79_%=
s_swappc_b64 s[42:43], s[44:45]
Lc79_%=:
v_exp_f32_e32 v45, v45
s_nop 0
v_cndmask_b32_e64 v45, 0, v45, s[40:41]
v_add_f32_e32 v46, v46, v45
v_cvt_pk_bf16_f32 v44, v45, v45
s_nop 1
v_mov_b32_dpp v44, v44 row_ror:8 row_mask:0xa bank_mask:0xf
s_waitcnt vmcnt(8)
s_nop 0
v_and_b32_dpp v28, v44, v4 row_newbcast:0 row_mask:0xf bank_mask:0xf
v_and_b32_dpp v29, v44, v5 row_newbcast:0 row_mask:0xf bank_mask:0xf
v_and_b32_dpp v30, v44, v6 row_newbcast:0 row_mask:0xf bank_mask:0xf
v_and_b32_dpp v31, v44, v7 row_newbcast:0 row_mask:0xf bank_mask:0xf
v_and_b32_dpp v32, v44, v4 row_newbcast:2 row_mask:0xf bank_mask:0xf
v_and_b32_dpp v33, v44, v5 row_newbcast:2 row_mask:0xf bank_mask:0xf
v_and_b32_dpp v34, v44, v6 row_newbcast:2 row_mask:0xf bank_mask:0xf
v_and_b32_dpp v35, v44, v7 row_newbcast:2 row_mask:0xf bank_mask:0xf
v_mfma_f32_16x16x32_bf16 v[36:39], v[28:31], v[80:83], v[36:39]
v_and_b32_dpp v28, v44, v4 row_newbcast:4 row_mask:0xf bank_mask:0xf
v_and_b32_dpp v29, v44, v5 row_newbcast:4 row_mask:0xf bank_mask:0xf
v_and_b32_dpp v30, v44, v6 row_newbcast:4 row_mask:0xf bank_mask:0xf
v_and_b32_dpp v31, v44, v7 row_newbcast:4 row_mask:0xf bank_mask:0xf
v_mfma_f32_16x16x32_bf16 v[36:39], v[32:35], v[84:87], v[36:39]
v_and_b32_dpp v32, v44, v4 row_newbcast:6 row_mask:0xf bank_mask:0xf
v_and_b32_dpp v33, v44, v5 row_newbcast:6 row_mask:0xf bank_mask:0xf
v_and_b32_dpp v34, v44, v6 row_newbcast:6 row_mask:0xf bank_mask:0xf
v_and_b32_dpp v35, v44, v7 row_newbcast:6 row_mask:0xf bank_mask:0xf
v_mfma_f32_16x16x32_bf16 v[36:39], v[28:31], v[88:91], v[36:39]
s_nop 0
v_mfma_f32_16x16x32_bf16 v[36:39], v[32:35], v[92:95], v[36:39]
s_add_u32 s58, s20, 4014080
s_addc_u32 s59, s21, 0
Lp80_%=:
s_memrealtime s[70:71]
s_waitcnt lgkmcnt(0)
s_sub_u32 s72, s68, s70
s_cmp_gt_i32 s72, 0
s_cbranch_scc0 Lg81_%=
s_sleep 1
s_branch Lp80_%=
Lg81_%=:
s_add_u32 s68, s68, s69
global_load_dwordx4 v[80:83], v0, s[58:59] offset:0 nt
global_load_dwordx4 v[84:87], v0, s[58:59] offset:128 nt
global_load_dwordx4 v[88:91], v0, s[58:59] offset:2048 nt
global_load_dwordx4 v[92:95], v0, s[58:59] offset:2176 nt
s_waitcnt vmcnt(8)
v_mfma_f32_16x16x32_bf16 v[28:31], v[8:11], v[48:51], 0
v_mfma_f32_16x16x32_bf16 v[32:35], v[8:11], v[56:59], 0
v_mfma_f32_16x16x32_bf16 v[28:31], v[12:15], v[52:55], v[28:31]
v_mfma_f32_16x16x32_bf16 v[32:35], v[12:15], v[60:63], v[32:35]
s_add_u32 s58, s22, 4014080
s_addc_u32 s59, s23, 0
global_load_dwordx4 v[48:51], v1, s[58:59] offset:0 nt
global_load_dwordx4 v[52:55], v1, s[58:59] offset:256 nt
global_load_dwordx4 v[56:59], v1, s[58:59] offset:512 nt
global_load_dwordx4 v[60:63], v1, s[58:59] offset:768 nt
s_nop 0
v_add_f32_dpp v44, v29, v28 quad_perm:[1,1,3,3] row_mask:0x3 bank_mask:0xf
s_nop 1
v_add_f32_dpp v44, v33, v32 quad_perm:[1,1,3,3] row_mask:0xc bank_mask:0xf
v_fma_f32 v45, v44, s38, v2
v_cmp_lt_f32_e32 vcc, 0, v45
s_nop 0
s_and_b64 s[48:49], vcc, s[40:41]
s_cbranch_scc0 Lc82_%=
s_swappc_b64 s[42:43], s[44:45]
Lc82_%=:
v_exp_f32_e32 v45, v45
s_nop 0
v_cndmask_b32_e64 v45, 0, v45, s[40:41]
v_add_f32_e32 v46, v46, v45
v_cvt_pk_bf16_f32 v44, v45, v45
s_nop 1
v_mov_b32_dpp v44, v44 row_ror:8 row_mask:0xa bank_mask:0xf
s_waitcnt vmcnt(8)
s_nop 0
v_and_b32_dpp v28, v44, v4 row_newbcast:0 row_mask:0xf bank_mask:0xf
v_and_b32_dpp v29, v44, v5 row_newbcast:0 row_mask:0xf bank_mask:0xf
v_and_b32_dpp v30, v44, v6 row_newbcast:0 row_mask:0xf bank_mask:0xf
v_and_b32_dpp v31, v44, v7 row_newbcast:0 row_mask:0xf bank_mask:0xf
v_and_b32_dpp v32, v44, v4 row_newbcast:2 row_mask:0xf bank_mask:0xf
v_and_b32_dpp v33, v44, v5 row_newbcast:2 row_mask:0xf bank_mask:0xf
v_and_b32_dpp v34, v44, v6 row_newbcast:2 row_mask:0xf bank_mask:0xf
v_and_b32_dpp v35, v44, v7 row_newbcast:2 row_mask:0xf bank_mask:0xf
v_mfma_f32_16x16x32_bf16 v[36:39], v[28:31], v[64:67], v[36:39]
v_and_b32_dpp v28, v44, v4 row_newbcast:4 row_mask:0xf bank_mask:0xf
v_and_b32_dpp v29, v44, v5 row_newbcast:4 row_mask:0xf bank_mask:0xf
v_and_b32_dpp v30, v44, v6 row_newbcast:4 row_mask:0xf bank_mask:0xf
v_and_b32_dpp v31, v44, v7 row_newbcast:4 row_mask:0xf bank_mask:0xf
v_mfma_f32_16x16x32_bf16 v[36:39], v[32:35], v[68:71], v[36:39]
v_and_b32_dpp v32, v44, v4 row_newbcast:6 row_mask:0xf bank_mask:0xf
v_and_b32_dpp v33, v44, v5 row_newbcast:6 row_mask:0xf bank_mask:0xf
v_and_b32_dpp v34, v44, v6 row_newbcast:6 row_mask:0xf bank_mask:0xf
v_and_b32_dpp v35, v44, v7 row_newbcast:6 row_mask:0xf bank_mask:0xf
v_mfma_f32_16x16x32_bf16 v[36:39], v[28:31], v[72:75], v[36:39]
s_nop 0
v_mfma_f32_16x16x32_bf16 v[36:39], v[32:35], v[76:79], v[36:39]
s_add_u32 s58, s24, 2064384
s_addc_u32 s59, s25, 0
Lp83_%=:
s_memrealtime s[70:71]
s_waitcnt lgkmcnt(0)
s_sub_u32 s72, s68, s70
s_cmp_gt_i32 s72, 0
s_cbranch_scc0 Lg84_%=
s_sleep 1
s_branch Lp83_%=
Lg84_%=:
s_add_u32 s68, s68, s69
global_load_dwordx4 v[64:67], v0, s[58:59] offset:0
global_load_dwordx4 v[68:71], v0, s[58:59] offset:128
global_load_dwordx4 v[72:75], v0, s[58:59] offset:2048
global_load_dwordx4 v[76:79], v0, s[58:59] offset:2176
s_waitcnt vmcnt(8)
v_mfma_f32_16x16x32_bf16 v[28:31], v[8:11], v[80:83], 0
v_mfma_f32_16x16x32_bf16 v[32:35], v[8:11], v[88:91], 0
v_mfma_f32_16x16x32_bf16 v[28:31], v[12:15], v[84:87], v[28:31]
v_mfma_f32_16x16x32_bf16 v[32:35], v[12:15], v[92:95], v[32:35]
s_add_u32 s58, s26, 2064384
s_addc_u32 s59, s27, 0
global_load_dwordx4 v[80:83], v1, s[58:59] offset:0
global_load_dwordx4 v[84:87], v1, s[58:59] offset:256
global_load_dwordx4 v[88:91], v1, s[58:59] offset:512
global_load_dwordx4 v[92:95], v1, s[58:59] offset:768
s_nop 0
v_add_f32_dpp v44, v29, v28 quad_perm:[1,1,3,3] row_mask:0x3 bank_mask:0xf
s_nop 1
v_add_f32_dpp v44, v33, v32 quad_perm:[1,1,3,3] row_mask:0xc bank_mask:0xf
v_fma_f32 v45, v44, s38, v2
v_cmp_lt_f32_e32 vcc, 0, v45
s_nop 0
s_and_b64 s[48:49], vcc, s[40:41]
s_cbranch_scc0 Lc85_%=
s_swappc_b64 s[42:43], s[44:45]
Lc85_%=:
v_exp_f32_e32 v45, v45
s_nop 0
v_cndmask_b32_e64 v45, 0, v45, s[40:41]
v_add_f32_e32 v46, v46, v45
v_cvt_pk_bf16_f32 v44, v45, v45
s_nop 1
v_mov_b32_dpp v44, v44 row_ror:8 row_mask:0xa bank_mask:0xf
s_waitcnt vmcnt(8)
s_nop 0
v_and_b32_dpp v28, v44, v4 row_newbcast:0 row_mask:0xf bank_mask:0xf
v_and_b32_dpp v29, v44, v5 row_newbcast:0 row_mask:0xf bank_mask:0xf
v_and_b32_dpp v30, v44, v6 row_newbcast:0 row_mask:0xf bank_mask:0xf
v_and_b32_dpp v31, v44, v7 row_newbcast:0 row_mask:0xf bank_mask:0xf
v_and_b32_dpp v32, v44, v4 row_newbcast:2 row_mask:0xf bank_mask:0xf
v_and_b32_dpp v33, v44, v5 row_newbcast:2 row_mask:0xf bank_mask:0xf
v_and_b32_dpp v34, v44, v6 row_newbcast:2 row_mask:0xf bank_mask:0xf
v_and_b32_dpp v35, v44, v7 row_newbcast:2 row_mask:0xf bank_mask:0xf
v_mfma_f32_16x16x32_bf16 v[36:39], v[28:31], v[48:51], v[36:39]
v_and_b32_dpp v28, v44, v4 row_newbcast:4 row_mask:0xf bank_mask:0xf
v_and_b32_dpp v29, v44, v5 row_newbcast:4 row_mask:0xf bank_mask:0xf
v_and_b32_dpp v30, v44, v6 row_newbcast:4 row_mask:0xf bank_mask:0xf
v_and_b32_dpp v31, v44, v7 row_newbcast:4 row_mask:0xf bank_mask:0xf
v_mfma_f32_16x16x32_bf16 v[36:39], v[32:35], v[52:55], v[36:39]
v_and_b32_dpp v32, v44, v4 row_newbcast:6 row_mask:0xf bank_mask:0xf
v_and_b32_dpp v33, v44, v5 row_newbcast:6 row_mask:0xf bank_mask:0xf
v_and_b32_dpp v34, v44, v6 row_newbcast:6 row_mask:0xf bank_mask:0xf
v_and_b32_dpp v35, v44, v7 row_newbcast:6 row_mask:0xf bank_mask:0xf
v_mfma_f32_16x16x32_bf16 v[36:39], v[28:31], v[56:59], v[36:39]
s_nop 0
v_mfma_f32_16x16x32_bf16 v[36:39], v[32:35], v[60:63], v[36:39]
s_add_u32 s58, s24, 2179072
s_addc_u32 s59, s25, 0
Lp86_%=:
s_memrealtime s[70:71]
s_waitcnt lgkmcnt(0)
s_sub_u32 s72, s68, s70
s_cmp_gt_i32 s72, 0
s_cbranch_scc0 Lg87_%=
s_sleep 1
s_branch Lp86_%=
Lg87_%=:
s_add_u32 s68, s68, s69
global_load_dwordx4 v[48:51], v0, s[58:59] offset:0 nt
global_load_dwordx4 v[52:55], v0, s[58:59] offset:128 nt
global_load_dwordx4 v[56:59], v0, s[58:59] offset:2048 nt
global_load_dwordx4 v[60:63], v0, s[58:59] offset:2176 nt
v_add_f32_dpp v29, v46, v46 row_ror:8 row_mask:0xf bank_mask:0xf
s_nop 1
v_add_f32_dpp v29, v29, v29 row_ror:4 row_mask:0xf bank_mask:0xf
s_nop 1
v_add_f32_dpp v29, v29, v29 row_ror:2 row_mask:0xf bank_mask:0xf
s_nop 1
v_add_f32_dpp v29, v29, v29 row_ror:1 row_mask:0xf bank_mask:0xf
s_nop 0
v_readlane_b32 s48, v29, 0
v_readlane_b32 s49, v29, 16
v_readlane_b32 s50, v29, 32
v_readlane_b32 s51, v29, 48
v_mov_b32_e32 v29, s48
v_add_f32_e32 v29, s49, v29
v_add_f32_e32 v29, s50, v29
v_add_f32_e32 v29, s51, v29
v_mbcnt_lo_u32_b32 v45, -1, 0
v_mbcnt_hi_u32_b32 v45, -1, v45
v_lshlrev_b32_e32 v45, 4, v45
v_add_u32_e32 v45, s75, v45
v_sub_f32_e32 v28, 0, v2
s_sub_u32 s49, s75, s74
s_lshr_b32 s49, s49, 6
s_add_u32 s49, s49, s74
v_mov_b32_e32 v44, s49
s_mov_b32 exec_hi, 0
ds_write_b128 v45, v[36:39]
s_mov_b32 exec_lo, 1
ds_write_b64 v44, v[28:29] offset:2048
s_waitcnt lgkmcnt(0)
v_mov_b32_e32 v45, s74
v_mov_b32_e32 v44, 1
ds_add_rtn_u32 v44, v45, v44 offset:2112
s_waitcnt lgkmcnt(0)
v_readfirstlane_b32 s48, v44
s_mov_b64 exec, -1
s_and_b32 s48, s48, 3
s_cmp_eq_u32 s48, 3
s_nop 1
s_cbranch_scc0 Lnp88_%=
v_mbcnt_lo_u32_b32 v45, -1, 0
v_mbcnt_hi_u32_b32 v45, -1, v45
v_lshlrev_b32_e32 v45, 4, v45
v_add_u32_e32 v45, s74, v45
ds_read_b128 v[28:31], v45
ds_read_b128 v[8:11], v45 offset:512
ds_read_b128 v[12:15], v45 offset:1024
v_mbcnt_lo_u32_b32 v44, -1, 0
v_mbcnt_hi_u32_b32 v44, -1, v44
v_and_b32_e32 v44, 3, v44
v_lshlrev_b32_e32 v44, 3, v44
v_add_u32_e32 v44, s74, v44
ds_read_b64 v[32:33], v44 offset:2048
s_waitcnt lgkmcnt(0)
v_max_f32_dpp v34, v32, v32 quad_perm:[1,0,3,2] row_mask:0xf bank_mask:0xf
s_nop 1
v_max_f32_dpp v34, v34, v34 quad_perm:[2,3,0,1] row_mask:0xf bank_mask:0xf
v_sub_f32_e32 v44, v32, v34
v_exp_f32_e32 v44, v44
s_nop 0
v_mul_f32_e32 v35, v44, v33
s_nop 1
v_add_f32_dpp v35, v35, v35 quad_perm:[1,0,3,2] row_mask:0xf bank_mask:0xf
s_nop 1
v_add_f32_dpp v35, v35, v35 quad_perm:[2,3,0,1] row_mask:0xf bank_mask:0xf
v_mul_f32_dpp v36, v44, v28 quad_perm:[0,0,0,0] row_mask:0xf bank_mask:0xf
v_mul_f32_dpp v37, v44, v29 quad_perm:[0,0,0,0] row_mask:0xf bank_mask:0xf
v_mul_f32_dpp v38, v44, v30 quad_perm:[0,0,0,0] row_mask:0xf bank_mask:0xf
v_mul_f32_dpp v39, v44, v31 quad_perm:[0,0,0,0] row_mask:0xf bank_mask:0xf
ds_read_b128 v[28:31], v45 offset:1536
v_fmac_f32_dpp v36, v44, v8 quad_perm:[1,1,1,1] row_mask:0xf bank_mask:0xf
v_fmac_f32_dpp v37, v44, v9 quad_perm:[1,1,1,1] row_mask:0xf bank_mask:0xf
v_fmac_f32_dpp v38, v44, v10 quad_perm:[1,1,1,1] row_mask:0xf bank_mask:0xf
v_fmac_f32_dpp v39, v44, v11 quad_perm:[1,1,1,1] row_mask:0xf bank_mask:0xf
v_fmac_f32_dpp v36, v44, v12 quad_perm:[2,2,2,2] row_mask:0xf bank_mask:0xf
v_fmac_f32_dpp v37, v44, v13 quad_perm:[2,2,2,2] row_mask:0xf bank_mask:0xf
v_fmac_f32_dpp v38, v44, v14 quad_perm:[2,2,2,2] row_mask:0xf bank_mask:0xf
v_fmac_f32_dpp v39, v44, v15 quad_perm:[2,2,2,2] row_mask:0xf bank_mask:0xf
s_waitcnt lgkmcnt(0)
v_fmac_f32_dpp v36, v44, v28 quad_perm:[3,3,3,3] row_mask:0xf bank_mask:0xf
v_fmac_f32_dpp v37, v44, v29 quad_perm:[3,3,3,3] row_mask:0xf bank_mask:0xf
v_fmac_f32_dpp v38, v44, v30 quad_perm:[3,3,3,3] row_mask:0xf bank_mask:0xf
v_fmac_f32_dpp v39, v44, v31 quad_perm:[3,3,3,3] row_mask:0xf bank_mask:0xf
v_mov_b32_e32 v45, s74
v_mov_b32_e32 v44, 1
ds_write_b32 v45, v44 offset:2116
s_add_u32 s58, s66, 0
s_addc_u32 s59, s67, 0
v_permlane32_swap_b32 v36, v38
v_permlane32_swap_b32 v37, v39
v_mov_b32_e32 v28, v36
v_mov_b32_e32 v29, s37
v_mov_b32_e32 v30, v37
v_mov_b32_e32 v31, s37
v_mov_b32_e32 v8, v34
v_mov_b32_e32 v9, s37
v_mov_b32_e32 v10, v35
v_mov_b32_e32 v11, s37
s_waitcnt lgkmcnt(0)
v_mbcnt_lo_u32_b32 v45, -1, 0
v_mbcnt_hi_u32_b32 v45, -1, v45
v_lshlrev_b32_e32 v45, 4, v45
global_store_dwordx4 v45, v[28:31], s[58:59] offset:0 sc1
s_mov_b64 exec, 0xff
global_store_dwordx4 v45, v[8:11], s[58:59] offset:1024 sc1
s_mov_b64 exec, -1
s_nop 3
Lnp88_%=:
s_nop 1
v_mov_b32_e32 v36, 0
v_mov_b32_e32 v37, 0
v_mov_b32_e32 v38, 0
v_mov_b32_e32 v39, 0
v_mov_b32_e32 v46, 0
v_mov_b32_e32 v2, 0x7f800000
s_add_u32 s30, s26, 2179072
s_addc_u32 s31, s27, 0
s_add_u32 s28, s24, 2293760
s_addc_u32 s29, s25, 0
s_waitcnt vmcnt(8)
v_mfma_f32_16x16x32_bf16 v[28:31], v[16:19], v[64:67], 0
v_mfma_f32_16x16x32_bf16 v[32:35], v[16:19], v[72:75], 0
v_mfma_f32_16x16x32_bf16 v[28:31], v[20:23], v[68:71], v[28:31]
v_mfma_f32_16x16x32_bf16 v[32:35], v[20:23], v[76:79], v[32:35]
global_load_dwordx4 v[64:67], v1, s[30:31] offset:0 nt
global_load_dwordx4 v[68:71], v1, s[30:31] offset:256 nt
global_load_dwordx4 v[72:75], v1, s[30:31] offset:512 nt
global_load_dwordx4 v[76:79], v1, s[30:31] offset:768 nt
s_add_u32 s30, s30, 114688
s_addc_u32 s31, s31, 0
s_nop 0
v_add_f32_dpp v44, v29, v28 quad_perm:[1,1,3,3] row_mask:0x3 bank_mask:0xf
s_nop 1
v_add_f32_dpp v44, v33, v32 quad_perm:[1,1,3,3] row_mask:0xc bank_mask:0xf
v_fma_f32 v45, v44, s38, v3
v_cmp_lt_f32_e32 vcc, 0, v45
s_nop 0
s_and_b64 s[48:49], vcc, s[40:41]
s_cbranch_scc0 Lc89_%=
s_swappc_b64 s[42:43], s[46:47]
Lc89_%=:
v_exp_f32_e32 v45, v45
s_nop 0
v_cndmask_b32_e64 v45, 0, v45, s[40:41]
v_add_f32_e32 v47, v47, v45
v_cvt_pk_bf16_f32 v44, v45, v45
s_nop 1
v_mov_b32_dpp v44, v44 row_ror:8 row_mask:0xa bank_mask:0xf
s_waitcnt vmcnt(8)
s_nop 0
v_and_b32_dpp v28, v44, v4 row_newbcast:0 row_mask:0xf bank_mask:0xf
v_and_b32_dpp v29, v44, v5 row_newbcast:0 row_mask:0xf bank_mask:0xf
v_and_b32_dpp v30, v44, v6 row_newbcast:0 row_mask:0xf bank_mask:0xf
v_and_b32_dpp v31, v44, v7 row_newbcast:0 row_mask:0xf bank_mask:0xf
v_and_b32_dpp v32, v44, v4 row_newbcast:2 row_mask:0xf bank_mask:0xf
v_and_b32_dpp v33, v44, v5 row_newbcast:2 row_mask:0xf bank_mask:0xf
v_and_b32_dpp v34, v44, v6 row_newbcast:2 row_mask:0xf bank_mask:0xf
v_and_b32_dpp v35, v44, v7 row_newbcast:2 row_mask:0xf bank_mask:0xf
v_mfma_f32_16x16x32_bf16 v[40:43], v[28:31], v[80:83], v[40:43]
v_and_b32_dpp v28, v44, v4 row_newbcast:4 row_mask:0xf bank_mask:0xf
v_and_b32_dpp v29, v44, v5 row_newbcast:4 row_mask:0xf bank_mask:0xf
v_and_b32_dpp v30, v44, v6 row_newbcast:4 row_mask:0xf bank_mask:0xf
v_and_b32_dpp v31, v44, v7 row_newbcast:4 row_mask:0xf bank_mask:0xf
v_mfma_f32_16x16x32_bf16 v[40:43], v[32:35], v[84:87], v[40:43]
v_and_b32_dpp v32, v44, v4 row_newbcast:6 row_mask:0xf bank_mask:0xf
v_and_b32_dpp v33, v44, v5 row_newbcast:6 row_mask:0xf bank_mask:0xf
v_and_b32_dpp v34, v44, v6 row_newbcast:6 row_mask:0xf bank_mask:0xf
v_and_b32_dpp v35, v44, v7 row_newbcast:6 row_mask:0xf bank_mask:0xf
v_mfma_f32_16x16x32_bf16 v[40:43], v[28:31], v[88:91], v[40:43]
s_nop 0
v_mfma_f32_16x16x32_bf16 v[40:43], v[32:35], v[92:95], v[40:43]
Lp90_%=:
s_memrealtime s[70:71]
s_waitcnt lgkmcnt(0)
s_sub_u32 s72, s68, s70
s_cmp_gt_i32 s72, 0
s_cbranch_scc0 Lg91_%=
s_sleep 1
s_branch Lp90_%=
Lg91_%=:
s_add_u32 s68, s68, s69
global_load_dwordx4 v[80:83], v0, s[28:29] offset:0 nt
global_load_dwordx4 v[84:87], v0, s[28:29] offset:128 nt
global_load_dwordx4 v[88:91], v0, s[28:29] offset:2048 nt
global_load_dwordx4 v[92:95], v0, s[28:29] offset:2176 nt
s_add_u32 s28, s28, 114688
s_addc_u32 s29, s29, 0
s_mov_b32 s39, 4
s_nop 3
Lloop92_%=:
s_waitcnt vmcnt(8)
v_mfma_f32_16x16x32_bf16 v[28:31], v[16:19], v[48:51], 0
v_mfma_f32_16x16x32_bf16 v[32:35], v[16:19], v[56:59], 0
v_mfma_f32_16x16x32_bf16 v[28:31], v[20:23], v[52:55], v[28:31]
v_mfma_f32_16x16x32_bf16 v[32:35], v[20:23], v[60:63], v[32:35]
global_load_dwordx4 v[48:51], v1, s[30:31] offset:0 nt
global_load_dwordx4 v[52:55], v1, s[30:31] offset:256 nt
global_load_dwordx4 v[56:59], v1, s[30:31] offset:512 nt
global_load_dwordx4 v[60:63], v1, s[30:31] offset:768 nt
s_add_u32 s30, s30, 114688
s_addc_u32 s31, s31, 0
s_nop 0
v_add_f32_dpp v44, v29, v28 quad_perm:[1,1,3,3] row_mask:0x3 bank_mask:0xf
s_nop 1
v_add_f32_dpp v44, v33, v32 quad_perm:[1,1,3,3] row_mask:0xc bank_mask:0xf
v_fma_f32 v45, v44, s38, v3
v_cmp_lt_f32_e32 vcc, 0, v45
s_nop 0
s_and_b64 s[48:49], vcc, s[40:41]
s_cbranch_scc0 Lc93_%=
s_swappc_b64 s[42:43], s[46:47]
Lc93_%=:
v_exp_f32_e32 v45, v45
s_nop 0
v_cndmask_b32_e64 v45, 0, v45, s[40:41]
v_add_f32_e32 v47, v47, v45
v_cvt_pk_bf16_f32 v44, v45, v45
s_nop 1
v_mov_b32_dpp v44, v44 row_ror:8 row_mask:0xa bank_mask:0xf
s_waitcnt vmcnt(8)
s_nop 0
v_and_b32_dpp v28, v44, v4 row_newbcast:0 row_mask:0xf bank_mask:0xf
v_and_b32_dpp v29, v44, v5 row_newbcast:0 row_mask:0xf bank_mask:0xf
v_and_b32_dpp v30, v44, v6 row_newbcast:0 row_mask:0xf bank_mask:0xf
v_and_b32_dpp v31, v44, v7 row_newbcast:0 row_mask:0xf bank_mask:0xf
v_and_b32_dpp v32, v44, v4 row_newbcast:2 row_mask:0xf bank_mask:0xf
v_and_b32_dpp v33, v44, v5 row_newbcast:2 row_mask:0xf bank_mask:0xf
v_and_b32_dpp v34, v44, v6 row_newbcast:2 row_mask:0xf bank_mask:0xf
v_and_b32_dpp v35, v44, v7 row_newbcast:2 row_mask:0xf bank_mask:0xf
v_mfma_f32_16x16x32_bf16 v[40:43], v[28:31], v[64:67], v[40:43]
v_and_b32_dpp v28, v44, v4 row_newbcast:4 row_mask:0xf bank_mask:0xf
v_and_b32_dpp v29, v44, v5 row_newbcast:4 row_mask:0xf bank_mask:0xf
v_and_b32_dpp v30, v44, v6 row_newbcast:4 row_mask:0xf bank_mask:0xf
v_and_b32_dpp v31, v44, v7 row_newbcast:4 row_mask:0xf bank_mask:0xf
v_mfma_f32_16x16x32_bf16 v[40:43], v[32:35], v[68:71], v[40:43]
v_and_b32_dpp v32, v44, v4 row_newbcast:6 row_mask:0xf bank_mask:0xf
v_and_b32_dpp v33, v44, v5 row_newbcast:6 row_mask:0xf bank_mask:0xf
v_and_b32_dpp v34, v44, v6 row_newbcast:6 row_mask:0xf bank_mask:0xf
v_and_b32_dpp v35, v44, v7 row_newbcast:6 row_mask:0xf bank_mask:0xf
v_mfma_f32_16x16x32_bf16 v[40:43], v[28:31], v[72:75], v[40:43]
s_nop 0
v_mfma_f32_16x16x32_bf16 v[40:43], v[32:35], v[76:79], v[40:43]
Lp94_%=:
s_memrealtime s[70:71]
s_waitcnt lgkmcnt(0)
s_sub_u32 s72, s68, s70
s_cmp_gt_i32 s72, 0
s_cbranch_scc0 Lg95_%=
s_sleep 1
s_branch Lp94_%=
Lg95_%=:
s_add_u32 s68, s68, s69
global_load_dwordx4 v[64:67], v0, s[28:29] offset:0
global_load_dwordx4 v[68:71], v0, s[28:29] offset:128
global_load_dwordx4 v[72:75], v0, s[28:29] offset:2048
global_load_dwordx4 v[76:79], v0, s[28:29] offset:2176
s_add_u32 s28, s28, 114688
s_addc_u32 s29, s29, 0
s_waitcnt vmcnt(8)
v_mfma_f32_16x16x32_bf16 v[28:31], v[16:19], v[80:83], 0
v_mfma_f32_16x16x32_bf16 v[32:35], v[16:19], v[88:91], 0
v_mfma_f32_16x16x32_bf16 v[28:31], v[20:23], v[84:87], v[28:31]
v_mfma_f32_16x16x32_bf16 v[32:35], v[20:23], v[92:95], v[32:35]
global_load_dwordx4 v[80:83], v1, s[30:31] offset:0
global_load_dwordx4 v[84:87], v1, s[30:31] offset:256
global_load_dwordx4 v[88:91], v1, s[30:31] offset:512
global_load_dwordx4 v[92:95], v1, s[30:31] offset:768
s_add_u32 s30, s30, 114688
s_addc_u32 s31, s31, 0
s_nop 0
v_add_f32_dpp v44, v29, v28 quad_perm:[1,1,3,3] row_mask:0x3 bank_mask:0xf
s_nop 1
v_add_f32_dpp v44, v33, v32 quad_perm:[1,1,3,3] row_mask:0xc bank_mask:0xf
v_fma_f32 v45, v44, s38, v3
v_cmp_lt_f32_e32 vcc, 0, v45
s_nop 0
s_and_b64 s[48:49], vcc, s[40:41]
s_cbranch_scc0 Lc96_%=
s_swappc_b64 s[42:43], s[46:47]
Lc96_%=:
v_exp_f32_e32 v45, v45
s_nop 0
v_cndmask_b32_e64 v45, 0, v45, s[40:41]
v_add_f32_e32 v47, v47, v45
v_cvt_pk_bf16_f32 v44, v45, v45
s_nop 1
v_mov_b32_dpp v44, v44 row_ror:8 row_mask:0xa bank_mask:0xf
s_waitcnt vmcnt(8)
s_nop 0
v_and_b32_dpp v28, v44, v4 row_newbcast:0 row_mask:0xf bank_mask:0xf
v_and_b32_dpp v29, v44, v5 row_newbcast:0 row_mask:0xf bank_mask:0xf
v_and_b32_dpp v30, v44, v6 row_newbcast:0 row_mask:0xf bank_mask:0xf
v_and_b32_dpp v31, v44, v7 row_newbcast:0 row_mask:0xf bank_mask:0xf
v_and_b32_dpp v32, v44, v4 row_newbcast:2 row_mask:0xf bank_mask:0xf
v_and_b32_dpp v33, v44, v5 row_newbcast:2 row_mask:0xf bank_mask:0xf
v_and_b32_dpp v34, v44, v6 row_newbcast:2 row_mask:0xf bank_mask:0xf
v_and_b32_dpp v35, v44, v7 row_newbcast:2 row_mask:0xf bank_mask:0xf
v_mfma_f32_16x16x32_bf16 v[40:43], v[28:31], v[48:51], v[40:43]
v_and_b32_dpp v28, v44, v4 row_newbcast:4 row_mask:0xf bank_mask:0xf
v_and_b32_dpp v29, v44, v5 row_newbcast:4 row_mask:0xf bank_mask:0xf
v_and_b32_dpp v30, v44, v6 row_newbcast:4 row_mask:0xf bank_mask:0xf
v_and_b32_dpp v31, v44, v7 row_newbcast:4 row_mask:0xf bank_mask:0xf
v_mfma_f32_16x16x32_bf16 v[40:43], v[32:35], v[52:55], v[40:43]
v_and_b32_dpp v32, v44, v4 row_newbcast:6 row_mask:0xf bank_mask:0xf
v_and_b32_dpp v33, v44, v5 row_newbcast:6 row_mask:0xf bank_mask:0xf
v_and_b32_dpp v34, v44, v6 row_newbcast:6 row_mask:0xf bank_mask:0xf
v_and_b32_dpp v35, v44, v7 row_newbcast:6 row_mask:0xf bank_mask:0xf
v_mfma_f32_16x16x32_bf16 v[40:43], v[28:31], v[56:59], v[40:43]
s_nop 0
v_mfma_f32_16x16x32_bf16 v[40:43], v[32:35], v[60:63], v[40:43]
Lp97_%=:
s_memrealtime s[70:71]
s_waitcnt lgkmcnt(0)
s_sub_u32 s72, s68, s70
s_cmp_gt_i32 s72, 0
s_cbranch_scc0 Lg98_%=
s_sleep 1
s_branch Lp97_%=
Lg98_%=:
s_add_u32 s68, s68, s69
global_load_dwordx4 v[48:51], v0, s[28:29] offset:0 nt
global_load_dwordx4 v[52:55], v0, s[28:29] offset:128 nt
global_load_dwordx4 v[56:59], v0, s[28:29] offset:2048 nt
global_load_dwordx4 v[60:63], v0, s[28:29] offset:2176 nt
s_add_u32 s28, s28, 114688
s_addc_u32 s29, s29, 0
s_waitcnt vmcnt(8)
v_mfma_f32_16x16x32_bf16 v[28:31], v[16:19], v[64:67], 0
v_mfma_f32_16x16x32_bf16 v[32:35], v[16:19], v[72:75], 0
v_mfma_f32_16x16x32_bf16 v[28:31], v[20:23], v[68:71], v[28:31]
v_mfma_f32_16x16x32_bf16 v[32:35], v[20:23], v[76:79], v[32:35]
global_load_dwordx4 v[64:67], v1, s[30:31] offset:0 nt
global_load_dwordx4 v[68:71], v1, s[30:31] offset:256 nt
global_load_dwordx4 v[72:75], v1, s[30:31] offset:512 nt
global_load_dwordx4 v[76:79], v1, s[30:31] offset:768 nt
s_add_u32 s30, s30, 114688
s_addc_u32 s31, s31, 0
s_nop 0
v_add_f32_dpp v44, v29, v28 quad_perm:[1,1,3,3] row_mask:0x3 bank_mask:0xf
s_nop 1
v_add_f32_dpp v44, v33, v32 quad_perm:[1,1,3,3] row_mask:0xc bank_mask:0xf
v_fma_f32 v45, v44, s38, v3
v_cmp_lt_f32_e32 vcc, 0, v45
s_nop 0
s_and_b64 s[48:49], vcc, s[40:41]
s_cbranch_scc0 Lc99_%=
s_swappc_b64 s[42:43], s[46:47]
Lc99_%=:
v_exp_f32_e32 v45, v45
s_nop 0
v_cndmask_b32_e64 v45, 0, v45, s[40:41]
v_add_f32_e32 v47, v47, v45
v_cvt_pk_bf16_f32 v44, v45, v45
s_nop 1
v_mov_b32_dpp v44, v44 row_ror:8 row_mask:0xa bank_mask:0xf
s_waitcnt vmcnt(8)
s_nop 0
v_and_b32_dpp v28, v44, v4 row_newbcast:0 row_mask:0xf bank_mask:0xf
v_and_b32_dpp v29, v44, v5 row_newbcast:0 row_mask:0xf bank_mask:0xf
v_and_b32_dpp v30, v44, v6 row_newbcast:0 row_mask:0xf bank_mask:0xf
v_and_b32_dpp v31, v44, v7 row_newbcast:0 row_mask:0xf bank_mask:0xf
v_and_b32_dpp v32, v44, v4 row_newbcast:2 row_mask:0xf bank_mask:0xf
v_and_b32_dpp v33, v44, v5 row_newbcast:2 row_mask:0xf bank_mask:0xf
v_and_b32_dpp v34, v44, v6 row_newbcast:2 row_mask:0xf bank_mask:0xf
v_and_b32_dpp v35, v44, v7 row_newbcast:2 row_mask:0xf bank_mask:0xf
v_mfma_f32_16x16x32_bf16 v[40:43], v[28:31], v[80:83], v[40:43]
v_and_b32_dpp v28, v44, v4 row_newbcast:4 row_mask:0xf bank_mask:0xf
v_and_b32_dpp v29, v44, v5 row_newbcast:4 row_mask:0xf bank_mask:0xf
v_and_b32_dpp v30, v44, v6 row_newbcast:4 row_mask:0xf bank_mask:0xf
v_and_b32_dpp v31, v44, v7 row_newbcast:4 row_mask:0xf bank_mask:0xf
v_mfma_f32_16x16x32_bf16 v[40:43], v[32:35], v[84:87], v[40:43]
v_and_b32_dpp v32, v44, v4 row_newbcast:6 row_mask:0xf bank_mask:0xf
v_and_b32_dpp v33, v44, v5 row_newbcast:6 row_mask:0xf bank_mask:0xf
v_and_b32_dpp v34, v44, v6 row_newbcast:6 row_mask:0xf bank_mask:0xf
v_and_b32_dpp v35, v44, v7 row_newbcast:6 row_mask:0xf bank_mask:0xf
v_mfma_f32_16x16x32_bf16 v[40:43], v[28:31], v[88:91], v[40:43]
s_nop 0
v_mfma_f32_16x16x32_bf16 v[40:43], v[32:35], v[92:95], v[40:43]
Lp100_%=:
s_memrealtime s[70:71]
s_waitcnt lgkmcnt(0)
s_sub_u32 s72, s68, s70
s_cmp_gt_i32 s72, 0
s_cbranch_scc0 Lg101_%=
s_sleep 1
s_branch Lp100_%=
Lg101_%=:
s_add_u32 s68, s68, s69
global_load_dwordx4 v[80:83], v0, s[28:29] offset:0 nt
global_load_dwordx4 v[84:87], v0, s[28:29] offset:128 nt
global_load_dwordx4 v[88:91], v0, s[28:29] offset:2048 nt
global_load_dwordx4 v[92:95], v0, s[28:29] offset:2176 nt
s_add_u32 s28, s28, 114688
s_addc_u32 s29, s29, 0
s_sub_u32 s39, s39, 1
s_cmp_lg_u32 s39, 0
s_nop 3
s_cbranch_scc1 Lloop92_%=
s_waitcnt vmcnt(8)
v_mfma_f32_16x16x32_bf16 v[28:31], v[16:19], v[48:51], 0
v_mfma_f32_16x16x32_bf16 v[32:35], v[16:19], v[56:59], 0
v_mfma_f32_16x16x32_bf16 v[28:31], v[20:23], v[52:55], v[28:31]
v_mfma_f32_16x16x32_bf16 v[32:35], v[20:23], v[60:63], v[32:35]
s_add_u32 s58, s26, 3670016
s_addc_u32 s59, s27, 0
global_load_dwordx4 v[48:51], v1, s[58:59] offset:0 nt
global_load_dwordx4 v[52:55], v1, s[58:59] offset:256 nt
global_load_dwordx4 v[56:59], v1, s[58:59] offset:512 nt
global_load_dwordx4 v[60:63], v1, s[58:59] offset:768 nt
s_nop 0
v_add_f32_dpp v44, v29, v28 quad_perm:[1,1,3,3] row_mask:0x3 bank_mask:0xf
s_nop 1
v_add_f32_dpp v44, v33, v32 quad_perm:[1,1,3,3] row_mask:0xc bank_mask:0xf
v_fma_f32 v45, v44, s38, v3
v_cmp_lt_f32_e32 vcc, 0, v45
s_nop 0
s_and_b64 s[48:49], vcc, s[40:41]
s_cbranch_scc0 Lc102_%=
s_swappc_b64 s[42:43], s[46:47]
Lc102_%=:
v_exp_f32_e32 v45, v45
s_nop 0
v_cndmask_b32_e64 v45, 0, v45, s[40:41]
v_add_f32_e32 v47, v47, v45
v_cvt_pk_bf16_f32 v44, v45, v45
s_nop 1
v_mov_b32_dpp v44, v44 row_ror:8 row_mask:0xa bank_mask:0xf
s_waitcnt vmcnt(8)
s_nop 0
v_and_b32_dpp v28, v44, v4 row_newbcast:0 row_mask:0xf bank_mask:0xf
v_and_b32_dpp v29, v44, v5 row_newbcast:0 row_mask:0xf bank_mask:0xf
v_and_b32_dpp v30, v44, v6 row_newbcast:0 row_mask:0xf bank_mask:0xf
v_and_b32_dpp v31, v44, v7 row_newbcast:0 row_mask:0xf bank_mask:0xf
v_and_b32_dpp v32, v44, v4 row_newbcast:2 row_mask:0xf bank_mask:0xf
v_and_b32_dpp v33, v44, v5 row_newbcast:2 row_mask:0xf bank_mask:0xf
v_and_b32_dpp v34, v44, v6 row_newbcast:2 row_mask:0xf bank_mask:0xf
v_and_b32_dpp v35, v44, v7 row_newbcast:2 row_mask:0xf bank_mask:0xf
v_mfma_f32_16x16x32_bf16 v[40:43], v[28:31], v[64:67], v[40:43]
v_and_b32_dpp v28, v44, v4 row_newbcast:4 row_mask:0xf bank_mask:0xf
v_and_b32_dpp v29, v44, v5 row_newbcast:4 row_mask:0xf bank_mask:0xf
v_and_b32_dpp v30, v44, v6 row_newbcast:4 row_mask:0xf bank_mask:0xf
v_and_b32_dpp v31, v44, v7 row_newbcast:4 row_mask:0xf bank_mask:0xf
v_mfma_f32_16x16x32_bf16 v[40:43], v[32:35], v[68:71], v[40:43]
v_and_b32_dpp v32, v44, v4 row_newbcast:6 row_mask:0xf bank_mask:0xf
v_and_b32_dpp v33, v44, v5 row_newbcast:6 row_mask:0xf bank_mask:0xf
v_and_b32_dpp v34, v44, v6 row_newbcast:6 row_mask:0xf bank_mask:0xf
v_and_b32_dpp v35, v44, v7 row_newbcast:6 row_mask:0xf bank_mask:0xf
v_mfma_f32_16x16x32_bf16 v[40:43], v[28:31], v[72:75], v[40:43]
s_nop 0
v_mfma_f32_16x16x32_bf16 v[40:43], v[32:35], v[76:79], v[40:43]
s_add_u32 s58, s24, 3784704
s_addc_u32 s59, s25, 0
Lp103_%=:
s_memrealtime s[70:71]
s_waitcnt lgkmcnt(0)
s_sub_u32 s72, s68, s70
s_cmp_gt_i32 s72, 0
s_cbranch_scc0 Lg104_%=
s_sleep 1
s_branch Lp103_%=
Lg104_%=:
s_add_u32 s68, s68, s69
global_load_dwordx4 v[64:67], v0, s[58:59] offset:0
global_load_dwordx4 v[68:71], v0, s[58:59] offset:128
global_load_dwordx4 v[72:75], v0, s[58:59] offset:2048
global_load_dwordx4 v[76:79], v0, s[58:59] offset:2176
s_waitcnt vmcnt(8)
v_mfma_f32_16x16x32_bf16 v[28:31], v[16:19], v[80:83], 0
v_mfma_f32_16x16x32_bf16 v[32:35], v[16:19], v[88:91], 0
v_mfma_f32_16x16x32_bf16 v[28:31], v[20:23], v[84:87], v[28:31]
v_mfma_f32_16x16x32_bf16 v[32:35], v[20:23], v[92:95], v[32:35]
s_add_u32 s58, s26, 3784704
s_addc_u32 s59, s27, 0
global_load_dwordx4 v[80:83], v1, s[58:59] offset:0
global_load_dwordx4 v[84:87], v1, s[58:59] offset:256
global_load_dwordx4 v[88:91], v1, s[58:59] offset:512
global_load_dwordx4 v[92:95], v1, s[58:59] offset:768
s_nop 0
v_add_f32_dpp v44, v29, v28 quad_perm:[1,1,3,3] row_mask:0x3 bank_mask:0xf
s_nop 1
v_add_f32_dpp v44, v33, v32 quad_perm:[1,1,3,3] row_mask:0xc bank_mask:0xf
v_fma_f32 v45, v44, s38, v3
v_cmp_lt_f32_e32 vcc, 0, v45
s_nop 0
s_and_b64 s[48:49], vcc, s[40:41]
s_cbranch_scc0 Lc105_%=
s_swappc_b64 s[42:43], s[46:47]
Lc105_%=:
v_exp_f32_e32 v45, v45
s_nop 0
v_cndmask_b32_e64 v45, 0, v45, s[40:41]
v_add_f32_e32 v47, v47, v45
v_cvt_pk_bf16_f32 v44, v45, v45
s_nop 1
v_mov_b32_dpp v44, v44 row_ror:8 row_mask:0xa bank_mask:0xf
s_waitcnt vmcnt(8)
s_nop 0
v_and_b32_dpp v28, v44, v4 row_newbcast:0 row_mask:0xf bank_mask:0xf
v_and_b32_dpp v29, v44, v5 row_newbcast:0 row_mask:0xf bank_mask:0xf
v_and_b32_dpp v30, v44, v6 row_newbcast:0 row_mask:0xf bank_mask:0xf
v_and_b32_dpp v31, v44, v7 row_newbcast:0 row_mask:0xf bank_mask:0xf
v_and_b32_dpp v32, v44, v4 row_newbcast:2 row_mask:0xf bank_mask:0xf
v_and_b32_dpp v33, v44, v5 row_newbcast:2 row_mask:0xf bank_mask:0xf
v_and_b32_dpp v34, v44, v6 row_newbcast:2 row_mask:0xf bank_mask:0xf
v_and_b32_dpp v35, v44, v7 row_newbcast:2 row_mask:0xf bank_mask:0xf
v_mfma_f32_16x16x32_bf16 v[40:43], v[28:31], v[48:51], v[40:43]
v_and_b32_dpp v28, v44, v4 row_newbcast:4 row_mask:0xf bank_mask:0xf
v_and_b32_dpp v29, v44, v5 row_newbcast:4 row_mask:0xf bank_mask:0xf
v_and_b32_dpp v30, v44, v6 row_newbcast:4 row_mask:0xf bank_mask:0xf
v_and_b32_dpp v31, v44, v7 row_newbcast:4 row_mask:0xf bank_mask:0xf
v_mfma_f32_16x16x32_bf16 v[40:43], v[32:35], v[52:55], v[40:43]
v_and_b32_dpp v32, v44, v4 row_newbcast:6 row_mask:0xf bank_mask:0xf
v_and_b32_dpp v33, v44, v5 row_newbcast:6 row_mask:0xf bank_mask:0xf
v_and_b32_dpp v34, v44, v6 row_newbcast:6 row_mask:0xf bank_mask:0xf
v_and_b32_dpp v35, v44, v7 row_newbcast:6 row_mask:0xf bank_mask:0xf
v_mfma_f32_16x16x32_bf16 v[40:43], v[28:31], v[56:59], v[40:43]
s_nop 0
v_mfma_f32_16x16x32_bf16 v[40:43], v[32:35], v[60:63], v[40:43]
s_add_u32 s58, s24, 3899392
s_addc_u32 s59, s25, 0
Lp106_%=:
s_memrealtime s[70:71]
s_waitcnt lgkmcnt(0)
s_sub_u32 s72, s68, s70
s_cmp_gt_i32 s72, 0
s_cbranch_scc0 Lg107_%=
s_sleep 1
s_branch Lp106_%=
Lg107_%=:
s_add_u32 s68, s68, s69
global_load_dwordx4 v[48:51], v0, s[58:59] offset:0 nt
global_load_dwordx4 v[52:55], v0, s[58:59] offset:128 nt
global_load_dwordx4 v[56:59], v0, s[58:59] offset:2048 nt
global_load_dwordx4 v[60:63], v0, s[58:59] offset:2176 nt
s_waitcnt vmcnt(8)
v_mfma_f32_16x16x32_bf16 v[28:31], v[16:19], v[64:67], 0
v_mfma_f32_16x16x32_bf16 v[32:35], v[16:19], v[72:75], 0
v_mfma_f32_16x16x32_bf16 v[28:31], v[20:23], v[68:71], v[28:31]
v_mfma_f32_16x16x32_bf16 v[32:35], v[20:23], v[76:79], v[32:35]
s_add_u32 s58, s26, 3899392
s_addc_u32 s59, s27, 0
global_load_dwordx4 v[64:67], v1, s[58:59] offset:0 nt
global_load_dwordx4 v[68:71], v1, s[58:59] offset:256 nt
global_load_dwordx4 v[72:75], v1, s[58:59] offset:512 nt
global_load_dwordx4 v[76:79], v1, s[58:59] offset:768 nt
s_nop 0
v_add_f32_dpp v44, v29, v28 quad_perm:[1,1,3,3] row_mask:0x3 bank_mask:0xf
s_nop 1
v_add_f32_dpp v44, v33, v32 quad_perm:[1,1,3,3] row_mask:0xc bank_mask:0xf
v_fma_f32 v45, v44, s38, v3
v_cmp_lt_f32_e32 vcc, 0, v45
s_nop 0
s_and_b64 s[48:49], vcc, s[40:41]
s_cbranch_scc0 Lc108_%=
s_swappc_b64 s[42:43], s[46:47]
Lc108_%=:
v_exp_f32_e32 v45, v45
s_nop 0
v_cndmask_b32_e64 v45, 0, v45, s[40:41]
v_add_f32_e32 v47, v47, v45
v_cvt_pk_bf16_f32 v44, v45, v45
s_nop 1
v_mov_b32_dpp v44, v44 row_ror:8 row_mask:0xa bank_mask:0xf
s_waitcnt vmcnt(8)
s_nop 0
v_and_b32_dpp v28, v44, v4 row_newbcast:0 row_mask:0xf bank_mask:0xf
v_and_b32_dpp v29, v44, v5 row_newbcast:0 row_mask:0xf bank_mask:0xf
v_and_b32_dpp v30, v44, v6 row_newbcast:0 row_mask:0xf bank_mask:0xf
v_and_b32_dpp v31, v44, v7 row_newbcast:0 row_mask:0xf bank_mask:0xf
v_and_b32_dpp v32, v44, v4 row_newbcast:2 row_mask:0xf bank_mask:0xf
v_and_b32_dpp v33, v44, v5 row_newbcast:2 row_mask:0xf bank_mask:0xf
v_and_b32_dpp v34, v44, v6 row_newbcast:2 row_mask:0xf bank_mask:0xf
v_and_b32_dpp v35, v44, v7 row_newbcast:2 row_mask:0xf bank_mask:0xf
v_mfma_f32_16x16x32_bf16 v[40:43], v[28:31], v[80:83], v[40:43]
v_and_b32_dpp v28, v44, v4 row_newbcast:4 row_mask:0xf bank_mask:0xf
v_and_b32_dpp v29, v44, v5 row_newbcast:4 row_mask:0xf bank_mask:0xf
v_and_b32_dpp v30, v44, v6 row_newbcast:4 row_mask:0xf bank_mask:0xf
v_and_b32_dpp v31, v44, v7 row_newbcast:4 row_mask:0xf bank_mask:0xf
v_mfma_f32_16x16x32_bf16 v[40:43], v[32:35], v[84:87], v[40:43]
v_and_b32_dpp v32, v44, v4 row_newbcast:6 row_mask:0xf bank_mask:0xf
v_and_b32_dpp v33, v44, v5 row_newbcast:6 row_mask:0xf bank_mask:0xf
v_and_b32_dpp v34, v44, v6 row_newbcast:6 row_mask:0xf bank_mask:0xf
v_and_b32_dpp v35, v44, v7 row_newbcast:6 row_mask:0xf bank_mask:0xf
v_mfma_f32_16x16x32_bf16 v[40:43], v[28:31], v[88:91], v[40:43]
s_nop 0
v_mfma_f32_16x16x32_bf16 v[40:43], v[32:35], v[92:95], v[40:43]
s_add_u32 s58, s24, 4014080
s_addc_u32 s59, s25, 0
Lp109_%=:
s_memrealtime s[70:71]
s_waitcnt lgkmcnt(0)
s_sub_u32 s72, s68, s70
s_cmp_gt_i32 s72, 0
s_cbranch_scc0 Lg110_%=
s_sleep 1
s_branch Lp109_%=
Lg110_%=:
s_add_u32 s68, s68, s69
global_load_dwordx4 v[80:83], v0, s[58:59] offset:0 nt
global_load_dwordx4 v[84:87], v0, s[58:59] offset:128 nt
global_load_dwordx4 v[88:91], v0, s[58:59] offset:2048 nt
global_load_dwordx4 v[92:95], v0, s[58:59] offset:2176 nt
s_add_u32 s58, s64, 32768
s_addc_u32 s59, s65, 0
v_mbcnt_lo_u32_b32 v10, -1, 0
v_mbcnt_hi_u32_b32 v10, -1, v10
v_lshlrev_b32_e32 v10, 3, v10
global_load_dwordx2 v[8:9], v10, s[58:59] offset:0 sc1
s_waitcnt vmcnt(9)
v_mfma_f32_16x16x32_bf16 v[28:31], v[16:19], v[48:51], 0
v_mfma_f32_16x16x32_bf16 v[32:35], v[16:19], v[56:59], 0
v_mfma_f32_16x16x32_bf16 v[28:31], v[20:23], v[52:55], v[28:31]
v_mfma_f32_16x16x32_bf16 v[32:35], v[20:23], v[60:63], v[32:35]
s_add_u32 s58, s26, 4014080
s_addc_u32 s59, s27, 0
global_load_dwordx4 v[48:51], v1, s[58:59] offset:0 nt
global_load_dwordx4 v[52:55], v1, s[58:59] offset:256 nt
global_load_dwordx4 v[56:59], v1, s[58:59] offset:512 nt
global_load_dwordx4 v[60:63], v1, s[58:59] offset:768 nt
s_nop 0
v_add_f32_dpp v44, v29, v28 quad_perm:[1,1,3,3] row_mask:0x3 bank_mask:0xf
s_nop 1
v_add_f32_dpp v44, v33, v32 quad_perm:[1,1,3,3] row_mask:0xc bank_mask:0xf
v_fma_f32 v45, v44, s38, v3
v_cmp_lt_f32_e32 vcc, 0, v45
s_nop 0
s_and_b64 s[48:49], vcc, s[40:41]
s_cbranch_scc0 Lc111_%=
s_swappc_b64 s[42:43], s[46:47]
Lc111_%=:
v_exp_f32_e32 v45, v45
s_nop 0
v_cndmask_b32_e64 v45, 0, v45, s[40:41]
v_add_f32_e32 v47, v47, v45
v_cvt_pk_bf16_f32 v44, v45, v45
s_nop 1
v_mov_b32_dpp v44, v44 row_ror:8 row_mask:0xa bank_mask:0xf
s_waitcnt vmcnt(9)
s_nop 0
v_and_b32_dpp v28, v44, v4 row_newbcast:0 row_mask:0xf bank_mask:0xf
v_and_b32_dpp v29, v44, v5 row_newbcast:0 row_mask:0xf bank_mask:0xf
v_and_b32_dpp v30, v44, v6 row_newbcast:0 row_mask:0xf bank_mask:0xf
v_and_b32_dpp v31, v44, v7 row_newbcast:0 row_mask:0xf bank_mask:0xf
v_and_b32_dpp v32, v44, v4 row_newbcast:2 row_mask:0xf bank_mask:0xf
v_and_b32_dpp v33, v44, v5 row_newbcast:2 row_mask:0xf bank_mask:0xf
v_and_b32_dpp v34, v44, v6 row_newbcast:2 row_mask:0xf bank_mask:0xf
v_and_b32_dpp v35, v44, v7 row_newbcast:2 row_mask:0xf bank_mask:0xf
v_mfma_f32_16x16x32_bf16 v[40:43], v[28:31], v[64:67], v[40:43]
v_and_b32_dpp v28, v44, v4 row_newbcast:4 row_mask:0xf bank_mask:0xf
v_and_b32_dpp v29, v44, v5 row_newbcast:4 row_mask:0xf bank_mask:0xf
v_and_b32_dpp v30, v44, v6 row_newbcast:4 row_mask:0xf bank_mask:0xf
v_and_b32_dpp v31, v44, v7 row_newbcast:4 row_mask:0xf bank_mask:0xf
v_mfma_f32_16x16x32_bf16 v[40:43], v[32:35], v[68:71], v[40:43]
v_and_b32_dpp v32, v44, v4 row_newbcast:6 row_mask:0xf bank_mask:0xf
v_and_b32_dpp v33, v44, v5 row_newbcast:6 row_mask:0xf bank_mask:0xf
v_and_b32_dpp v34, v44, v6 row_newbcast:6 row_mask:0xf bank_mask:0xf
v_and_b32_dpp v35, v44, v7 row_newbcast:6 row_mask:0xf bank_mask:0xf
v_mfma_f32_16x16x32_bf16 v[40:43], v[28:31], v[72:75], v[40:43]
s_nop 0
v_mfma_f32_16x16x32_bf16 v[40:43], v[32:35], v[76:79], v[40:43]
s_add_u32 s58, s24, 1490944
s_addc_u32 s59, s25, 0
Lp112_%=:
s_memrealtime s[70:71]
s_waitcnt lgkmcnt(0)
s_sub_u32 s72, s68, s70
s_cmp_gt_i32 s72, 0
s_cbranch_scc0 Lg113_%=
s_sleep 1
s_branch Lp112_%=
Lg113_%=:
s_add_u32 s68, s68, s69
global_load_dwordx4 v[64:67], v0, s[58:59] offset:0 nt
global_load_dwordx4 v[68:71], v0, s[58:59] offset:128 nt
global_load_dwordx4 v[72:75], v0, s[58:59] offset:2048 nt
global_load_dwordx4 v[76:79], v0, s[58:59] offset:2176 nt
s_waitcnt vmcnt(9)
v_mfma_f32_16x16x32_bf16 v[28:31], v[16:19], v[80:83], 0
v_mfma_f32_16x16x32_bf16 v[32:35], v[16:19], v[88:91], 0
v_mfma_f32_16x16x32_bf16 v[28:31], v[20:23], v[84:87], v[28:31]
v_mfma_f32_16x16x32_bf16 v[32:35], v[20:23], v[92:95], v[32:35]
s_add_u32 s58, s26, 1490944
s_addc_u32 s59, s27, 0
global_load_dwordx4 v[80:83], v1, s[58:59] offset:0 nt
global_load_dwordx4 v[84:87], v1, s[58:59] offset:256 nt
global_load_dwordx4 v[88:91], v1, s[58:59] offset:512 nt
global_load_dwordx4 v[92:95], v1, s[58:59] offset:768 nt
s_nop 0
v_add_f32_dpp v44, v29, v28 quad_perm:[1,1,3,3] row_mask:0x3 bank_mask:0xf
s_nop 1
v_add_f32_dpp v44, v33, v32 quad_perm:[1,1,3,3] row_mask:0xc bank_mask:0xf
v_fma_f32 v45, v44, s38, v3
v_cmp_lt_f32_e32 vcc, 0, v45
s_nop 0
s_and_b64 s[48:49], vcc, s[40:41]
s_cbranch_scc0 Lc114_%=
s_swappc_b64 s[42:43], s[46:47]
Lc114_%=:
v_exp_f32_e32 v45, v45
s_nop 0
v_cndmask_b32_e64 v45, 0, v45, s[40:41]
v_add_f32_e32 v47, v47, v45
v_cvt_pk_bf16_f32 v44, v45, v45
s_nop 1
v_mov_b32_dpp v44, v44 row_ror:8 row_mask:0xa bank_mask:0xf
s_waitcnt vmcnt(8)
s_nop 0
v_and_b32_dpp v28, v44, v4 row_newbcast:0 row_mask:0xf bank_mask:0xf
v_and_b32_dpp v29, v44, v5 row_newbcast:0 row_mask:0xf bank_mask:0xf
v_and_b32_dpp v30, v44, v6 row_newbcast:0 row_mask:0xf bank_mask:0xf
v_and_b32_dpp v31, v44, v7 row_newbcast:0 row_mask:0xf bank_mask:0xf
v_and_b32_dpp v32, v44, v4 row_newbcast:2 row_mask:0xf bank_mask:0xf
v_and_b32_dpp v33, v44, v5 row_newbcast:2 row_mask:0xf bank_mask:0xf
v_and_b32_dpp v34, v44, v6 row_newbcast:2 row_mask:0xf bank_mask:0xf
v_and_b32_dpp v35, v44, v7 row_newbcast:2 row_mask:0xf bank_mask:0xf
v_mfma_f32_16x16x32_bf16 v[40:43], v[28:31], v[48:51], v[40:43]
v_and_b32_dpp v28, v44, v4 row_newbcast:4 row_mask:0xf bank_mask:0xf
v_and_b32_dpp v29, v44, v5 row_newbcast:4 row_mask:0xf bank_mask:0xf
v_and_b32_dpp v30, v44, v6 row_newbcast:4 row_mask:0xf bank_mask:0xf
v_and_b32_dpp v31, v44, v7 row_newbcast:4 row_mask:0xf bank_mask:0xf
v_mfma_f32_16x16x32_bf16 v[40:43], v[32:35], v[52:55], v[40:43]
v_and_b32_dpp v32, v44, v4 row_newbcast:6 row_mask:0xf bank_mask:0xf
v_and_b32_dpp v33, v44, v5 row_newbcast:6 row_mask:0xf bank_mask:0xf
v_and_b32_dpp v34, v44, v6 row_newbcast:6 row_mask:0xf bank_mask:0xf
v_and_b32_dpp v35, v44, v7 row_newbcast:6 row_mask:0xf bank_mask:0xf
v_mfma_f32_16x16x32_bf16 v[40:43], v[28:31], v[56:59], v[40:43]
s_nop 0
v_mfma_f32_16x16x32_bf16 v[40:43], v[32:35], v[60:63], v[40:43]
s_add_u32 s58, s24, 1605632
s_addc_u32 s59, s25, 0
Lp115_%=:
s_memrealtime s[70:71]
s_waitcnt lgkmcnt(0)
s_sub_u32 s72, s68, s70
s_cmp_gt_i32 s72, 0
s_cbranch_scc0 Lg116_%=
s_sleep 1
s_branch Lp115_%=
Lg116_%=:
s_add_u32 s68, s68, s69
global_load_dwordx4 v[48:51], v0, s[58:59] offset:0 nt
global_load_dwordx4 v[52:55], v0, s[58:59] offset:128 nt
global_load_dwordx4 v[56:59], v0, s[58:59] offset:2048 nt
global_load_dwordx4 v[60:63], v0, s[58:59] offset:2176 nt
v_cmp_ne_u32_e32 vcc, s37, v9
s_nop 0
s_cbranch_vccz Lok117_%=
s_add_u32 s58, s64, 32768
s_addc_u32 s59, s65, 0
Lag118_%=:
s_sleep 2
global_load_dwordx2 v[8:9], v10, s[58:59] offset:0 sc1
s_waitcnt vmcnt(0)
v_cmp_ne_u32_e32 vcc, s37, v9
s_cbranch_vccz Lok117_%=
s_sub_u32 s56, s56, 1
s_cbranch_scc0 Lag118_%=
s_mov_b32 s56, 0
Lok117_%=:
s_nop 1
v_mov_b32_e32 v45, v8
v_mbcnt_lo_u32_b32 v44, -1, 0
v_mbcnt_hi_u32_b32 v44, -1, v44
v_and_b32_e32 v8, 1, v44
v_lshlrev_b32_e32 v8, 6, v8
v_and_b32_e32 v44, 48, v44
v_add_u32_e32 v44, v8, v44
ds_bpermute_b32 v8, v44, v45
ds_bpermute_b32 v9, v44, v45 offset:4
ds_bpermute_b32 v10, v44, v45 offset:8
ds_bpermute_b32 v11, v44, v45 offset:12
ds_bpermute_b32 v12, v44, v45 offset:128
ds_bpermute_b32 v13, v44, v45 offset:132
ds_bpermute_b32 v14, v44, v45 offset:136
ds_bpermute_b32 v15, v44, v45 offset:140
s_waitcnt lgkmcnt(0)
s_mov_b32 s69, s73
s_memrealtime s[70:71]
s_waitcnt lgkmcnt(0)
s_sub_u32 s72, s70, s68
s_cmp_gt_i32 s72, 150
s_sub_u32 s73, s70, 150
s_cmp_gt_i32 s72, 150
s_cselect_b32 s68, s73, s68
s_nop 1
v_mfma_f32_16x16x32_bf16 v[28:31], v[8:11], v[128:131], 0
v_mfma_f32_16x16x32_bf16 v[32:35], v[8:11], v[136:139], 0
v_mfma_f32_16x16x32_bf16 v[28:31], v[12:15], v[132:135], v[28:31]
v_mfma_f32_16x16x32_bf16 v[32:35], v[12:15], v[140:143], v[32:35]
s_add_u32 s58, s24, 114688
s_addc_u32 s59, s25, 0
Lp119_%=:
s_memrealtime s[70:71]
s_waitcnt lgkmcnt(0)
s_sub_u32 s72, s68, s70
s_cmp_gt_i32 s72, 0
s_cbranch_scc0 Lg120_%=
s_sleep 1
s_branch Lp119_%=
Lg120_%=:
s_add_u32 s68, s68, s69
global_load_dwordx4 v[128:131], v0, s[58:59] offset:0 nt
global_load_dwordx4 v[132:135], v0, s[58:59] offset:128 nt
global_load_dwordx4 v[136:139], v0, s[58:59] offset:2048 nt
global_load_dwordx4 v[140:143], v0, s[58:59] offset:2176 nt
v_add_f32_dpp v44, v29, v28 quad_perm:[1,1,3,3] row_mask:0x3 bank_mask:0xf
s_nop 1
v_add_f32_dpp v44, v33, v32 quad_perm:[1,1,3,3] row_mask:0xc bank_mask:0xf
v_fma_f32 v45, v44, s38, v2
v_cmp_lt_f32_e32 vcc, 0, v45
s_nop 0
s_and_b64 s[48:49], vcc, s[40:41]
s_cbranch_scc0 Lc121_%=
s_swappc_b64 s[42:43], s[44:45]
Lc121_%=:
v_exp_f32_e32 v45, v45
s_nop 0
v_cndmask_b32_e64 v45, 0, v45, s[40:41]
v_add_f32_e32 v46, v46, v45
v_cvt_pk_bf16_f32 v44, v45, v45
s_nop 1
v_mov_b32_dpp v44, v44 row_ror:8 row_mask:0xa bank_mask:0xf
s_nop 1
v_and_b32_dpp v28, v44, v4 row_newbcast:0 row_mask:0xf bank_mask:0xf
v_and_b32_dpp v29, v44, v5 row_newbcast:0 row_mask:0xf bank_mask:0xf
v_and_b32_dpp v30, v44, v6 row_newbcast:0 row_mask:0xf bank_mask:0xf
v_and_b32_dpp v31, v44, v7 row_newbcast:0 row_mask:0xf bank_mask:0xf
v_and_b32_dpp v32, v44, v4 row_newbcast:2 row_mask:0xf bank_mask:0xf
v_and_b32_dpp v33, v44, v5 row_newbcast:2 row_mask:0xf bank_mask:0xf
v_and_b32_dpp v34, v44, v6 row_newbcast:2 row_mask:0xf bank_mask:0xf
v_and_b32_dpp v35, v44, v7 row_newbcast:2 row_mask:0xf bank_mask:0xf
v_mfma_f32_16x16x32_bf16 v[36:39], v[28:31], v[144:147], v[36:39]
v_and_b32_dpp v28, v44, v4 row_newbcast:4 row_mask:0xf bank_mask:0xf
v_and_b32_dpp v29, v44, v5 row_newbcast:4 row_mask:0xf bank_mask:0xf
v_and_b32_dpp v30, v44, v6 row_newbcast:4 row_mask:0xf bank_mask:0xf
v_and_b32_dpp v31, v44, v7 row_newbcast:4 row_mask:0xf bank_mask:0xf
v_mfma_f32_16x16x32_bf16 v[36:39], v[32:35], v[148:151], v[36:39]
v_and_b32_dpp v32, v44, v4 row_newbcast:6 row_mask:0xf bank_mask:0xf
v_and_b32_dpp v33, v44, v5 row_newbcast:6 row_mask:0xf bank_mask:0xf
v_and_b32_dpp v34, v44, v6 row_newbcast:6 row_mask:0xf bank_mask:0xf
v_and_b32_dpp v35, v44, v7 row_newbcast:6 row_mask:0xf bank_mask:0xf
v_mfma_f32_16x16x32_bf16 v[36:39], v[28:31], v[152:155], v[36:39]
s_nop 0
v_mfma_f32_16x16x32_bf16 v[36:39], v[32:35], v[156:159], v[36:39]
s_add_u32 s58, s26, 114688
s_addc_u32 s59, s27, 0
global_load_dwordx4 v[144:147], v1, s[58:59] offset:0 nt
global_load_dwordx4 v[148:151], v1, s[58:59] offset:256 nt
global_load_dwordx4 v[152:155], v1, s[58:59] offset:512 nt
global_load_dwordx4 v[156:159], v1, s[58:59] offset:768 nt
v_mfma_f32_16x16x32_bf16 v[28:31], v[8:11], v[160:163], 0
v_mfma_f32_16x16x32_bf16 v[32:35], v[8:11], v[168:171], 0
v_mfma_f32_16x16x32_bf16 v[28:31], v[12:15], v[164:167], v[28:31]
v_mfma_f32_16x16x32_bf16 v[32:35], v[12:15], v[172:175], v[32:35]
s_add_u32 s58, s24, 229376
s_addc_u32 s59, s25, 0
Lp122_%=:
s_memrealtime s[70:71]
s_waitcnt lgkmcnt(0)
s_sub_u32 s72, s68, s70
s_cmp_gt_i32 s72, 0
s_cbranch_scc0 Lg123_%=
s_sleep 1
s_branch Lp122_%=
Lg123_%=:
s_add_u32 s68, s68, s69
global_load_dwordx4 v[160:163], v0, s[58:59] offset:0 nt
global_load_dwordx4 v[164:167], v0, s[58:59] offset:128 nt
global_load_dwordx4 v[168:171], v0, s[58:59] offset:2048 nt
global_load_dwordx4 v[172:175], v0, s[58:59] offset:2176 nt
v_add_f32_dpp v44, v29, v28 quad_perm:[1,1,3,3] row_mask:0x3 bank_mask:0xf
s_nop 1
v_add_f32_dpp v44, v33, v32 quad_perm:[1,1,3,3] row_mask:0xc bank_mask:0xf
v_fma_f32 v45, v44, s38, v2
v_cmp_lt_f32_e32 vcc, 0, v45
s_nop 0
s_and_b64 s[48:49], vcc, s[40:41]
s_cbranch_scc0 Lc124_%=
s_swappc_b64 s[42:43], s[44:45]
Lc124_%=:
v_exp_f32_e32 v45, v45
s_nop 0
v_cndmask_b32_e64 v45, 0, v45, s[40:41]
v_add_f32_e32 v46, v46, v45
v_cvt_pk_bf16_f32 v44, v45, v45
s_nop 1
v_mov_b32_dpp v44, v44 row_ror:8 row_mask:0xa bank_mask:0xf
s_nop 1
v_and_b32_dpp v28, v44, v4 row_newbcast:0 row_mask:0xf bank_mask:0xf
v_and_b32_dpp v29, v44, v5 row_newbcast:0 row_mask:0xf bank_mask:0xf
v_and_b32_dpp v30, v44, v6 row_newbcast:0 row_mask:0xf bank_mask:0xf
v_and_b32_dpp v31, v44, v7 row_newbcast:0 row_mask:0xf bank_mask:0xf
v_and_b32_dpp v32, v44, v4 row_newbcast:2 row_mask:0xf bank_mask:0xf
v_and_b32_dpp v33, v44, v5 row_newbcast:2 row_mask:0xf bank_mask:0xf
v_and_b32_dpp v34, v44, v6 row_newbcast:2 row_mask:0xf bank_mask:0xf
v_and_b32_dpp v35, v44, v7 row_newbcast:2 row_mask:0xf bank_mask:0xf
v_mfma_f32_16x16x32_bf16 v[36:39], v[28:31], v[176:179], v[36:39]
v_and_b32_dpp v28, v44, v4 row_newbcast:4 row_mask:0xf bank_mask:0xf
v_and_b32_dpp v29, v44, v5 row_newbcast:4 row_mask:0xf bank_mask:0xf
v_and_b32_dpp v30, v44, v6 row_newbcast:4 row_mask:0xf bank_mask:0xf
v_and_b32_dpp v31, v44, v7 row_newbcast:4 row_mask:0xf bank_mask:0xf
v_mfma_f32_16x16x32_bf16 v[36:39], v[32:35], v[180:183], v[36:39]
v_and_b32_dpp v32, v44, v4 row_newbcast:6 row_mask:0xf bank_mask:0xf
v_and_b32_dpp v33, v44, v5 row_newbcast:6 row_mask:0xf bank_mask:0xf
v_and_b32_dpp v34, v44, v6 row_newbcast:6 row_mask:0xf bank_mask:0xf
v_and_b32_dpp v35, v44, v7 row_newbcast:6 row_mask:0xf bank_mask:0xf
v_mfma_f32_16x16x32_bf16 v[36:39], v[28:31], v[184:187], v[36:39]
s_nop 0
v_mfma_f32_16x16x32_bf16 v[36:39], v[32:35], v[188:191], v[36:39]
s_add_u32 s58, s26, 229376
s_addc_u32 s59, s27, 0
global_load_dwordx4 v[176:179], v1, s[58:59] offset:0 nt
global_load_dwordx4 v[180:183], v1, s[58:59] offset:256 nt
global_load_dwordx4 v[184:187], v1, s[58:59] offset:512 nt
global_load_dwordx4 v[188:191], v1, s[58:59] offset:768 nt
v_mfma_f32_16x16x32_bf16 v[28:31], v[8:11], v[192:195], 0
v_mfma_f32_16x16x32_bf16 v[32:35], v[8:11], v[200:203], 0
v_mfma_f32_16x16x32_bf16 v[28:31], v[12:15], v[196:199], v[28:31]
v_mfma_f32_16x16x32_bf16 v[32:35], v[12:15], v[204:207], v[32:35]
s_add_u32 s58, s24, 344064
s_addc_u32 s59, s25, 0
Lp125_%=:
s_memrealtime s[70:71]
s_waitcnt lgkmcnt(0)
s_sub_u32 s72, s68, s70
s_cmp_gt_i32 s72, 0
s_cbranch_scc0 Lg126_%=
s_sleep 1
s_branch Lp125_%=
Lg126_%=:
s_add_u32 s68, s68, s69
global_load_dwordx4 v[192:195], v0, s[58:59] offset:0 nt
global_load_dwordx4 v[196:199], v0, s[58:59] offset:128 nt
global_load_dwordx4 v[200:203], v0, s[58:59] offset:2048 nt
global_load_dwordx4 v[204:207], v0, s[58:59] offset:2176 nt
v_add_f32_dpp v44, v29, v28 quad_perm:[1,1,3,3] row_mask:0x3 bank_mask:0xf
s_nop 1
v_add_f32_dpp v44, v33, v32 quad_perm:[1,1,3,3] row_mask:0xc bank_mask:0xf
v_fma_f32 v45, v44, s38, v2
v_cmp_lt_f32_e32 vcc, 0, v45
s_nop 0
s_and_b64 s[48:49], vcc, s[40:41]
s_cbranch_scc0 Lc127_%=
s_swappc_b64 s[42:43], s[44:45]
Lc127_%=:
v_exp_f32_e32 v45, v45
s_nop 0
v_cndmask_b32_e64 v45, 0, v45, s[40:41]
v_add_f32_e32 v46, v46, v45
v_cvt_pk_bf16_f32 v44, v45, v45
s_nop 1
v_mov_b32_dpp v44, v44 row_ror:8 row_mask:0xa bank_mask:0xf
s_nop 1
v_and_b32_dpp v28, v44, v4 row_newbcast:0 row_mask:0xf bank_mask:0xf
v_and_b32_dpp v29, v44, v5 row_newbcast:0 row_mask:0xf bank_mask:0xf
v_and_b32_dpp v30, v44, v6 row_newbcast:0 row_mask:0xf bank_mask:0xf
v_and_b32_dpp v31, v44, v7 row_newbcast:0 row_mask:0xf bank_mask:0xf
v_and_b32_dpp v32, v44, v4 row_newbcast:2 row_mask:0xf bank_mask:0xf
v_and_b32_dpp v33, v44, v5 row_newbcast:2 row_mask:0xf bank_mask:0xf
v_and_b32_dpp v34, v44, v6 row_newbcast:2 row_mask:0xf bank_mask:0xf
v_and_b32_dpp v35, v44, v7 row_newbcast:2 row_mask:0xf bank_mask:0xf
v_mfma_f32_16x16x32_bf16 v[36:39], v[28:31], v[208:211], v[36:39]
v_and_b32_dpp v28, v44, v4 row_newbcast:4 row_mask:0xf bank_mask:0xf
v_and_b32_dpp v29, v44, v5 row_newbcast:4 row_mask:0xf bank_mask:0xf
v_and_b32_dpp v30, v44, v6 row_newbcast:4 row_mask:0xf bank_mask:0xf
v_and_b32_dpp v31, v44, v7 row_newbcast:4 row_mask:0xf bank_mask:0xf
v_mfma_f32_16x16x32_bf16 v[36:39], v[32:35], v[212:215], v[36:39]
v_and_b32_dpp v32, v44, v4 row_newbcast:6 row_mask:0xf bank_mask:0xf
v_and_b32_dpp v33, v44, v5 row_newbcast:6 row_mask:0xf bank_mask:0xf
v_and_b32_dpp v34, v44, v6 row_newbcast:6 row_mask:0xf bank_mask:0xf
v_and_b32_dpp v35, v44, v7 row_newbcast:6 row_mask:0xf bank_mask:0xf
v_mfma_f32_16x16x32_bf16 v[36:39], v[28:31], v[216:219], v[36:39]
s_nop 0
v_mfma_f32_16x16x32_bf16 v[36:39], v[32:35], v[220:223], v[36:39]
s_add_u32 s58, s26, 344064
s_addc_u32 s59, s27, 0
global_load_dwordx4 v[208:211], v1, s[58:59] offset:0 nt
global_load_dwordx4 v[212:215], v1, s[58:59] offset:256 nt
global_load_dwordx4 v[216:219], v1, s[58:59] offset:512 nt
global_load_dwordx4 v[220:223], v1, s[58:59] offset:768 nt
v_mfma_f32_16x16x32_bf16 v[28:31], v[8:11], v[224:227], 0
v_mfma_f32_16x16x32_bf16 v[32:35], v[8:11], v[232:235], 0
v_mfma_f32_16x16x32_bf16 v[28:31], v[12:15], v[228:231], v[28:31]
v_mfma_f32_16x16x32_bf16 v[32:35], v[12:15], v[236:239], v[32:35]
s_add_u32 s58, s24, 458752
s_addc_u32 s59, s25, 0
Lp128_%=:
s_memrealtime s[70:71]
s_waitcnt lgkmcnt(0)
s_sub_u32 s72, s68, s70
s_cmp_gt_i32 s72, 0
s_cbranch_scc0 Lg129_%=
s_sleep 1
s_branch Lp128_%=
Lg129_%=:
s_add_u32 s68, s68, s69
global_load_dwordx4 v[224:227], v0, s[58:59] offset:0 nt
global_load_dwordx4 v[228:231], v0, s[58:59] offset:128 nt
global_load_dwordx4 v[232:235], v0, s[58:59] offset:2048 nt
global_load_dwordx4 v[236:239], v0, s[58:59] offset:2176 nt
v_add_f32_dpp v44, v29, v28 quad_perm:[1,1,3,3] row_mask:0x3 bank_mask:0xf
s_nop 1
v_add_f32_dpp v44, v33, v32 quad_perm:[1,1,3,3] row_mask:0xc bank_mask:0xf
v_fma_f32 v45, v44, s38, v2
v_cmp_lt_f32_e32 vcc, 0, v45
s_nop 0
s_and_b64 s[48:49], vcc, s[40:41]
s_cbranch_scc0 Lc130_%=
s_swappc_b64 s[42:43], s[44:45]
Lc130_%=:
v_exp_f32_e32 v45, v45
s_nop 0
v_cndmask_b32_e64 v45, 0, v45, s[40:41]
v_add_f32_e32 v46, v46, v45
v_cvt_pk_bf16_f32 v44, v45, v45
s_nop 1
v_mov_b32_dpp v44, v44 row_ror:8 row_mask:0xa bank_mask:0xf
s_nop 1
v_and_b32_dpp v28, v44, v4 row_newbcast:0 row_mask:0xf bank_mask:0xf
v_and_b32_dpp v29, v44, v5 row_newbcast:0 row_mask:0xf bank_mask:0xf
v_and_b32_dpp v30, v44, v6 row_newbcast:0 row_mask:0xf bank_mask:0xf
v_and_b32_dpp v31, v44, v7 row_newbcast:0 row_mask:0xf bank_mask:0xf
v_and_b32_dpp v32, v44, v4 row_newbcast:2 row_mask:0xf bank_mask:0xf
v_and_b32_dpp v33, v44, v5 row_newbcast:2 row_mask:0xf bank_mask:0xf
v_and_b32_dpp v34, v44, v6 row_newbcast:2 row_mask:0xf bank_mask:0xf
v_and_b32_dpp v35, v44, v7 row_newbcast:2 row_mask:0xf bank_mask:0xf
v_mfma_f32_16x16x32_bf16 v[36:39], v[28:31], v[240:243], v[36:39]
v_and_b32_dpp v28, v44, v4 row_newbcast:4 row_mask:0xf bank_mask:0xf
v_and_b32_dpp v29, v44, v5 row_newbcast:4 row_mask:0xf bank_mask:0xf
v_and_b32_dpp v30, v44, v6 row_newbcast:4 row_mask:0xf bank_mask:0xf
v_and_b32_dpp v31, v44, v7 row_newbcast:4 row_mask:0xf bank_mask:0xf
v_mfma_f32_16x16x32_bf16 v[36:39], v[32:35], v[244:247], v[36:39]
v_and_b32_dpp v32, v44, v4 row_newbcast:6 row_mask:0xf bank_mask:0xf
v_and_b32_dpp v33, v44, v5 row_newbcast:6 row_mask:0xf bank_mask:0xf
v_and_b32_dpp v34, v44, v6 row_newbcast:6 row_mask:0xf bank_mask:0xf
v_and_b32_dpp v35, v44, v7 row_newbcast:6 row_mask:0xf bank_mask:0xf
v_mfma_f32_16x16x32_bf16 v[36:39], v[28:31], v[248:251], v[36:39]
s_nop 0
v_mfma_f32_16x16x32_bf16 v[36:39], v[32:35], v[252:255], v[36:39]
s_add_u32 s58, s26, 458752
s_addc_u32 s59, s27, 0
global_load_dwordx4 v[240:243], v1, s[58:59] offset:0 nt
global_load_dwordx4 v[244:247], v1, s[58:59] offset:256 nt
global_load_dwordx4 v[248:251], v1, s[58:59] offset:512 nt
global_load_dwordx4 v[252:255], v1, s[58:59] offset:768 nt
v_mfma_f32_16x16x32_bf16 v[28:31], v[8:11], a[0:3], 0
v_mfma_f32_16x16x32_bf16 v[32:35], v[8:11], a[8:11], 0
v_mfma_f32_16x16x32_bf16 v[28:31], v[12:15], a[4:7], v[28:31]
v_mfma_f32_16x16x32_bf16 v[32:35], v[12:15], a[12:15], v[32:35]
s_add_u32 s58, s24, 573440
s_addc_u32 s59, s25, 0
Lp131_%=:
s_memrealtime s[70:71]
s_waitcnt lgkmcnt(0)
s_sub_u32 s72, s68, s70
s_cmp_gt_i32 s72, 0
s_cbranch_scc0 Lg132_%=
s_sleep 1
s_branch Lp131_%=
Lg132_%=:
s_add_u32 s68, s68, s69
global_load_dwordx4 a[0:3], v0, s[58:59] offset:0 nt
global_load_dwordx4 a[4:7], v0, s[58:59] offset:128 nt
global_load_dwordx4 a[8:11], v0, s[58:59] offset:2048 nt
global_load_dwordx4 a[12:15], v0, s[58:59] offset:2176 nt
v_add_f32_dpp v44, v29, v28 quad_perm:[1,1,3,3] row_mask:0x3 bank_mask:0xf
s_nop 1
v_add_f32_dpp v44, v33, v32 quad_perm:[1,1,3,3] row_mask:0xc bank_mask:0xf
v_fma_f32 v45, v44, s38, v2
v_cmp_lt_f32_e32 vcc, 0, v45
s_nop 0
s_and_b64 s[48:49], vcc, s[40:41]
s_cbranch_scc0 Lc133_%=
s_swappc_b64 s[42:43], s[44:45]
Lc133_%=:
v_exp_f32_e32 v45, v45
s_nop 0
v_cndmask_b32_e64 v45, 0, v45, s[40:41]
v_add_f32_e32 v46, v46, v45
v_cvt_pk_bf16_f32 v44, v45, v45
s_nop 1
v_mov_b32_dpp v44, v44 row_ror:8 row_mask:0xa bank_mask:0xf
s_nop 1
v_and_b32_dpp v28, v44, v4 row_newbcast:0 row_mask:0xf bank_mask:0xf
v_and_b32_dpp v29, v44, v5 row_newbcast:0 row_mask:0xf bank_mask:0xf
v_and_b32_dpp v30, v44, v6 row_newbcast:0 row_mask:0xf bank_mask:0xf
v_and_b32_dpp v31, v44, v7 row_newbcast:0 row_mask:0xf bank_mask:0xf
v_and_b32_dpp v32, v44, v4 row_newbcast:2 row_mask:0xf bank_mask:0xf
v_and_b32_dpp v33, v44, v5 row_newbcast:2 row_mask:0xf bank_mask:0xf
v_and_b32_dpp v34, v44, v6 row_newbcast:2 row_mask:0xf bank_mask:0xf
v_and_b32_dpp v35, v44, v7 row_newbcast:2 row_mask:0xf bank_mask:0xf
v_mfma_f32_16x16x32_bf16 v[36:39], v[28:31], a[16:19], v[36:39]
v_and_b32_dpp v28, v44, v4 row_newbcast:4 row_mask:0xf bank_mask:0xf
v_and_b32_dpp v29, v44, v5 row_newbcast:4 row_mask:0xf bank_mask:0xf
v_and_b32_dpp v30, v44, v6 row_newbcast:4 row_mask:0xf bank_mask:0xf
v_and_b32_dpp v31, v44, v7 row_newbcast:4 row_mask:0xf bank_mask:0xf
v_mfma_f32_16x16x32_bf16 v[36:39], v[32:35], a[20:23], v[36:39]
v_and_b32_dpp v32, v44, v4 row_newbcast:6 row_mask:0xf bank_mask:0xf
v_and_b32_dpp v33, v44, v5 row_newbcast:6 row_mask:0xf bank_mask:0xf
v_and_b32_dpp v34, v44, v6 row_newbcast:6 row_mask:0xf bank_mask:0xf
v_and_b32_dpp v35, v44, v7 row_newbcast:6 row_mask:0xf bank_mask:0xf
v_mfma_f32_16x16x32_bf16 v[36:39], v[28:31], a[24:27], v[36:39]
s_nop 0
v_mfma_f32_16x16x32_bf16 v[36:39], v[32:35], a[28:31], v[36:39]
s_add_u32 s58, s26, 573440
s_addc_u32 s59, s27, 0
global_load_dwordx4 a[16:19], v1, s[58:59] offset:0 nt
global_load_dwordx4 a[20:23], v1, s[58:59] offset:256 nt
global_load_dwordx4 a[24:27], v1, s[58:59] offset:512 nt
global_load_dwordx4 a[28:31], v1, s[58:59] offset:768 nt
v_mfma_f32_16x16x32_bf16 v[28:31], v[8:11], a[32:35], 0
v_mfma_f32_16x16x32_bf16 v[32:35], v[8:11], a[40:43], 0
v_mfma_f32_16x16x32_bf16 v[28:31], v[12:15], a[36:39], v[28:31]
v_mfma_f32_16x16x32_bf16 v[32:35], v[12:15], a[44:47], v[32:35]
s_add_u32 s58, s24, 688128
s_addc_u32 s59, s25, 0
Lp134_%=:
s_memrealtime s[70:71]
s_waitcnt lgkmcnt(0)
s_sub_u32 s72, s68, s70
s_cmp_gt_i32 s72, 0
s_cbranch_scc0 Lg135_%=
s_sleep 1
s_branch Lp134_%=
Lg135_%=:
s_add_u32 s68, s68, s69
global_load_dwordx4 a[32:35], v0, s[58:59] offset:0 nt
global_load_dwordx4 a[36:39], v0, s[58:59] offset:128 nt
global_load_dwordx4 a[40:43], v0, s[58:59] offset:2048 nt
global_load_dwordx4 a[44:47], v0, s[58:59] offset:2176 nt
v_add_f32_dpp v44, v29, v28 quad_perm:[1,1,3,3] row_mask:0x3 bank_mask:0xf
s_nop 1
v_add_f32_dpp v44, v33, v32 quad_perm:[1,1,3,3] row_mask:0xc bank_mask:0xf
v_fma_f32 v45, v44, s38, v2
v_cmp_lt_f32_e32 vcc, 0, v45
s_nop 0
s_and_b64 s[48:49], vcc, s[40:41]
s_cbranch_scc0 Lc136_%=
s_swappc_b64 s[42:43], s[44:45]
Lc136_%=:
v_exp_f32_e32 v45, v45
s_nop 0
v_cndmask_b32_e64 v45, 0, v45, s[40:41]
v_add_f32_e32 v46, v46, v45
v_cvt_pk_bf16_f32 v44, v45, v45
s_nop 1
v_mov_b32_dpp v44, v44 row_ror:8 row_mask:0xa bank_mask:0xf
s_nop 1
v_and_b32_dpp v28, v44, v4 row_newbcast:0 row_mask:0xf bank_mask:0xf
v_and_b32_dpp v29, v44, v5 row_newbcast:0 row_mask:0xf bank_mask:0xf
v_and_b32_dpp v30, v44, v6 row_newbcast:0 row_mask:0xf bank_mask:0xf
v_and_b32_dpp v31, v44, v7 row_newbcast:0 row_mask:0xf bank_mask:0xf
v_and_b32_dpp v32, v44, v4 row_newbcast:2 row_mask:0xf bank_mask:0xf
v_and_b32_dpp v33, v44, v5 row_newbcast:2 row_mask:0xf bank_mask:0xf
v_and_b32_dpp v34, v44, v6 row_newbcast:2 row_mask:0xf bank_mask:0xf
v_and_b32_dpp v35, v44, v7 row_newbcast:2 row_mask:0xf bank_mask:0xf
v_mfma_f32_16x16x32_bf16 v[36:39], v[28:31], a[48:51], v[36:39]
v_and_b32_dpp v28, v44, v4 row_newbcast:4 row_mask:0xf bank_mask:0xf
v_and_b32_dpp v29, v44, v5 row_newbcast:4 row_mask:0xf bank_mask:0xf
v_and_b32_dpp v30, v44, v6 row_newbcast:4 row_mask:0xf bank_mask:0xf
v_and_b32_dpp v31, v44, v7 row_newbcast:4 row_mask:0xf bank_mask:0xf
v_mfma_f32_16x16x32_bf16 v[36:39], v[32:35], a[52:55], v[36:39]
v_and_b32_dpp v32, v44, v4 row_newbcast:6 row_mask:0xf bank_mask:0xf
v_and_b32_dpp v33, v44, v5 row_newbcast:6 row_mask:0xf bank_mask:0xf
v_and_b32_dpp v34, v44, v6 row_newbcast:6 row_mask:0xf bank_mask:0xf
v_and_b32_dpp v35, v44, v7 row_newbcast:6 row_mask:0xf bank_mask:0xf
v_mfma_f32_16x16x32_bf16 v[36:39], v[28:31], a[56:59], v[36:39]
s_nop 0
v_mfma_f32_16x16x32_bf16 v[36:39], v[32:35], a[60:63], v[36:39]
s_add_u32 s58, s26, 688128
s_addc_u32 s59, s27, 0
global_load_dwordx4 a[48:51], v1, s[58:59] offset:0 nt
global_load_dwordx4 a[52:55], v1, s[58:59] offset:256 nt
global_load_dwordx4 a[56:59], v1, s[58:59] offset:512 nt
global_load_dwordx4 a[60:63], v1, s[58:59] offset:768 nt
v_mfma_f32_16x16x32_bf16 v[28:31], v[8:11], a[64:67], 0
v_mfma_f32_16x16x32_bf16 v[32:35], v[8:11], a[72:75], 0
v_mfma_f32_16x16x32_bf16 v[28:31], v[12:15], a[68:71], v[28:31]
v_mfma_f32_16x16x32_bf16 v[32:35], v[12:15], a[76:79], v[32:35]
s_add_u32 s58, s24, 802816
s_addc_u32 s59, s25, 0
Lp137_%=:
s_memrealtime s[70:71]
s_waitcnt lgkmcnt(0)
s_sub_u32 s72, s68, s70
s_cmp_gt_i32 s72, 0
s_cbranch_scc0 Lg138_%=
s_sleep 1
s_branch Lp137_%=
Lg138_%=:
s_add_u32 s68, s68, s69
s_waitcnt vmcnt(56)
global_load_dwordx4 a[64:67], v0, s[58:59] offset:0 nt
global_load_dwordx4 a[68:71], v0, s[58:59] offset:128 nt
global_load_dwordx4 a[72:75], v0, s[58:59] offset:2048 nt
global_load_dwordx4 a[76:79], v0, s[58:59] offset:2176 nt
v_add_f32_dpp v44, v29, v28 quad_perm:[1,1,3,3] row_mask:0x3 bank_mask:0xf
s_nop 1
v_add_f32_dpp v44, v33, v32 quad_perm:[1,1,3,3] row_mask:0xc bank_mask:0xf
v_fma_f32 v45, v44, s38, v2
v_cmp_lt_f32_e32 vcc, 0, v45
s_nop 0
s_and_b64 s[48:49], vcc, s[40:41]
s_cbranch_scc0 Lc139_%=
s_swappc_b64 s[42:43], s[44:45]
Lc139_%=:
v_exp_f32_e32 v45, v45
s_nop 0
v_cndmask_b32_e64 v45, 0, v45, s[40:41]
v_add_f32_e32 v46, v46, v45
v_cvt_pk_bf16_f32 v44, v45, v45
s_nop 1
v_mov_b32_dpp v44, v44 row_ror:8 row_mask:0xa bank_mask:0xf
s_nop 1
v_and_b32_dpp v28, v44, v4 row_newbcast:0 row_mask:0xf bank_mask:0xf
v_and_b32_dpp v29, v44, v5 row_newbcast:0 row_mask:0xf bank_mask:0xf
v_and_b32_dpp v30, v44, v6 row_newbcast:0 row_mask:0xf bank_mask:0xf
v_and_b32_dpp v31, v44, v7 row_newbcast:0 row_mask:0xf bank_mask:0xf
v_and_b32_dpp v32, v44, v4 row_newbcast:2 row_mask:0xf bank_mask:0xf
v_and_b32_dpp v33, v44, v5 row_newbcast:2 row_mask:0xf bank_mask:0xf
v_and_b32_dpp v34, v44, v6 row_newbcast:2 row_mask:0xf bank_mask:0xf
v_and_b32_dpp v35, v44, v7 row_newbcast:2 row_mask:0xf bank_mask:0xf
v_mfma_f32_16x16x32_bf16 v[36:39], v[28:31], a[80:83], v[36:39]
v_and_b32_dpp v28, v44, v4 row_newbcast:4 row_mask:0xf bank_mask:0xf
v_and_b32_dpp v29, v44, v5 row_newbcast:4 row_mask:0xf bank_mask:0xf
v_and_b32_dpp v30, v44, v6 row_newbcast:4 row_mask:0xf bank_mask:0xf
v_and_b32_dpp v31, v44, v7 row_newbcast:4 row_mask:0xf bank_mask:0xf
v_mfma_f32_16x16x32_bf16 v[36:39], v[32:35], a[84:87], v[36:39]
v_and_b32_dpp v32, v44, v4 row_newbcast:6 row_mask:0xf bank_mask:0xf
v_and_b32_dpp v33, v44, v5 row_newbcast:6 row_mask:0xf bank_mask:0xf
v_and_b32_dpp v34, v44, v6 row_newbcast:6 row_mask:0xf bank_mask:0xf
v_and_b32_dpp v35, v44, v7 row_newbcast:6 row_mask:0xf bank_mask:0xf
v_mfma_f32_16x16x32_bf16 v[36:39], v[28:31], a[88:91], v[36:39]
s_nop 0
v_mfma_f32_16x16x32_bf16 v[36:39], v[32:35], a[92:95], v[36:39]
s_add_u32 s58, s26, 802816
s_addc_u32 s59, s27, 0
s_waitcnt vmcnt(56)
global_load_dwordx4 a[80:83], v1, s[58:59] offset:0 nt
global_load_dwordx4 a[84:87], v1, s[58:59] offset:256 nt
global_load_dwordx4 a[88:91], v1, s[58:59] offset:512 nt
global_load_dwordx4 a[92:95], v1, s[58:59] offset:768 nt
v_mfma_f32_16x16x32_bf16 v[28:31], v[8:11], a[96:99], 0
v_mfma_f32_16x16x32_bf16 v[32:35], v[8:11], a[104:107], 0
v_mfma_f32_16x16x32_bf16 v[28:31], v[12:15], a[100:103], v[28:31]
v_mfma_f32_16x16x32_bf16 v[32:35], v[12:15], a[108:111], v[32:35]
s_add_u32 s58, s24, 917504
s_addc_u32 s59, s25, 0
Lp140_%=:
s_memrealtime s[70:71]
s_waitcnt lgkmcnt(0)
s_sub_u32 s72, s68, s70
s_cmp_gt_i32 s72, 0
s_cbranch_scc0 Lg141_%=
s_sleep 1
s_branch Lp140_%=
Lg141_%=:
s_add_u32 s68, s68, s69
s_waitcnt vmcnt(56)
global_load_dwordx4 a[96:99], v0, s[58:59] offset:0 nt
global_load_dwordx4 a[100:103], v0, s[58:59] offset:128 nt
global_load_dwordx4 a[104:107], v0, s[58:59] offset:2048 nt
global_load_dwordx4 a[108:111], v0, s[58:59] offset:2176 nt
v_add_f32_dpp v44, v29, v28 quad_perm:[1,1,3,3] row_mask:0x3 bank_mask:0xf
s_nop 1
v_add_f32_dpp v44, v33, v32 quad_perm:[1,1,3,3] row_mask:0xc bank_mask:0xf
v_fma_f32 v45, v44, s38, v2
v_cmp_lt_f32_e32 vcc, 0, v45
s_nop 0
s_and_b64 s[48:49], vcc, s[40:41]
s_cbranch_scc0 Lc142_%=
s_swappc_b64 s[42:43], s[44:45]
Lc142_%=:
v_exp_f32_e32 v45, v45
s_nop 0
v_cndmask_b32_e64 v45, 0, v45, s[40:41]
v_add_f32_e32 v46, v46, v45
v_cvt_pk_bf16_f32 v44, v45, v45
s_nop 1
v_mov_b32_dpp v44, v44 row_ror:8 row_mask:0xa bank_mask:0xf
s_nop 1
v_and_b32_dpp v28, v44, v4 row_newbcast:0 row_mask:0xf bank_mask:0xf
v_and_b32_dpp v29, v44, v5 row_newbcast:0 row_mask:0xf bank_mask:0xf
v_and_b32_dpp v30, v44, v6 row_newbcast:0 row_mask:0xf bank_mask:0xf
v_and_b32_dpp v31, v44, v7 row_newbcast:0 row_mask:0xf bank_mask:0xf
v_and_b32_dpp v32, v44, v4 row_newbcast:2 row_mask:0xf bank_mask:0xf
v_and_b32_dpp v33, v44, v5 row_newbcast:2 row_mask:0xf bank_mask:0xf
v_and_b32_dpp v34, v44, v6 row_newbcast:2 row_mask:0xf bank_mask:0xf
v_and_b32_dpp v35, v44, v7 row_newbcast:2 row_mask:0xf bank_mask:0xf
v_mfma_f32_16x16x32_bf16 v[36:39], v[28:31], a[112:115], v[36:39]
v_and_b32_dpp v28, v44, v4 row_newbcast:4 row_mask:0xf bank_mask:0xf
v_and_b32_dpp v29, v44, v5 row_newbcast:4 row_mask:0xf bank_mask:0xf
v_and_b32_dpp v30, v44, v6 row_newbcast:4 row_mask:0xf bank_mask:0xf
v_and_b32_dpp v31, v44, v7 row_newbcast:4 row_mask:0xf bank_mask:0xf
v_mfma_f32_16x16x32_bf16 v[36:39], v[32:35], a[116:119], v[36:39]
v_and_b32_dpp v32, v44, v4 row_newbcast:6 row_mask:0xf bank_mask:0xf
v_and_b32_dpp v33, v44, v5 row_newbcast:6 row_mask:0xf bank_mask:0xf
v_and_b32_dpp v34, v44, v6 row_newbcast:6 row_mask:0xf bank_mask:0xf
v_and_b32_dpp v35, v44, v7 row_newbcast:6 row_mask:0xf bank_mask:0xf
v_mfma_f32_16x16x32_bf16 v[36:39], v[28:31], a[120:123], v[36:39]
s_nop 0
v_mfma_f32_16x16x32_bf16 v[36:39], v[32:35], a[124:127], v[36:39]
s_add_u32 s58, s26, 917504
s_addc_u32 s59, s27, 0
s_waitcnt vmcnt(56)
global_load_dwordx4 a[112:115], v1, s[58:59] offset:0 nt
global_load_dwordx4 a[116:119], v1, s[58:59] offset:256 nt
global_load_dwordx4 a[120:123], v1, s[58:59] offset:512 nt
global_load_dwordx4 a[124:127], v1, s[58:59] offset:768 nt
v_mfma_f32_16x16x32_bf16 v[28:31], v[8:11], a[128:131], 0
v_mfma_f32_16x16x32_bf16 v[32:35], v[8:11], a[136:139], 0
v_mfma_f32_16x16x32_bf16 v[28:31], v[12:15], a[132:135], v[28:31]
v_mfma_f32_16x16x32_bf16 v[32:35], v[12:15], a[140:143], v[32:35]
s_add_u32 s58, s24, 1032192
s_addc_u32 s59, s25, 0
Lp143_%=:
s_memrealtime s[70:71]
s_waitcnt lgkmcnt(0)
s_sub_u32 s72, s68, s70
s_cmp_gt_i32 s72, 0
s_cbranch_scc0 Lg144_%=
s_sleep 1
s_branch Lp143_%=
Lg144_%=:
s_add_u32 s68, s68, s69
s_waitcnt vmcnt(56)
global_load_dwordx4 a[128:131], v0, s[58:59] offset:0 nt
global_load_dwordx4 a[132:135], v0, s[58:59] offset:128 nt
global_load_dwordx4 a[136:139], v0, s[58:59] offset:2048 nt
global_load_dwordx4 a[140:143], v0, s[58:59] offset:2176 nt
v_add_f32_dpp v44, v29, v28 quad_perm:[1,1,3,3] row_mask:0x3 bank_mask:0xf
s_nop 1
v_add_f32_dpp v44, v33, v32 quad_perm:[1,1,3,3] row_mask:0xc bank_mask:0xf
v_fma_f32 v45, v44, s38, v2
v_cmp_lt_f32_e32 vcc, 0, v45
s_nop 0
s_and_b64 s[48:49], vcc, s[40:41]
s_cbranch_scc0 Lc145_%=
s_swappc_b64 s[42:43], s[44:45]
Lc145_%=:
v_exp_f32_e32 v45, v45
s_nop 0
v_cndmask_b32_e64 v45, 0, v45, s[40:41]
v_add_f32_e32 v46, v46, v45
v_cvt_pk_bf16_f32 v44, v45, v45
s_nop 1
v_mov_b32_dpp v44, v44 row_ror:8 row_mask:0xa bank_mask:0xf
s_nop 1
v_and_b32_dpp v28, v44, v4 row_newbcast:0 row_mask:0xf bank_mask:0xf
v_and_b32_dpp v29, v44, v5 row_newbcast:0 row_mask:0xf bank_mask:0xf
v_and_b32_dpp v30, v44, v6 row_newbcast:0 row_mask:0xf bank_mask:0xf
v_and_b32_dpp v31, v44, v7 row_newbcast:0 row_mask:0xf bank_mask:0xf
v_and_b32_dpp v32, v44, v4 row_newbcast:2 row_mask:0xf bank_mask:0xf
v_and_b32_dpp v33, v44, v5 row_newbcast:2 row_mask:0xf bank_mask:0xf
v_and_b32_dpp v34, v44, v6 row_newbcast:2 row_mask:0xf bank_mask:0xf
v_and_b32_dpp v35, v44, v7 row_newbcast:2 row_mask:0xf bank_mask:0xf
v_mfma_f32_16x16x32_bf16 v[36:39], v[28:31], a[144:147], v[36:39]
v_and_b32_dpp v28, v44, v4 row_newbcast:4 row_mask:0xf bank_mask:0xf
v_and_b32_dpp v29, v44, v5 row_newbcast:4 row_mask:0xf bank_mask:0xf
v_and_b32_dpp v30, v44, v6 row_newbcast:4 row_mask:0xf bank_mask:0xf
v_and_b32_dpp v31, v44, v7 row_newbcast:4 row_mask:0xf bank_mask:0xf
v_mfma_f32_16x16x32_bf16 v[36:39], v[32:35], a[148:151], v[36:39]
v_and_b32_dpp v32, v44, v4 row_newbcast:6 row_mask:0xf bank_mask:0xf
v_and_b32_dpp v33, v44, v5 row_newbcast:6 row_mask:0xf bank_mask:0xf
v_and_b32_dpp v34, v44, v6 row_newbcast:6 row_mask:0xf bank_mask:0xf
v_and_b32_dpp v35, v44, v7 row_newbcast:6 row_mask:0xf bank_mask:0xf
v_mfma_f32_16x16x32_bf16 v[36:39], v[28:31], a[152:155], v[36:39]
s_nop 0
v_mfma_f32_16x16x32_bf16 v[36:39], v[32:35], a[156:159], v[36:39]
s_add_u32 s58, s26, 1032192
s_addc_u32 s59, s27, 0
s_waitcnt vmcnt(56)
global_load_dwordx4 a[144:147], v1, s[58:59] offset:0 nt
global_load_dwordx4 a[148:151], v1, s[58:59] offset:256 nt
global_load_dwordx4 a[152:155], v1, s[58:59] offset:512 nt
global_load_dwordx4 a[156:159], v1, s[58:59] offset:768 nt
v_mfma_f32_16x16x32_bf16 v[28:31], v[8:11], a[160:163], 0
v_mfma_f32_16x16x32_bf16 v[32:35], v[8:11], a[168:171], 0
v_mfma_f32_16x16x32_bf16 v[28:31], v[12:15], a[164:167], v[28:31]
v_mfma_f32_16x16x32_bf16 v[32:35], v[12:15], a[172:175], v[32:35]
s_add_u32 s58, s24, 1146880
s_addc_u32 s59, s25, 0
Lp146_%=:
s_memrealtime s[70:71]
s_waitcnt lgkmcnt(0)
s_sub_u32 s72, s68, s70
s_cmp_gt_i32 s72, 0
s_cbranch_scc0 Lg147_%=
s_sleep 1
s_branch Lp146_%=
Lg147_%=:
s_add_u32 s68, s68, s69
s_waitcnt vmcnt(56)
global_load_dwordx4 a[160:163], v0, s[58:59] offset:0 nt
global_load_dwordx4 a[164:167], v0, s[58:59] offset:128 nt
global_load_dwordx4 a[168:171], v0, s[58:59] offset:2048 nt
global_load_dwordx4 a[172:175], v0, s[58:59] offset:2176 nt
v_add_f32_dpp v44, v29, v28 quad_perm:[1,1,3,3] row_mask:0x3 bank_mask:0xf
s_nop 1
v_add_f32_dpp v44, v33, v32 quad_perm:[1,1,3,3] row_mask:0xc bank_mask:0xf
v_fma_f32 v45, v44, s38, v2
v_cmp_lt_f32_e32 vcc, 0, v45
s_nop 0
s_and_b64 s[48:49], vcc, s[40:41]
s_cbranch_scc0 Lc148_%=
s_swappc_b64 s[42:43], s[44:45]
Lc148_%=:
v_exp_f32_e32 v45, v45
s_nop 0
v_cndmask_b32_e64 v45, 0, v45, s[40:41]
v_add_f32_e32 v46, v46, v45
v_cvt_pk_bf16_f32 v44, v45, v45
s_nop 1
v_mov_b32_dpp v44, v44 row_ror:8 row_mask:0xa bank_mask:0xf
s_nop 1
v_and_b32_dpp v28, v44, v4 row_newbcast:0 row_mask:0xf bank_mask:0xf
v_and_b32_dpp v29, v44, v5 row_newbcast:0 row_mask:0xf bank_mask:0xf
v_and_b32_dpp v30, v44, v6 row_newbcast:0 row_mask:0xf bank_mask:0xf
v_and_b32_dpp v31, v44, v7 row_newbcast:0 row_mask:0xf bank_mask:0xf
v_and_b32_dpp v32, v44, v4 row_newbcast:2 row_mask:0xf bank_mask:0xf
v_and_b32_dpp v33, v44, v5 row_newbcast:2 row_mask:0xf bank_mask:0xf
v_and_b32_dpp v34, v44, v6 row_newbcast:2 row_mask:0xf bank_mask:0xf
v_and_b32_dpp v35, v44, v7 row_newbcast:2 row_mask:0xf bank_mask:0xf
v_mfma_f32_16x16x32_bf16 v[36:39], v[28:31], a[176:179], v[36:39]
v_and_b32_dpp v28, v44, v4 row_newbcast:4 row_mask:0xf bank_mask:0xf
v_and_b32_dpp v29, v44, v5 row_newbcast:4 row_mask:0xf bank_mask:0xf
v_and_b32_dpp v30, v44, v6 row_newbcast:4 row_mask:0xf bank_mask:0xf
v_and_b32_dpp v31, v44, v7 row_newbcast:4 row_mask:0xf bank_mask:0xf
v_mfma_f32_16x16x32_bf16 v[36:39], v[32:35], a[180:183], v[36:39]
v_and_b32_dpp v32, v44, v4 row_newbcast:6 row_mask:0xf bank_mask:0xf
v_and_b32_dpp v33, v44, v5 row_newbcast:6 row_mask:0xf bank_mask:0xf
v_and_b32_dpp v34, v44, v6 row_newbcast:6 row_mask:0xf bank_mask:0xf
v_and_b32_dpp v35, v44, v7 row_newbcast:6 row_mask:0xf bank_mask:0xf
v_mfma_f32_16x16x32_bf16 v[36:39], v[28:31], a[184:187], v[36:39]
s_nop 0
v_mfma_f32_16x16x32_bf16 v[36:39], v[32:35], a[188:191], v[36:39]
s_add_u32 s58, s26, 1146880
s_addc_u32 s59, s27, 0
s_waitcnt vmcnt(56)
global_load_dwordx4 a[176:179], v1, s[58:59] offset:0 nt
global_load_dwordx4 a[180:183], v1, s[58:59] offset:256 nt
global_load_dwordx4 a[184:187], v1, s[58:59] offset:512 nt
global_load_dwordx4 a[188:191], v1, s[58:59] offset:768 nt
v_mfma_f32_16x16x32_bf16 v[28:31], v[8:11], a[192:195], 0
v_mfma_f32_16x16x32_bf16 v[32:35], v[8:11], a[200:203], 0
v_mfma_f32_16x16x32_bf16 v[28:31], v[12:15], a[196:199], v[28:31]
v_mfma_f32_16x16x32_bf16 v[32:35], v[12:15], a[204:207], v[32:35]
s_add_u32 s58, s24, 1261568
s_addc_u32 s59, s25, 0
Lp149_%=:
s_memrealtime s[70:71]
s_waitcnt lgkmcnt(0)
s_sub_u32 s72, s68, s70
s_cmp_gt_i32 s72, 0
s_cbranch_scc0 Lg150_%=
s_sleep 1
s_branch Lp149_%=
Lg150_%=:
s_add_u32 s68, s68, s69
s_waitcnt vmcnt(56)
global_load_dwordx4 a[192:195], v0, s[58:59] offset:0 nt
global_load_dwordx4 a[196:199], v0, s[58:59] offset:128 nt
global_load_dwordx4 a[200:203], v0, s[58:59] offset:2048 nt
global_load_dwordx4 a[204:207], v0, s[58:59] offset:2176 nt
v_add_f32_dpp v44, v29, v28 quad_perm:[1,1,3,3] row_mask:0x3 bank_mask:0xf
s_nop 1
v_add_f32_dpp v44, v33, v32 quad_perm:[1,1,3,3] row_mask:0xc bank_mask:0xf
v_fma_f32 v45, v44, s38, v2
v_cmp_lt_f32_e32 vcc, 0, v45
s_nop 0
s_and_b64 s[48:49], vcc, s[40:41]
s_cbranch_scc0 Lc151_%=
s_swappc_b64 s[42:43], s[44:45]
Lc151_%=:
v_exp_f32_e32 v45, v45
s_nop 0
v_cndmask_b32_e64 v45, 0, v45, s[40:41]
v_add_f32_e32 v46, v46, v45
v_cvt_pk_bf16_f32 v44, v45, v45
s_nop 1
v_mov_b32_dpp v44, v44 row_ror:8 row_mask:0xa bank_mask:0xf
s_nop 1
v_and_b32_dpp v28, v44, v4 row_newbcast:0 row_mask:0xf bank_mask:0xf
v_and_b32_dpp v29, v44, v5 row_newbcast:0 row_mask:0xf bank_mask:0xf
v_and_b32_dpp v30, v44, v6 row_newbcast:0 row_mask:0xf bank_mask:0xf
v_and_b32_dpp v31, v44, v7 row_newbcast:0 row_mask:0xf bank_mask:0xf
v_and_b32_dpp v32, v44, v4 row_newbcast:2 row_mask:0xf bank_mask:0xf
v_and_b32_dpp v33, v44, v5 row_newbcast:2 row_mask:0xf bank_mask:0xf
v_and_b32_dpp v34, v44, v6 row_newbcast:2 row_mask:0xf bank_mask:0xf
v_and_b32_dpp v35, v44, v7 row_newbcast:2 row_mask:0xf bank_mask:0xf
v_mfma_f32_16x16x32_bf16 v[36:39], v[28:31], a[208:211], v[36:39]
v_and_b32_dpp v28, v44, v4 row_newbcast:4 row_mask:0xf bank_mask:0xf
v_and_b32_dpp v29, v44, v5 row_newbcast:4 row_mask:0xf bank_mask:0xf
v_and_b32_dpp v30, v44, v6 row_newbcast:4 row_mask:0xf bank_mask:0xf
v_and_b32_dpp v31, v44, v7 row_newbcast:4 row_mask:0xf bank_mask:0xf
v_mfma_f32_16x16x32_bf16 v[36:39], v[32:35], a[212:215], v[36:39]
v_and_b32_dpp v32, v44, v4 row_newbcast:6 row_mask:0xf bank_mask:0xf
v_and_b32_dpp v33, v44, v5 row_newbcast:6 row_mask:0xf bank_mask:0xf
v_and_b32_dpp v34, v44, v6 row_newbcast:6 row_mask:0xf bank_mask:0xf
v_and_b32_dpp v35, v44, v7 row_newbcast:6 row_mask:0xf bank_mask:0xf
v_mfma_f32_16x16x32_bf16 v[36:39], v[28:31], a[216:219], v[36:39]
s_nop 0
v_mfma_f32_16x16x32_bf16 v[36:39], v[32:35], a[220:223], v[36:39]
s_add_u32 s58, s26, 1261568
s_addc_u32 s59, s27, 0
s_waitcnt vmcnt(56)
global_load_dwordx4 a[208:211], v1, s[58:59] offset:0 nt
global_load_dwordx4 a[212:215], v1, s[58:59] offset:256 nt
global_load_dwordx4 a[216:219], v1, s[58:59] offset:512 nt
global_load_dwordx4 a[220:223], v1, s[58:59] offset:768 nt
v_mfma_f32_16x16x32_bf16 v[28:31], v[8:11], a[224:227], 0
v_mfma_f32_16x16x32_bf16 v[32:35], v[8:11], a[232:235], 0
v_mfma_f32_16x16x32_bf16 v[28:31], v[12:15], a[228:231], v[28:31]
v_mfma_f32_16x16x32_bf16 v[32:35], v[12:15], a[236:239], v[32:35]
s_add_u32 s58, s24, 1376256
s_addc_u32 s59, s25, 0
Lp152_%=:
s_memrealtime s[70:71]
s_waitcnt lgkmcnt(0)
s_sub_u32 s72, s68, s70
s_cmp_gt_i32 s72, 0
s_cbranch_scc0 Lg153_%=
s_sleep 1
s_branch Lp152_%=
Lg153_%=:
s_add_u32 s68, s68, s69
s_waitcnt vmcnt(56)
global_load_dwordx4 a[224:227], v0, s[58:59] offset:0 nt
global_load_dwordx4 a[228:231], v0, s[58:59] offset:128 nt
global_load_dwordx4 a[232:235], v0, s[58:59] offset:2048 nt
global_load_dwordx4 a[236:239], v0, s[58:59] offset:2176 nt
v_add_f32_dpp v44, v29, v28 quad_perm:[1,1,3,3] row_mask:0x3 bank_mask:0xf
s_nop 1
v_add_f32_dpp v44, v33, v32 quad_perm:[1,1,3,3] row_mask:0xc bank_mask:0xf
v_fma_f32 v45, v44, s38, v2
v_cmp_lt_f32_e32 vcc, 0, v45
s_nop 0
s_and_b64 s[48:49], vcc, s[40:41]
s_cbranch_scc0 Lc154_%=
s_swappc_b64 s[42:43], s[44:45]
Lc154_%=:
v_exp_f32_e32 v45, v45
s_nop 0
v_cndmask_b32_e64 v45, 0, v45, s[40:41]
v_add_f32_e32 v46, v46, v45
v_cvt_pk_bf16_f32 v44, v45, v45
s_nop 1
v_mov_b32_dpp v44, v44 row_ror:8 row_mask:0xa bank_mask:0xf
s_nop 1
v_and_b32_dpp v28, v44, v4 row_newbcast:0 row_mask:0xf bank_mask:0xf
v_and_b32_dpp v29, v44, v5 row_newbcast:0 row_mask:0xf bank_mask:0xf
v_and_b32_dpp v30, v44, v6 row_newbcast:0 row_mask:0xf bank_mask:0xf
v_and_b32_dpp v31, v44, v7 row_newbcast:0 row_mask:0xf bank_mask:0xf
v_and_b32_dpp v32, v44, v4 row_newbcast:2 row_mask:0xf bank_mask:0xf
v_and_b32_dpp v33, v44, v5 row_newbcast:2 row_mask:0xf bank_mask:0xf
v_and_b32_dpp v34, v44, v6 row_newbcast:2 row_mask:0xf bank_mask:0xf
v_and_b32_dpp v35, v44, v7 row_newbcast:2 row_mask:0xf bank_mask:0xf
v_mfma_f32_16x16x32_bf16 v[36:39], v[28:31], a[240:243], v[36:39]
v_and_b32_dpp v28, v44, v4 row_newbcast:4 row_mask:0xf bank_mask:0xf
v_and_b32_dpp v29, v44, v5 row_newbcast:4 row_mask:0xf bank_mask:0xf
v_and_b32_dpp v30, v44, v6 row_newbcast:4 row_mask:0xf bank_mask:0xf
v_and_b32_dpp v31, v44, v7 row_newbcast:4 row_mask:0xf bank_mask:0xf
v_mfma_f32_16x16x32_bf16 v[36:39], v[32:35], a[244:247], v[36:39]
v_and_b32_dpp v32, v44, v4 row_newbcast:6 row_mask:0xf bank_mask:0xf
v_and_b32_dpp v33, v44, v5 row_newbcast:6 row_mask:0xf bank_mask:0xf
v_and_b32_dpp v34, v44, v6 row_newbcast:6 row_mask:0xf bank_mask:0xf
v_and_b32_dpp v35, v44, v7 row_newbcast:6 row_mask:0xf bank_mask:0xf
v_mfma_f32_16x16x32_bf16 v[36:39], v[28:31], a[248:251], v[36:39]
s_nop 0
v_mfma_f32_16x16x32_bf16 v[36:39], v[32:35], a[252:255], v[36:39]
s_add_u32 s58, s26, 1376256
s_addc_u32 s59, s27, 0
s_waitcnt vmcnt(56)
global_load_dwordx4 a[240:243], v1, s[58:59] offset:0 nt
global_load_dwordx4 a[244:247], v1, s[58:59] offset:256 nt
global_load_dwordx4 a[248:251], v1, s[58:59] offset:512 nt
global_load_dwordx4 a[252:255], v1, s[58:59] offset:768 nt
v_mfma_f32_16x16x32_bf16 v[28:31], v[8:11], v[96:99], 0
v_mfma_f32_16x16x32_bf16 v[32:35], v[8:11], v[104:107], 0
v_mfma_f32_16x16x32_bf16 v[28:31], v[12:15], v[100:103], v[28:31]
v_mfma_f32_16x16x32_bf16 v[32:35], v[12:15], v[108:111], v[32:35]
s_nop 6
v_add_f32_dpp v44, v29, v28 quad_perm:[1,1,3,3] row_mask:0x3 bank_mask:0xf
s_nop 1
v_add_f32_dpp v44, v33, v32 quad_perm:[1,1,3,3] row_mask:0xc bank_mask:0xf
v_fma_f32 v45, v44, s38, v2
v_cmp_lt_f32_e32 vcc, 0, v45
s_nop 0
s_and_b64 s[48:49], vcc, s[40:41]
s_cbranch_scc0 Lc155_%=
s_swappc_b64 s[42:43], s[44:45]
Lc155_%=:
v_exp_f32_e32 v45, v45
s_nop 0
v_cndmask_b32_e64 v45, 0, v45, s[40:41]
v_add_f32_e32 v46, v46, v45
v_cvt_pk_bf16_f32 v44, v45, v45
s_nop 1
v_mov_b32_dpp v44, v44 row_ror:8 row_mask:0xa bank_mask:0xf
s_nop 1
v_and_b32_dpp v28, v44, v4 row_newbcast:0 row_mask:0xf bank_mask:0xf
v_and_b32_dpp v29, v44, v5 row_newbcast:0 row_mask:0xf bank_mask:0xf
v_and_b32_dpp v30, v44, v6 row_newbcast:0 row_mask:0xf bank_mask:0xf
v_and_b32_dpp v31, v44, v7 row_newbcast:0 row_mask:0xf bank_mask:0xf
v_and_b32_dpp v32, v44, v4 row_newbcast:2 row_mask:0xf bank_mask:0xf
v_and_b32_dpp v33, v44, v5 row_newbcast:2 row_mask:0xf bank_mask:0xf
v_and_b32_dpp v34, v44, v6 row_newbcast:2 row_mask:0xf bank_mask:0xf
v_and_b32_dpp v35, v44, v7 row_newbcast:2 row_mask:0xf bank_mask:0xf
v_mfma_f32_16x16x32_bf16 v[36:39], v[28:31], v[112:115], v[36:39]
v_and_b32_dpp v28, v44, v4 row_newbcast:4 row_mask:0xf bank_mask:0xf
v_and_b32_dpp v29, v44, v5 row_newbcast:4 row_mask:0xf bank_mask:0xf
v_and_b32_dpp v30, v44, v6 row_newbcast:4 row_mask:0xf bank_mask:0xf
v_and_b32_dpp v31, v44, v7 row_newbcast:4 row_mask:0xf bank_mask:0xf
v_mfma_f32_16x16x32_bf16 v[36:39], v[32:35], v[116:119], v[36:39]
v_and_b32_dpp v32, v44, v4 row_newbcast:6 row_mask:0xf bank_mask:0xf
v_and_b32_dpp v33, v44, v5 row_newbcast:6 row_mask:0xf bank_mask:0xf
v_and_b32_dpp v34, v44, v6 row_newbcast:6 row_mask:0xf bank_mask:0xf
v_and_b32_dpp v35, v44, v7 row_newbcast:6 row_mask:0xf bank_mask:0xf
v_mfma_f32_16x16x32_bf16 v[36:39], v[28:31], v[120:123], v[36:39]
s_nop 0
v_mfma_f32_16x16x32_bf16 v[36:39], v[32:35], v[124:127], v[36:39]
v_mbcnt_lo_u32_b32 v45, -1, 0
v_mbcnt_hi_u32_b32 v45, -1, v45
v_lshlrev_b32_e32 v45, 4, v45
v_add_u32_e32 v45, s57, v45
ds_read_b128 v[96:99], v45
ds_read_b128 v[100:103], v45 offset:1024
ds_read_b128 v[104:107], v45 offset:2048
ds_read_b128 v[108:111], v45 offset:3072
ds_read_b128 v[112:115], v45 offset:4096
ds_read_b128 v[116:119], v45 offset:5120
ds_read_b128 v[120:123], v45 offset:6144
ds_read_b128 v[124:127], v45 offset:7168
s_waitcnt lgkmcnt(4)
v_mfma_f32_16x16x32_bf16 v[28:31], v[8:11], v[96:99], 0
v_mfma_f32_16x16x32_bf16 v[32:35], v[8:11], v[104:107], 0
v_mfma_f32_16x16x32_bf16 v[28:31], v[12:15], v[100:103], v[28:31]
v_mfma_f32_16x16x32_bf16 v[32:35], v[12:15], v[108:111], v[32:35]
s_nop 6
v_add_f32_dpp v44, v29, v28 quad_perm:[1,1,3,3] row_mask:0x3 bank_mask:0xf
s_nop 1
v_add_f32_dpp v44, v33, v32 quad_perm:[1,1,3,3] row_mask:0xc bank_mask:0xf
v_fma_f32 v45, v44, s38, v2
v_cmp_lt_f32_e32 vcc, 0, v45
s_nop 0
s_and_b64 s[48:49], vcc, s[40:41]
s_cbranch_scc0 Lc156_%=
s_swappc_b64 s[42:43], s[44:45]
Lc156_%=:
v_exp_f32_e32 v45, v45
s_nop 0
v_cndmask_b32_e64 v45, 0, v45, s[40:41]
v_add_f32_e32 v46, v46, v45
v_cvt_pk_bf16_f32 v44, v45, v45
s_nop 1
v_mov_b32_dpp v44, v44 row_ror:8 row_mask:0xa bank_mask:0xf
s_waitcnt lgkmcnt(0)
s_nop 0
v_and_b32_dpp v28, v44, v4 row_newbcast:0 row_mask:0xf bank_mask:0xf
v_and_b32_dpp v29, v44, v5 row_newbcast:0 row_mask:0xf bank_mask:0xf
v_and_b32_dpp v30, v44, v6 row_newbcast:0 row_mask:0xf bank_mask:0xf
v_and_b32_dpp v31, v44, v7 row_newbcast:0 row_mask:0xf bank_mask:0xf
v_and_b32_dpp v32, v44, v4 row_newbcast:2 row_mask:0xf bank_mask:0xf
v_and_b32_dpp v33, v44, v5 row_newbcast:2 row_mask:0xf bank_mask:0xf
v_and_b32_dpp v34, v44, v6 row_newbcast:2 row_mask:0xf bank_mask:0xf
v_and_b32_dpp v35, v44, v7 row_newbcast:2 row_mask:0xf bank_mask:0xf
v_mfma_f32_16x16x32_bf16 v[36:39], v[28:31], v[112:115], v[36:39]
v_and_b32_dpp v28, v44, v4 row_newbcast:4 row_mask:0xf bank_mask:0xf
v_and_b32_dpp v29, v44, v5 row_newbcast:4 row_mask:0xf bank_mask:0xf
v_and_b32_dpp v30, v44, v6 row_newbcast:4 row_mask:0xf bank_mask:0xf
v_and_b32_dpp v31, v44, v7 row_newbcast:4 row_mask:0xf bank_mask:0xf
v_mfma_f32_16x16x32_bf16 v[36:39], v[32:35], v[116:119], v[36:39]
v_and_b32_dpp v32, v44, v4 row_newbcast:6 row_mask:0xf bank_mask:0xf
v_and_b32_dpp v33, v44, v5 row_newbcast:6 row_mask:0xf bank_mask:0xf
v_and_b32_dpp v34, v44, v6 row_newbcast:6 row_mask:0xf bank_mask:0xf
v_and_b32_dpp v35, v44, v7 row_newbcast:6 row_mask:0xf bank_mask:0xf
v_mfma_f32_16x16x32_bf16 v[36:39], v[28:31], v[120:123], v[36:39]
s_nop 0
v_mfma_f32_16x16x32_bf16 v[36:39], v[32:35], v[124:127], v[36:39]
v_mbcnt_lo_u32_b32 v45, -1, 0
v_mbcnt_hi_u32_b32 v45, -1, v45
v_lshlrev_b32_e32 v45, 4, v45
v_add_u32_e32 v45, s57, v45
ds_read_b128 v[96:99], v45 offset:8192
ds_read_b128 v[100:103], v45 offset:9216
ds_read_b128 v[104:107], v45 offset:10240
ds_read_b128 v[108:111], v45 offset:11264
ds_read_b128 v[112:115], v45 offset:12288
ds_read_b128 v[116:119], v45 offset:13312
ds_read_b128 v[120:123], v45 offset:14336
ds_read_b128 v[124:127], v45 offset:15360
s_waitcnt lgkmcnt(4)
v_mfma_f32_16x16x32_bf16 v[28:31], v[8:11], v[96:99], 0
v_mfma_f32_16x16x32_bf16 v[32:35], v[8:11], v[104:107], 0
v_mfma_f32_16x16x32_bf16 v[28:31], v[12:15], v[100:103], v[28:31]
v_mfma_f32_16x16x32_bf16 v[32:35], v[12:15], v[108:111], v[32:35]
s_nop 6
v_add_f32_dpp v44, v29, v28 quad_perm:[1,1,3,3] row_mask:0x3 bank_mask:0xf
s_nop 1
v_add_f32_dpp v44, v33, v32 quad_perm:[1,1,3,3] row_mask:0xc bank_mask:0xf
v_fma_f32 v45, v44, s38, v2
v_cmp_lt_f32_e32 vcc, 0, v45
s_nop 0
s_and_b64 s[48:49], vcc, s[40:41]
s_cbranch_scc0 Lc157_%=
s_swappc_b64 s[42:43], s[44:45]
Lc157_%=:
v_exp_f32_e32 v45, v45
s_nop 0
v_cndmask_b32_e64 v45, 0, v45, s[40:41]
v_add_f32_e32 v46, v46, v45
v_cvt_pk_bf16_f32 v44, v45, v45
s_nop 1
v_mov_b32_dpp v44, v44 row_ror:8 row_mask:0xa bank_mask:0xf
s_waitcnt lgkmcnt(0)
s_nop 0
v_and_b32_dpp v28, v44, v4 row_newbcast:0 row_mask:0xf bank_mask:0xf
v_and_b32_dpp v29, v44, v5 row_newbcast:0 row_mask:0xf bank_mask:0xf
v_and_b32_dpp v30, v44, v6 row_newbcast:0 row_mask:0xf bank_mask:0xf
v_and_b32_dpp v31, v44, v7 row_newbcast:0 row_mask:0xf bank_mask:0xf
v_and_b32_dpp v32, v44, v4 row_newbcast:2 row_mask:0xf bank_mask:0xf
v_and_b32_dpp v33, v44, v5 row_newbcast:2 row_mask:0xf bank_mask:0xf
v_and_b32_dpp v34, v44, v6 row_newbcast:2 row_mask:0xf bank_mask:0xf
v_and_b32_dpp v35, v44, v7 row_newbcast:2 row_mask:0xf bank_mask:0xf
v_mfma_f32_16x16x32_bf16 v[36:39], v[28:31], v[112:115], v[36:39]
v_and_b32_dpp v28, v44, v4 row_newbcast:4 row_mask:0xf bank_mask:0xf
v_and_b32_dpp v29, v44, v5 row_newbcast:4 row_mask:0xf bank_mask:0xf
v_and_b32_dpp v30, v44, v6 row_newbcast:4 row_mask:0xf bank_mask:0xf
v_and_b32_dpp v31, v44, v7 row_newbcast:4 row_mask:0xf bank_mask:0xf
v_mfma_f32_16x16x32_bf16 v[36:39], v[32:35], v[116:119], v[36:39]
v_and_b32_dpp v32, v44, v4 row_newbcast:6 row_mask:0xf bank_mask:0xf
v_and_b32_dpp v33, v44, v5 row_newbcast:6 row_mask:0xf bank_mask:0xf
v_and_b32_dpp v34, v44, v6 row_newbcast:6 row_mask:0xf bank_mask:0xf
v_and_b32_dpp v35, v44, v7 row_newbcast:6 row_mask:0xf bank_mask:0xf
v_mfma_f32_16x16x32_bf16 v[36:39], v[28:31], v[120:123], v[36:39]
s_nop 0
v_mfma_f32_16x16x32_bf16 v[36:39], v[32:35], v[124:127], v[36:39]
v_mbcnt_lo_u32_b32 v45, -1, 0
v_mbcnt_hi_u32_b32 v45, -1, v45
v_lshlrev_b32_e32 v45, 4, v45
v_add_u32_e32 v45, s57, v45
ds_read_b128 v[96:99], v45 offset:16384
ds_read_b128 v[100:103], v45 offset:17408
ds_read_b128 v[104:107], v45 offset:18432
ds_read_b128 v[108:111], v45 offset:19456
ds_read_b128 v[112:115], v45 offset:20480
ds_read_b128 v[116:119], v45 offset:21504
ds_read_b128 v[120:123], v45 offset:22528
ds_read_b128 v[124:127], v45 offset:23552
s_waitcnt lgkmcnt(4)
v_mfma_f32_16x16x32_bf16 v[28:31], v[8:11], v[96:99], 0
v_mfma_f32_16x16x32_bf16 v[32:35], v[8:11], v[104:107], 0
v_mfma_f32_16x16x32_bf16 v[28:31], v[12:15], v[100:103], v[28:31]
v_mfma_f32_16x16x32_bf16 v[32:35], v[12:15], v[108:111], v[32:35]
s_nop 6
v_add_f32_dpp v44, v29, v28 quad_perm:[1,1,3,3] row_mask:0x3 bank_mask:0xf
s_nop 1
v_add_f32_dpp v44, v33, v32 quad_perm:[1,1,3,3] row_mask:0xc bank_mask:0xf
v_fma_f32 v45, v44, s38, v2
v_cmp_lt_f32_e32 vcc, 0, v45
s_nop 0
s_and_b64 s[48:49], vcc, s[40:41]
s_cbranch_scc0 Lc158_%=
s_swappc_b64 s[42:43], s[44:45]
Lc158_%=:
v_exp_f32_e32 v45, v45
s_nop 0
v_cndmask_b32_e64 v45, 0, v45, s[40:41]
v_add_f32_e32 v46, v46, v45
v_cvt_pk_bf16_f32 v44, v45, v45
s_nop 1
v_mov_b32_dpp v44, v44 row_ror:8 row_mask:0xa bank_mask:0xf
s_waitcnt lgkmcnt(0)
s_nop 0
v_and_b32_dpp v28, v44, v4 row_newbcast:0 row_mask:0xf bank_mask:0xf
v_and_b32_dpp v29, v44, v5 row_newbcast:0 row_mask:0xf bank_mask:0xf
v_and_b32_dpp v30, v44, v6 row_newbcast:0 row_mask:0xf bank_mask:0xf
v_and_b32_dpp v31, v44, v7 row_newbcast:0 row_mask:0xf bank_mask:0xf
v_and_b32_dpp v32, v44, v4 row_newbcast:2 row_mask:0xf bank_mask:0xf
v_and_b32_dpp v33, v44, v5 row_newbcast:2 row_mask:0xf bank_mask:0xf
v_and_b32_dpp v34, v44, v6 row_newbcast:2 row_mask:0xf bank_mask:0xf
v_and_b32_dpp v35, v44, v7 row_newbcast:2 row_mask:0xf bank_mask:0xf
v_mfma_f32_16x16x32_bf16 v[36:39], v[28:31], v[112:115], v[36:39]
v_and_b32_dpp v28, v44, v4 row_newbcast:4 row_mask:0xf bank_mask:0xf
v_and_b32_dpp v29, v44, v5 row_newbcast:4 row_mask:0xf bank_mask:0xf
v_and_b32_dpp v30, v44, v6 row_newbcast:4 row_mask:0xf bank_mask:0xf
v_and_b32_dpp v31, v44, v7 row_newbcast:4 row_mask:0xf bank_mask:0xf
v_mfma_f32_16x16x32_bf16 v[36:39], v[32:35], v[116:119], v[36:39]
v_and_b32_dpp v32, v44, v4 row_newbcast:6 row_mask:0xf bank_mask:0xf
v_and_b32_dpp v33, v44, v5 row_newbcast:6 row_mask:0xf bank_mask:0xf
v_and_b32_dpp v34, v44, v6 row_newbcast:6 row_mask:0xf bank_mask:0xf
v_and_b32_dpp v35, v44, v7 row_newbcast:6 row_mask:0xf bank_mask:0xf
v_mfma_f32_16x16x32_bf16 v[36:39], v[28:31], v[120:123], v[36:39]
s_nop 0
v_mfma_f32_16x16x32_bf16 v[36:39], v[32:35], v[124:127], v[36:39]
v_mbcnt_lo_u32_b32 v45, -1, 0
v_mbcnt_hi_u32_b32 v45, -1, v45
v_lshlrev_b32_e32 v45, 4, v45
v_add_u32_e32 v45, s57, v45
ds_read_b128 v[96:99], v45 offset:24576
ds_read_b128 v[100:103], v45 offset:25600
ds_read_b128 v[104:107], v45 offset:26624
ds_read_b128 v[108:111], v45 offset:27648
ds_read_b128 v[112:115], v45 offset:28672
ds_read_b128 v[116:119], v45 offset:29696
ds_read_b128 v[120:123], v45 offset:30720
ds_read_b128 v[124:127], v45 offset:31744
s_waitcnt lgkmcnt(4)
v_mfma_f32_16x16x32_bf16 v[28:31], v[8:11], v[96:99], 0
v_mfma_f32_16x16x32_bf16 v[32:35], v[8:11], v[104:107], 0
v_mfma_f32_16x16x32_bf16 v[28:31], v[12:15], v[100:103], v[28:31]
v_mfma_f32_16x16x32_bf16 v[32:35], v[12:15], v[108:111], v[32:35]
s_nop 6
v_add_f32_dpp v44, v29, v28 quad_perm:[1,1,3,3] row_mask:0x3 bank_mask:0xf
s_nop 1
v_add_f32_dpp v44, v33, v32 quad_perm:[1,1,3,3] row_mask:0xc bank_mask:0xf
v_fma_f32 v45, v44, s38, v2
v_cmp_lt_f32_e32 vcc, 0, v45
s_nop 0
s_and_b64 s[48:49], vcc, s[40:41]
s_cbranch_scc0 Lc159_%=
s_swappc_b64 s[42:43], s[44:45]
Lc159_%=:
v_exp_f32_e32 v45, v45
s_nop 0
v_cndmask_b32_e64 v45, 0, v45, s[40:41]
v_add_f32_e32 v46, v46, v45
v_cvt_pk_bf16_f32 v44, v45, v45
s_nop 1
v_mov_b32_dpp v44, v44 row_ror:8 row_mask:0xa bank_mask:0xf
s_waitcnt lgkmcnt(0)
s_nop 0
v_and_b32_dpp v28, v44, v4 row_newbcast:0 row_mask:0xf bank_mask:0xf
v_and_b32_dpp v29, v44, v5 row_newbcast:0 row_mask:0xf bank_mask:0xf
v_and_b32_dpp v30, v44, v6 row_newbcast:0 row_mask:0xf bank_mask:0xf
v_and_b32_dpp v31, v44, v7 row_newbcast:0 row_mask:0xf bank_mask:0xf
v_and_b32_dpp v32, v44, v4 row_newbcast:2 row_mask:0xf bank_mask:0xf
v_and_b32_dpp v33, v44, v5 row_newbcast:2 row_mask:0xf bank_mask:0xf
v_and_b32_dpp v34, v44, v6 row_newbcast:2 row_mask:0xf bank_mask:0xf
v_and_b32_dpp v35, v44, v7 row_newbcast:2 row_mask:0xf bank_mask:0xf
v_mfma_f32_16x16x32_bf16 v[36:39], v[28:31], v[112:115], v[36:39]
v_and_b32_dpp v28, v44, v4 row_newbcast:4 row_mask:0xf bank_mask:0xf
v_and_b32_dpp v29, v44, v5 row_newbcast:4 row_mask:0xf bank_mask:0xf
v_and_b32_dpp v30, v44, v6 row_newbcast:4 row_mask:0xf bank_mask:0xf
v_and_b32_dpp v31, v44, v7 row_newbcast:4 row_mask:0xf bank_mask:0xf
v_mfma_f32_16x16x32_bf16 v[36:39], v[32:35], v[116:119], v[36:39]
v_and_b32_dpp v32, v44, v4 row_newbcast:6 row_mask:0xf bank_mask:0xf
v_and_b32_dpp v33, v44, v5 row_newbcast:6 row_mask:0xf bank_mask:0xf
v_and_b32_dpp v34, v44, v6 row_newbcast:6 row_mask:0xf bank_mask:0xf
v_and_b32_dpp v35, v44, v7 row_newbcast:6 row_mask:0xf bank_mask:0xf
v_mfma_f32_16x16x32_bf16 v[36:39], v[28:31], v[120:123], v[36:39]
s_nop 0
v_mfma_f32_16x16x32_bf16 v[36:39], v[32:35], v[124:127], v[36:39]
v_mbcnt_lo_u32_b32 v45, -1, 0
v_mbcnt_hi_u32_b32 v45, -1, v45
v_lshlrev_b32_e32 v45, 4, v45
v_add_u32_e32 v45, s57, v45
ds_read_b128 v[96:99], v45 offset:32768
ds_read_b128 v[100:103], v45 offset:33792
ds_read_b128 v[104:107], v45 offset:34816
ds_read_b128 v[108:111], v45 offset:35840
ds_read_b128 v[112:115], v45 offset:36864
ds_read_b128 v[116:119], v45 offset:37888
ds_read_b128 v[120:123], v45 offset:38912
s_waitcnt lgkmcnt(3)
v_mfma_f32_16x16x32_bf16 v[28:31], v[8:11], v[96:99], 0
v_mfma_f32_16x16x32_bf16 v[32:35], v[8:11], v[104:107], 0
v_mfma_f32_16x16x32_bf16 v[28:31], v[12:15], v[100:103], v[28:31]
v_mfma_f32_16x16x32_bf16 v[32:35], v[12:15], v[108:111], v[32:35]
s_nop 6
v_add_f32_dpp v44, v29, v28 quad_perm:[1,1,3,3] row_mask:0x3 bank_mask:0xf
s_nop 1
v_add_f32_dpp v44, v33, v32 quad_perm:[1,1,3,3] row_mask:0xc bank_mask:0xf
v_fma_f32 v45, v44, s38, v2
v_cmp_lt_f32_e32 vcc, 0, v45
s_nop 0
s_and_b64 s[48:49], vcc, s[40:41]
s_cbranch_scc0 Lc160_%=
s_swappc_b64 s[42:43], s[44:45]
Lc160_%=:
v_exp_f32_e32 v45, v45
s_nop 0
v_cndmask_b32_e64 v45, 0, v45, s[40:41]
v_add_f32_e32 v46, v46, v45
v_cvt_pk_bf16_f32 v44, v45, v45
s_nop 1
v_mov_b32_dpp v44, v44 row_ror:8 row_mask:0xa bank_mask:0xf
s_waitcnt lgkmcnt(0)
s_nop 0
v_and_b32_dpp v28, v44, v4 row_newbcast:0 row_mask:0xf bank_mask:0xf
v_and_b32_dpp v29, v44, v5 row_newbcast:0 row_mask:0xf bank_mask:0xf
v_and_b32_dpp v30, v44, v6 row_newbcast:0 row_mask:0xf bank_mask:0xf
v_and_b32_dpp v31, v44, v7 row_newbcast:0 row_mask:0xf bank_mask:0xf
v_and_b32_dpp v32, v44, v4 row_newbcast:2 row_mask:0xf bank_mask:0xf
v_and_b32_dpp v33, v44, v5 row_newbcast:2 row_mask:0xf bank_mask:0xf
v_and_b32_dpp v34, v44, v6 row_newbcast:2 row_mask:0xf bank_mask:0xf
v_and_b32_dpp v35, v44, v7 row_newbcast:2 row_mask:0xf bank_mask:0xf
v_mfma_f32_16x16x32_bf16 v[36:39], v[28:31], v[112:115], v[36:39]
v_and_b32_dpp v28, v44, v4 row_newbcast:4 row_mask:0xf bank_mask:0xf
v_and_b32_dpp v29, v44, v5 row_newbcast:4 row_mask:0xf bank_mask:0xf
v_and_b32_dpp v30, v44, v6 row_newbcast:4 row_mask:0xf bank_mask:0xf
v_and_b32_dpp v31, v44, v7 row_newbcast:4 row_mask:0xf bank_mask:0xf
v_mfma_f32_16x16x32_bf16 v[36:39], v[32:35], v[116:119], v[36:39]
v_and_b32_dpp v32, v44, v4 row_newbcast:6 row_mask:0xf bank_mask:0xf
v_and_b32_dpp v33, v44, v5 row_newbcast:6 row_mask:0xf bank_mask:0xf
v_and_b32_dpp v34, v44, v6 row_newbcast:6 row_mask:0xf bank_mask:0xf
v_and_b32_dpp v35, v44, v7 row_newbcast:6 row_mask:0xf bank_mask:0xf
v_mfma_f32_16x16x32_bf16 v[36:39], v[28:31], v[120:123], v[36:39]
s_nop 0
v_mfma_f32_16x16x32_bf16 v[36:39], v[32:35], v[24:27], v[36:39]
s_add_u32 s58, s24, 0
s_addc_u32 s59, s25, 0
Lp161_%=:
s_memrealtime s[70:71]
s_waitcnt lgkmcnt(0)
s_sub_u32 s72, s68, s70
s_cmp_gt_i32 s72, 0
s_cbranch_scc0 Lg162_%=
s_sleep 1
s_branch Lp161_%=
Lg162_%=:
s_add_u32 s68, s68, s69
s_waitcnt vmcnt(56)
global_load_dwordx4 v[96:99], v0, s[58:59] offset:0 nt
global_load_dwordx4 v[100:103], v0, s[58:59] offset:128 nt
global_load_dwordx4 v[104:107], v0, s[58:59] offset:2048 nt
global_load_dwordx4 v[108:111], v0, s[58:59] offset:2176 nt
s_add_u32 s58, s26, 0
s_addc_u32 s59, s27, 0
s_waitcnt vmcnt(56)
global_load_dwordx4 v[112:115], v1, s[58:59] offset:0 nt
global_load_dwordx4 v[116:119], v1, s[58:59] offset:256 nt
global_load_dwordx4 v[120:123], v1, s[58:59] offset:512 nt
global_load_dwordx4 v[124:127], v1, s[58:59] offset:768 nt
v_mfma_f32_16x16x32_bf16 v[28:31], v[16:19], v[64:67], 0
v_mfma_f32_16x16x32_bf16 v[32:35], v[16:19], v[72:75], 0
v_mfma_f32_16x16x32_bf16 v[28:31], v[20:23], v[68:71], v[28:31]
v_mfma_f32_16x16x32_bf16 v[32:35], v[20:23], v[76:79], v[32:35]
v_mbcnt_lo_u32_b32 v45, -1, 0
v_mbcnt_hi_u32_b32 v45, -1, v45
v_lshlrev_b32_e32 v45, 4, v45
v_add_u32_e32 v45, s57, v45
ds_write_b128 v45, v[64:67]
ds_write_b128 v45, v[68:71] offset:1024
ds_write_b128 v45, v[72:75] offset:2048
ds_write_b128 v45, v[76:79] offset:3072
s_waitcnt lgkmcnt(0)
s_add_u32 s58, s26, 1605632
s_addc_u32 s59, s27, 0
s_waitcnt vmcnt(56)
global_load_dwordx4 v[64:67], v1, s[58:59] offset:0 nt
global_load_dwordx4 v[68:71], v1, s[58:59] offset:256 nt
global_load_dwordx4 v[72:75], v1, s[58:59] offset:512 nt
global_load_dwordx4 v[76:79], v1, s[58:59] offset:768 nt
v_add_f32_dpp v44, v29, v28 quad_perm:[1,1,3,3] row_mask:0x3 bank_mask:0xf
s_nop 1
v_add_f32_dpp v44, v33, v32 quad_perm:[1,1,3,3] row_mask:0xc bank_mask:0xf
v_fma_f32 v45, v44, s38, v3
v_cmp_lt_f32_e32 vcc, 0, v45
s_nop 0
s_and_b64 s[48:49], vcc, s[40:41]
s_cbranch_scc0 Lc163_%=
s_swappc_b64 s[42:43], s[46:47]
Lc163_%=:
v_exp_f32_e32 v45, v45
s_nop 0
v_cndmask_b32_e64 v45, 0, v45, s[40:41]
v_add_f32_e32 v47, v47, v45
v_cvt_pk_bf16_f32 v44, v45, v45
s_nop 1
v_mov_b32_dpp v44, v44 row_ror:8 row_mask:0xa bank_mask:0xf
s_nop 1
v_and_b32_dpp v28, v44, v4 row_newbcast:0 row_mask:0xf bank_mask:0xf
v_and_b32_dpp v29, v44, v5 row_newbcast:0 row_mask:0xf bank_mask:0xf
v_and_b32_dpp v30, v44, v6 row_newbcast:0 row_mask:0xf bank_mask:0xf
v_and_b32_dpp v31, v44, v7 row_newbcast:0 row_mask:0xf bank_mask:0xf
v_and_b32_dpp v32, v44, v4 row_newbcast:2 row_mask:0xf bank_mask:0xf
v_and_b32_dpp v33, v44, v5 row_newbcast:2 row_mask:0xf bank_mask:0xf
v_and_b32_dpp v34, v44, v6 row_newbcast:2 row_mask:0xf bank_mask:0xf
v_and_b32_dpp v35, v44, v7 row_newbcast:2 row_mask:0xf bank_mask:0xf
v_mfma_f32_16x16x32_bf16 v[40:43], v[28:31], v[80:83], v[40:43]
v_and_b32_dpp v28, v44, v4 row_newbcast:4 row_mask:0xf bank_mask:0xf
v_and_b32_dpp v29, v44, v5 row_newbcast:4 row_mask:0xf bank_mask:0xf
v_and_b32_dpp v30, v44, v6 row_newbcast:4 row_mask:0xf bank_mask:0xf
v_and_b32_dpp v31, v44, v7 row_newbcast:4 row_mask:0xf bank_mask:0xf
v_mfma_f32_16x16x32_bf16 v[40:43], v[32:35], v[84:87], v[40:43]
v_and_b32_dpp v32, v44, v4 row_newbcast:6 row_mask:0xf bank_mask:0xf
v_and_b32_dpp v33, v44, v5 row_newbcast:6 row_mask:0xf bank_mask:0xf
v_and_b32_dpp v34, v44, v6 row_newbcast:6 row_mask:0xf bank_mask:0xf
v_and_b32_dpp v35, v44, v7 row_newbcast:6 row_mask:0xf bank_mask:0xf
v_mfma_f32_16x16x32_bf16 v[40:43], v[28:31], v[88:91], v[40:43]
s_nop 0
v_mfma_f32_16x16x32_bf16 v[40:43], v[32:35], v[92:95], v[40:43]
v_mbcnt_lo_u32_b32 v45, -1, 0
v_mbcnt_hi_u32_b32 v45, -1, v45
v_lshlrev_b32_e32 v45, 4, v45
v_add_u32_e32 v45, s57, v45
ds_write_b128 v45, v[80:83] offset:4096
ds_write_b128 v45, v[84:87] offset:5120
ds_write_b128 v45, v[88:91] offset:6144
ds_write_b128 v45, v[92:95] offset:7168
s_waitcnt lgkmcnt(0)
s_add_u32 s58, s24, 1720320
s_addc_u32 s59, s25, 0
Lp164_%=:
s_memrealtime s[70:71]
s_waitcnt lgkmcnt(0)
s_sub_u32 s72, s68, s70
s_cmp_gt_i32 s72, 0
s_cbranch_scc0 Lg165_%=
s_sleep 1
s_branch Lp164_%=
Lg165_%=:
s_add_u32 s68, s68, s69
s_waitcnt vmcnt(56)
global_load_dwordx4 v[80:83], v0, s[58:59] offset:0 nt
global_load_dwordx4 v[84:87], v0, s[58:59] offset:128 nt
global_load_dwordx4 v[88:91], v0, s[58:59] offset:2048 nt
global_load_dwordx4 v[92:95], v0, s[58:59] offset:2176 nt
v_mfma_f32_16x16x32_bf16 v[28:31], v[16:19], v[128:131], 0
v_mfma_f32_16x16x32_bf16 v[32:35], v[16:19], v[136:139], 0
v_mfma_f32_16x16x32_bf16 v[28:31], v[20:23], v[132:135], v[28:31]
v_mfma_f32_16x16x32_bf16 v[32:35], v[20:23], v[140:143], v[32:35]
s_nop 6
v_add_f32_dpp v44, v29, v28 quad_perm:[1,1,3,3] row_mask:0x3 bank_mask:0xf
s_nop 1
v_add_f32_dpp v44, v33, v32 quad_perm:[1,1,3,3] row_mask:0xc bank_mask:0xf
v_fma_f32 v45, v44, s38, v3
v_cmp_lt_f32_e32 vcc, 0, v45
s_nop 0
s_and_b64 s[48:49], vcc, s[40:41]
s_cbranch_scc0 Lc166_%=
s_swappc_b64 s[42:43], s[46:47]
Lc166_%=:
v_exp_f32_e32 v45, v45
s_nop 0
v_cndmask_b32_e64 v45, 0, v45, s[40:41]
v_add_f32_e32 v47, v47, v45
v_cvt_pk_bf16_f32 v44, v45, v45
s_nop 1
v_mov_b32_dpp v44, v44 row_ror:8 row_mask:0xa bank_mask:0xf
s_nop 1
v_and_b32_dpp v28, v44, v4 row_newbcast:0 row_mask:0xf bank_mask:0xf
v_and_b32_dpp v29, v44, v5 row_newbcast:0 row_mask:0xf bank_mask:0xf
v_and_b32_dpp v30, v44, v6 row_newbcast:0 row_mask:0xf bank_mask:0xf
v_and_b32_dpp v31, v44, v7 row_newbcast:0 row_mask:0xf bank_mask:0xf
v_and_b32_dpp v32, v44, v4 row_newbcast:2 row_mask:0xf bank_mask:0xf
v_and_b32_dpp v33, v44, v5 row_newbcast:2 row_mask:0xf bank_mask:0xf
v_and_b32_dpp v34, v44, v6 row_newbcast:2 row_mask:0xf bank_mask:0xf
v_and_b32_dpp v35, v44, v7 row_newbcast:2 row_mask:0xf bank_mask:0xf
v_mfma_f32_16x16x32_bf16 v[40:43], v[28:31], v[144:147], v[40:43]
v_and_b32_dpp v28, v44, v4 row_newbcast:4 row_mask:0xf bank_mask:0xf
v_and_b32_dpp v29, v44, v5 row_newbcast:4 row_mask:0xf bank_mask:0xf
v_and_b32_dpp v30, v44, v6 row_newbcast:4 row_mask:0xf bank_mask:0xf
v_and_b32_dpp v31, v44, v7 row_newbcast:4 row_mask:0xf bank_mask:0xf
v_mfma_f32_16x16x32_bf16 v[40:43], v[32:35], v[148:151], v[40:43]
v_and_b32_dpp v32, v44, v4 row_newbcast:6 row_mask:0xf bank_mask:0xf
v_and_b32_dpp v33, v44, v5 row_newbcast:6 row_mask:0xf bank_mask:0xf
v_and_b32_dpp v34, v44, v6 row_newbcast:6 row_mask:0xf bank_mask:0xf
v_and_b32_dpp v35, v44, v7 row_newbcast:6 row_mask:0xf bank_mask:0xf
v_mfma_f32_16x16x32_bf16 v[40:43], v[28:31], v[152:155], v[40:43]
s_nop 0
v_mfma_f32_16x16x32_bf16 v[40:43], v[32:35], v[156:159], v[40:43]
v_mfma_f32_16x16x32_bf16 v[28:31], v[16:19], v[160:163], 0
v_mfma_f32_16x16x32_bf16 v[32:35], v[16:19], v[168:171], 0
v_mfma_f32_16x16x32_bf16 v[28:31], v[20:23], v[164:167], v[28:31]
v_mfma_f32_16x16x32_bf16 v[32:35], v[20:23], v[172:175], v[32:35]
s_nop 6
v_add_f32_dpp v44, v29, v28 quad_perm:[1,1,3,3] row_mask:0x3 bank_mask:0xf
s_nop 1
v_add_f32_dpp v44, v33, v32 quad_perm:[1,1,3,3] row_mask:0xc bank_mask:0xf
v_fma_f32 v45, v44, s38, v3
v_cmp_lt_f32_e32 vcc, 0, v45
s_nop 0
s_and_b64 s[48:49], vcc, s[40:41]
s_cbranch_scc0 Lc167_%=
s_swappc_b64 s[42:43], s[46:47]
Lc167_%=:
v_exp_f32_e32 v45, v45
s_nop 0
v_cndmask_b32_e64 v45, 0, v45, s[40:41]
v_add_f32_e32 v47, v47, v45
v_cvt_pk_bf16_f32 v44, v45, v45
s_nop 1
v_mov_b32_dpp v44, v44 row_ror:8 row_mask:0xa bank_mask:0xf
s_nop 1
v_and_b32_dpp v28, v44, v4 row_newbcast:0 row_mask:0xf bank_mask:0xf
v_and_b32_dpp v29, v44, v5 row_newbcast:0 row_mask:0xf bank_mask:0xf
v_and_b32_dpp v30, v44, v6 row_newbcast:0 row_mask:0xf bank_mask:0xf
v_and_b32_dpp v31, v44, v7 row_newbcast:0 row_mask:0xf bank_mask:0xf
v_and_b32_dpp v32, v44, v4 row_newbcast:2 row_mask:0xf bank_mask:0xf
v_and_b32_dpp v33, v44, v5 row_newbcast:2 row_mask:0xf bank_mask:0xf
v_and_b32_dpp v34, v44, v6 row_newbcast:2 row_mask:0xf bank_mask:0xf
v_and_b32_dpp v35, v44, v7 row_newbcast:2 row_mask:0xf bank_mask:0xf
v_mfma_f32_16x16x32_bf16 v[40:43], v[28:31], v[176:179], v[40:43]
v_and_b32_dpp v28, v44, v4 row_newbcast:4 row_mask:0xf bank_mask:0xf
v_and_b32_dpp v29, v44, v5 row_newbcast:4 row_mask:0xf bank_mask:0xf
v_and_b32_dpp v30, v44, v6 row_newbcast:4 row_mask:0xf bank_mask:0xf
v_and_b32_dpp v31, v44, v7 row_newbcast:4 row_mask:0xf bank_mask:0xf
v_mfma_f32_16x16x32_bf16 v[40:43], v[32:35], v[180:183], v[40:43]
v_and_b32_dpp v32, v44, v4 row_newbcast:6 row_mask:0xf bank_mask:0xf
v_and_b32_dpp v33, v44, v5 row_newbcast:6 row_mask:0xf bank_mask:0xf
v_and_b32_dpp v34, v44, v6 row_newbcast:6 row_mask:0xf bank_mask:0xf
v_and_b32_dpp v35, v44, v7 row_newbcast:6 row_mask:0xf bank_mask:0xf
v_mfma_f32_16x16x32_bf16 v[40:43], v[28:31], v[184:187], v[40:43]
s_nop 0
v_mfma_f32_16x16x32_bf16 v[40:43], v[32:35], v[188:191], v[40:43]
v_mfma_f32_16x16x32_bf16 v[28:31], v[16:19], v[192:195], 0
v_mfma_f32_16x16x32_bf16 v[32:35], v[16:19], v[200:203], 0
v_mfma_f32_16x16x32_bf16 v[28:31], v[20:23], v[196:199], v[28:31]
v_mfma_f32_16x16x32_bf16 v[32:35], v[20:23], v[204:207], v[32:35]
s_nop 6
v_add_f32_dpp v44, v29, v28 quad_perm:[1,1,3,3] row_mask:0x3 bank_mask:0xf
s_nop 1
v_add_f32_dpp v44, v33, v32 quad_perm:[1,1,3,3] row_mask:0xc bank_mask:0xf
v_fma_f32 v45, v44, s38, v3
v_cmp_lt_f32_e32 vcc, 0, v45
s_nop 0
s_and_b64 s[48:49], vcc, s[40:41]
s_cbranch_scc0 Lc168_%=
s_swappc_b64 s[42:43], s[46:47]
Lc168_%=:
v_exp_f32_e32 v45, v45
s_nop 0
v_cndmask_b32_e64 v45, 0, v45, s[40:41]
v_add_f32_e32 v47, v47, v45
v_cvt_pk_bf16_f32 v44, v45, v45
s_nop 1
v_mov_b32_dpp v44, v44 row_ror:8 row_mask:0xa bank_mask:0xf
s_nop 1
v_and_b32_dpp v28, v44, v4 row_newbcast:0 row_mask:0xf bank_mask:0xf
v_and_b32_dpp v29, v44, v5 row_newbcast:0 row_mask:0xf bank_mask:0xf
v_and_b32_dpp v30, v44, v6 row_newbcast:0 row_mask:0xf bank_mask:0xf
v_and_b32_dpp v31, v44, v7 row_newbcast:0 row_mask:0xf bank_mask:0xf
v_and_b32_dpp v32, v44, v4 row_newbcast:2 row_mask:0xf bank_mask:0xf
v_and_b32_dpp v33, v44, v5 row_newbcast:2 row_mask:0xf bank_mask:0xf
v_and_b32_dpp v34, v44, v6 row_newbcast:2 row_mask:0xf bank_mask:0xf
v_and_b32_dpp v35, v44, v7 row_newbcast:2 row_mask:0xf bank_mask:0xf
v_mfma_f32_16x16x32_bf16 v[40:43], v[28:31], v[208:211], v[40:43]
v_and_b32_dpp v28, v44, v4 row_newbcast:4 row_mask:0xf bank_mask:0xf
v_and_b32_dpp v29, v44, v5 row_newbcast:4 row_mask:0xf bank_mask:0xf
v_and_b32_dpp v30, v44, v6 row_newbcast:4 row_mask:0xf bank_mask:0xf
v_and_b32_dpp v31, v44, v7 row_newbcast:4 row_mask:0xf bank_mask:0xf
v_mfma_f32_16x16x32_bf16 v[40:43], v[32:35], v[212:215], v[40:43]
v_and_b32_dpp v32, v44, v4 row_newbcast:6 row_mask:0xf bank_mask:0xf
v_and_b32_dpp v33, v44, v5 row_newbcast:6 row_mask:0xf bank_mask:0xf
v_and_b32_dpp v34, v44, v6 row_newbcast:6 row_mask:0xf bank_mask:0xf
v_and_b32_dpp v35, v44, v7 row_newbcast:6 row_mask:0xf bank_mask:0xf
v_mfma_f32_16x16x32_bf16 v[40:43], v[28:31], v[216:219], v[40:43]
s_nop 0
v_mfma_f32_16x16x32_bf16 v[40:43], v[32:35], v[220:223], v[40:43]
v_mfma_f32_16x16x32_bf16 v[28:31], v[16:19], v[224:227], 0
v_mfma_f32_16x16x32_bf16 v[32:35], v[16:19], v[232:235], 0
v_mfma_f32_16x16x32_bf16 v[28:31], v[20:23], v[228:231], v[28:31]
v_mfma_f32_16x16x32_bf16 v[32:35], v[20:23], v[236:239], v[32:35]
s_nop 6
v_add_f32_dpp v44, v29, v28 quad_perm:[1,1,3,3] row_mask:0x3 bank_mask:0xf
s_nop 1
v_add_f32_dpp v44, v33, v32 quad_perm:[1,1,3,3] row_mask:0xc bank_mask:0xf
v_fma_f32 v45, v44, s38, v3
v_cmp_lt_f32_e32 vcc, 0, v45
s_nop 0
s_and_b64 s[48:49], vcc, s[40:41]
s_cbranch_scc0 Lc169_%=
s_swappc_b64 s[42:43], s[46:47]
Lc169_%=:
v_exp_f32_e32 v45, v45
s_nop 0
v_cndmask_b32_e64 v45, 0, v45, s[40:41]
v_add_f32_e32 v47, v47, v45
v_cvt_pk_bf16_f32 v44, v45, v45
s_nop 1
v_mov_b32_dpp v44, v44 row_ror:8 row_mask:0xa bank_mask:0xf
s_nop 1
v_and_b32_dpp v28, v44, v4 row_newbcast:0 row_mask:0xf bank_mask:0xf
v_and_b32_dpp v29, v44, v5 row_newbcast:0 row_mask:0xf bank_mask:0xf
v_and_b32_dpp v30, v44, v6 row_newbcast:0 row_mask:0xf bank_mask:0xf
v_and_b32_dpp v31, v44, v7 row_newbcast:0 row_mask:0xf bank_mask:0xf
v_and_b32_dpp v32, v44, v4 row_newbcast:2 row_mask:0xf bank_mask:0xf
v_and_b32_dpp v33, v44, v5 row_newbcast:2 row_mask:0xf bank_mask:0xf
v_and_b32_dpp v34, v44, v6 row_newbcast:2 row_mask:0xf bank_mask:0xf
v_and_b32_dpp v35, v44, v7 row_newbcast:2 row_mask:0xf bank_mask:0xf
v_mfma_f32_16x16x32_bf16 v[40:43], v[28:31], v[240:243], v[40:43]
v_and_b32_dpp v28, v44, v4 row_newbcast:4 row_mask:0xf bank_mask:0xf
v_and_b32_dpp v29, v44, v5 row_newbcast:4 row_mask:0xf bank_mask:0xf
v_and_b32_dpp v30, v44, v6 row_newbcast:4 row_mask:0xf bank_mask:0xf
v_and_b32_dpp v31, v44, v7 row_newbcast:4 row_mask:0xf bank_mask:0xf
v_mfma_f32_16x16x32_bf16 v[40:43], v[32:35], v[244:247], v[40:43]
v_and_b32_dpp v32, v44, v4 row_newbcast:6 row_mask:0xf bank_mask:0xf
v_and_b32_dpp v33, v44, v5 row_newbcast:6 row_mask:0xf bank_mask:0xf
v_and_b32_dpp v34, v44, v6 row_newbcast:6 row_mask:0xf bank_mask:0xf
v_and_b32_dpp v35, v44, v7 row_newbcast:6 row_mask:0xf bank_mask:0xf
v_mfma_f32_16x16x32_bf16 v[40:43], v[28:31], v[248:251], v[40:43]
s_nop 0
v_mfma_f32_16x16x32_bf16 v[40:43], v[32:35], v[252:255], v[40:43]
v_mfma_f32_16x16x32_bf16 v[28:31], v[16:19], a[0:3], 0
v_mfma_f32_16x16x32_bf16 v[32:35], v[16:19], a[8:11], 0
v_mfma_f32_16x16x32_bf16 v[28:31], v[20:23], a[4:7], v[28:31]
v_mfma_f32_16x16x32_bf16 v[32:35], v[20:23], a[12:15], v[32:35]
s_nop 6
v_add_f32_dpp v44, v29, v28 quad_perm:[1,1,3,3] row_mask:0x3 bank_mask:0xf
s_nop 1
v_add_f32_dpp v44, v33, v32 quad_perm:[1,1,3,3] row_mask:0xc bank_mask:0xf
v_fma_f32 v45, v44, s38, v3
v_cmp_lt_f32_e32 vcc, 0, v45
s_nop 0
s_and_b64 s[48:49], vcc, s[40:41]
s_cbranch_scc0 Lc170_%=
s_swappc_b64 s[42:43], s[46:47]
Lc170_%=:
v_exp_f32_e32 v45, v45
s_nop 0
v_cndmask_b32_e64 v45, 0, v45, s[40:41]
v_add_f32_e32 v47, v47, v45
v_cvt_pk_bf16_f32 v44, v45, v45
s_nop 1
v_mov_b32_dpp v44, v44 row_ror:8 row_mask:0xa bank_mask:0xf
s_nop 1
v_and_b32_dpp v28, v44, v4 row_newbcast:0 row_mask:0xf bank_mask:0xf
v_and_b32_dpp v29, v44, v5 row_newbcast:0 row_mask:0xf bank_mask:0xf
v_and_b32_dpp v30, v44, v6 row_newbcast:0 row_mask:0xf bank_mask:0xf
v_and_b32_dpp v31, v44, v7 row_newbcast:0 row_mask:0xf bank_mask:0xf
v_and_b32_dpp v32, v44, v4 row_newbcast:2 row_mask:0xf bank_mask:0xf
v_and_b32_dpp v33, v44, v5 row_newbcast:2 row_mask:0xf bank_mask:0xf
v_and_b32_dpp v34, v44, v6 row_newbcast:2 row_mask:0xf bank_mask:0xf
v_and_b32_dpp v35, v44, v7 row_newbcast:2 row_mask:0xf bank_mask:0xf
v_mfma_f32_16x16x32_bf16 v[40:43], v[28:31], a[16:19], v[40:43]
v_and_b32_dpp v28, v44, v4 row_newbcast:4 row_mask:0xf bank_mask:0xf
v_and_b32_dpp v29, v44, v5 row_newbcast:4 row_mask:0xf bank_mask:0xf
v_and_b32_dpp v30, v44, v6 row_newbcast:4 row_mask:0xf bank_mask:0xf
v_and_b32_dpp v31, v44, v7 row_newbcast:4 row_mask:0xf bank_mask:0xf
v_mfma_f32_16x16x32_bf16 v[40:43], v[32:35], a[20:23], v[40:43]
v_and_b32_dpp v32, v44, v4 row_newbcast:6 row_mask:0xf bank_mask:0xf
v_and_b32_dpp v33, v44, v5 row_newbcast:6 row_mask:0xf bank_mask:0xf
v_and_b32_dpp v34, v44, v6 row_newbcast:6 row_mask:0xf bank_mask:0xf
v_and_b32_dpp v35, v44, v7 row_newbcast:6 row_mask:0xf bank_mask:0xf
v_mfma_f32_16x16x32_bf16 v[40:43], v[28:31], a[24:27], v[40:43]
s_nop 0
v_mfma_f32_16x16x32_bf16 v[40:43], v[32:35], a[28:31], v[40:43]
v_mfma_f32_16x16x32_bf16 v[28:31], v[16:19], a[32:35], 0
v_mfma_f32_16x16x32_bf16 v[32:35], v[16:19], a[40:43], 0
v_mfma_f32_16x16x32_bf16 v[28:31], v[20:23], a[36:39], v[28:31]
v_mfma_f32_16x16x32_bf16 v[32:35], v[20:23], a[44:47], v[32:35]
s_nop 6
v_add_f32_dpp v44, v29, v28 quad_perm:[1,1,3,3] row_mask:0x3 bank_mask:0xf
s_nop 1
v_add_f32_dpp v44, v33, v32 quad_perm:[1,1,3,3] row_mask:0xc bank_mask:0xf
v_fma_f32 v45, v44, s38, v3
v_cmp_lt_f32_e32 vcc, 0, v45
s_nop 0
s_and_b64 s[48:49], vcc, s[40:41]
s_cbranch_scc0 Lc171_%=
s_swappc_b64 s[42:43], s[46:47]
Lc171_%=:
v_exp_f32_e32 v45, v45
s_nop 0
v_cndmask_b32_e64 v45, 0, v45, s[40:41]
v_add_f32_e32 v47, v47, v45
v_cvt_pk_bf16_f32 v44, v45, v45
s_nop 1
v_mov_b32_dpp v44, v44 row_ror:8 row_mask:0xa bank_mask:0xf
s_nop 1
v_and_b32_dpp v28, v44, v4 row_newbcast:0 row_mask:0xf bank_mask:0xf
v_and_b32_dpp v29, v44, v5 row_newbcast:0 row_mask:0xf bank_mask:0xf
v_and_b32_dpp v30, v44, v6 row_newbcast:0 row_mask:0xf bank_mask:0xf
v_and_b32_dpp v31, v44, v7 row_newbcast:0 row_mask:0xf bank_mask:0xf
v_and_b32_dpp v32, v44, v4 row_newbcast:2 row_mask:0xf bank_mask:0xf
v_and_b32_dpp v33, v44, v5 row_newbcast:2 row_mask:0xf bank_mask:0xf
v_and_b32_dpp v34, v44, v6 row_newbcast:2 row_mask:0xf bank_mask:0xf
v_and_b32_dpp v35, v44, v7 row_newbcast:2 row_mask:0xf bank_mask:0xf
v_mfma_f32_16x16x32_bf16 v[40:43], v[28:31], a[48:51], v[40:43]
v_and_b32_dpp v28, v44, v4 row_newbcast:4 row_mask:0xf bank_mask:0xf
v_and_b32_dpp v29, v44, v5 row_newbcast:4 row_mask:0xf bank_mask:0xf
v_and_b32_dpp v30, v44, v6 row_newbcast:4 row_mask:0xf bank_mask:0xf
v_and_b32_dpp v31, v44, v7 row_newbcast:4 row_mask:0xf bank_mask:0xf
v_mfma_f32_16x16x32_bf16 v[40:43], v[32:35], a[52:55], v[40:43]
v_and_b32_dpp v32, v44, v4 row_newbcast:6 row_mask:0xf bank_mask:0xf
v_and_b32_dpp v33, v44, v5 row_newbcast:6 row_mask:0xf bank_mask:0xf
v_and_b32_dpp v34, v44, v6 row_newbcast:6 row_mask:0xf bank_mask:0xf
v_and_b32_dpp v35, v44, v7 row_newbcast:6 row_mask:0xf bank_mask:0xf
v_mfma_f32_16x16x32_bf16 v[40:43], v[28:31], a[56:59], v[40:43]
s_nop 0
v_mfma_f32_16x16x32_bf16 v[40:43], v[32:35], a[60:63], v[40:43]
v_mfma_f32_16x16x32_bf16 v[28:31], v[16:19], a[64:67], 0
v_mfma_f32_16x16x32_bf16 v[32:35], v[16:19], a[72:75], 0
v_mfma_f32_16x16x32_bf16 v[28:31], v[20:23], a[68:71], v[28:31]
v_mfma_f32_16x16x32_bf16 v[32:35], v[20:23], a[76:79], v[32:35]
s_nop 6
v_add_f32_dpp v44, v29, v28 quad_perm:[1,1,3,3] row_mask:0x3 bank_mask:0xf
s_nop 1
v_add_f32_dpp v44, v33, v32 quad_perm:[1,1,3,3] row_mask:0xc bank_mask:0xf
v_fma_f32 v45, v44, s38, v3
v_cmp_lt_f32_e32 vcc, 0, v45
s_nop 0
s_and_b64 s[48:49], vcc, s[40:41]
s_cbranch_scc0 Lc172_%=
s_swappc_b64 s[42:43], s[46:47]
Lc172_%=:
v_exp_f32_e32 v45, v45
s_nop 0
v_cndmask_b32_e64 v45, 0, v45, s[40:41]
v_add_f32_e32 v47, v47, v45
v_cvt_pk_bf16_f32 v44, v45, v45
s_nop 1
v_mov_b32_dpp v44, v44 row_ror:8 row_mask:0xa bank_mask:0xf
s_waitcnt vmcnt(56)
s_nop 0
v_and_b32_dpp v28, v44, v4 row_newbcast:0 row_mask:0xf bank_mask:0xf
v_and_b32_dpp v29, v44, v5 row_newbcast:0 row_mask:0xf bank_mask:0xf
v_and_b32_dpp v30, v44, v6 row_newbcast:0 row_mask:0xf bank_mask:0xf
v_and_b32_dpp v31, v44, v7 row_newbcast:0 row_mask:0xf bank_mask:0xf
v_and_b32_dpp v32, v44, v4 row_newbcast:2 row_mask:0xf bank_mask:0xf
v_and_b32_dpp v33, v44, v5 row_newbcast:2 row_mask:0xf bank_mask:0xf
v_and_b32_dpp v34, v44, v6 row_newbcast:2 row_mask:0xf bank_mask:0xf
v_and_b32_dpp v35, v44, v7 row_newbcast:2 row_mask:0xf bank_mask:0xf
v_mfma_f32_16x16x32_bf16 v[40:43], v[28:31], a[80:83], v[40:43]
v_and_b32_dpp v28, v44, v4 row_newbcast:4 row_mask:0xf bank_mask:0xf
v_and_b32_dpp v29, v44, v5 row_newbcast:4 row_mask:0xf bank_mask:0xf
v_and_b32_dpp v30, v44, v6 row_newbcast:4 row_mask:0xf bank_mask:0xf
v_and_b32_dpp v31, v44, v7 row_newbcast:4 row_mask:0xf bank_mask:0xf
v_mfma_f32_16x16x32_bf16 v[40:43], v[32:35], a[84:87], v[40:43]
v_and_b32_dpp v32, v44, v4 row_newbcast:6 row_mask:0xf bank_mask:0xf
v_and_b32_dpp v33, v44, v5 row_newbcast:6 row_mask:0xf bank_mask:0xf
v_and_b32_dpp v34, v44, v6 row_newbcast:6 row_mask:0xf bank_mask:0xf
v_and_b32_dpp v35, v44, v7 row_newbcast:6 row_mask:0xf bank_mask:0xf
v_mfma_f32_16x16x32_bf16 v[40:43], v[28:31], a[88:91], v[40:43]
s_nop 0
v_mfma_f32_16x16x32_bf16 v[40:43], v[32:35], a[92:95], v[40:43]
s_waitcnt vmcnt(52)
v_mfma_f32_16x16x32_bf16 v[28:31], v[16:19], a[96:99], 0
v_mfma_f32_16x16x32_bf16 v[32:35], v[16:19], a[104:107], 0
v_mfma_f32_16x16x32_bf16 v[28:31], v[20:23], a[100:103], v[28:31]
v_mfma_f32_16x16x32_bf16 v[32:35], v[20:23], a[108:111], v[32:35]
s_nop 6
v_add_f32_dpp v44, v29, v28 quad_perm:[1,1,3,3] row_mask:0x3 bank_mask:0xf
s_nop 1
v_add_f32_dpp v44, v33, v32 quad_perm:[1,1,3,3] row_mask:0xc bank_mask:0xf
v_fma_f32 v45, v44, s38, v3
v_cmp_lt_f32_e32 vcc, 0, v45
s_nop 0
s_and_b64 s[48:49], vcc, s[40:41]
s_cbranch_scc0 Lc173_%=
s_swappc_b64 s[42:43], s[46:47]
Lc173_%=:
v_exp_f32_e32 v45, v45
s_nop 0
v_cndmask_b32_e64 v45, 0, v45, s[40:41]
v_add_f32_e32 v47, v47, v45
v_cvt_pk_bf16_f32 v44, v45, v45
s_nop 1
v_mov_b32_dpp v44, v44 row_ror:8 row_mask:0xa bank_mask:0xf
s_waitcnt vmcnt(48)
s_nop 0
v_and_b32_dpp v28, v44, v4 row_newbcast:0 row_mask:0xf bank_mask:0xf
v_and_b32_dpp v29, v44, v5 row_newbcast:0 row_mask:0xf bank_mask:0xf
v_and_b32_dpp v30, v44, v6 row_newbcast:0 row_mask:0xf bank_mask:0xf
v_and_b32_dpp v31, v44, v7 row_newbcast:0 row_mask:0xf bank_mask:0xf
v_and_b32_dpp v32, v44, v4 row_newbcast:2 row_mask:0xf bank_mask:0xf
v_and_b32_dpp v33, v44, v5 row_newbcast:2 row_mask:0xf bank_mask:0xf
v_and_b32_dpp v34, v44, v6 row_newbcast:2 row_mask:0xf bank_mask:0xf
v_and_b32_dpp v35, v44, v7 row_newbcast:2 row_mask:0xf bank_mask:0xf
v_mfma_f32_16x16x32_bf16 v[40:43], v[28:31], a[112:115], v[40:43]
v_and_b32_dpp v28, v44, v4 row_newbcast:4 row_mask:0xf bank_mask:0xf
v_and_b32_dpp v29, v44, v5 row_newbcast:4 row_mask:0xf bank_mask:0xf
v_and_b32_dpp v30, v44, v6 row_newbcast:4 row_mask:0xf bank_mask:0xf
v_and_b32_dpp v31, v44, v7 row_newbcast:4 row_mask:0xf bank_mask:0xf
v_mfma_f32_16x16x32_bf16 v[40:43], v[32:35], a[116:119], v[40:43]
v_and_b32_dpp v32, v44, v4 row_newbcast:6 row_mask:0xf bank_mask:0xf
v_and_b32_dpp v33, v44, v5 row_newbcast:6 row_mask:0xf bank_mask:0xf
v_and_b32_dpp v34, v44, v6 row_newbcast:6 row_mask:0xf bank_mask:0xf
v_and_b32_dpp v35, v44, v7 row_newbcast:6 row_mask:0xf bank_mask:0xf
v_mfma_f32_16x16x32_bf16 v[40:43], v[28:31], a[120:123], v[40:43]
s_nop 0
v_mfma_f32_16x16x32_bf16 v[40:43], v[32:35], a[124:127], v[40:43]
s_waitcnt vmcnt(44)
v_mfma_f32_16x16x32_bf16 v[28:31], v[16:19], a[128:131], 0
v_mfma_f32_16x16x32_bf16 v[32:35], v[16:19], a[136:139], 0
v_mfma_f32_16x16x32_bf16 v[28:31], v[20:23], a[132:135], v[28:31]
v_mfma_f32_16x16x32_bf16 v[32:35], v[20:23], a[140:143], v[32:35]
s_nop 6
v_add_f32_dpp v44, v29, v28 quad_perm:[1,1,3,3] row_mask:0x3 bank_mask:0xf
s_nop 1
v_add_f32_dpp v44, v33, v32 quad_perm:[1,1,3,3] row_mask:0xc bank_mask:0xf
v_fma_f32 v45, v44, s38, v3
v_cmp_lt_f32_e32 vcc, 0, v45
s_nop 0
s_and_b64 s[48:49], vcc, s[40:41]
s_cbranch_scc0 Lc174_%=
s_swappc_b64 s[42:43], s[46:47]
Lc174_%=:
v_exp_f32_e32 v45, v45
s_nop 0
v_cndmask_b32_e64 v45, 0, v45, s[40:41]
v_add_f32_e32 v47, v47, v45
v_cvt_pk_bf16_f32 v44, v45, v45
s_nop 1
v_mov_b32_dpp v44, v44 row_ror:8 row_mask:0xa bank_mask:0xf
s_waitcnt vmcnt(40)
s_nop 0
v_and_b32_dpp v28, v44, v4 row_newbcast:0 row_mask:0xf bank_mask:0xf
v_and_b32_dpp v29, v44, v5 row_newbcast:0 row_mask:0xf bank_mask:0xf
v_and_b32_dpp v30, v44, v6 row_newbcast:0 row_mask:0xf bank_mask:0xf
v_and_b32_dpp v31, v44, v7 row_newbcast:0 row_mask:0xf bank_mask:0xf
v_and_b32_dpp v32, v44, v4 row_newbcast:2 row_mask:0xf bank_mask:0xf
v_and_b32_dpp v33, v44, v5 row_newbcast:2 row_mask:0xf bank_mask:0xf
v_and_b32_dpp v34, v44, v6 row_newbcast:2 row_mask:0xf bank_mask:0xf
v_and_b32_dpp v35, v44, v7 row_newbcast:2 row_mask:0xf bank_mask:0xf
v_mfma_f32_16x16x32_bf16 v[40:43], v[28:31], a[144:147], v[40:43]
v_and_b32_dpp v28, v44, v4 row_newbcast:4 row_mask:0xf bank_mask:0xf
v_and_b32_dpp v29, v44, v5 row_newbcast:4 row_mask:0xf bank_mask:0xf
v_and_b32_dpp v30, v44, v6 row_newbcast:4 row_mask:0xf bank_mask:0xf
v_and_b32_dpp v31, v44, v7 row_newbcast:4 row_mask:0xf bank_mask:0xf
v_mfma_f32_16x16x32_bf16 v[40:43], v[32:35], a[148:151], v[40:43]
v_and_b32_dpp v32, v44, v4 row_newbcast:6 row_mask:0xf bank_mask:0xf
v_and_b32_dpp v33, v44, v5 row_newbcast:6 row_mask:0xf bank_mask:0xf
v_and_b32_dpp v34, v44, v6 row_newbcast:6 row_mask:0xf bank_mask:0xf
v_and_b32_dpp v35, v44, v7 row_newbcast:6 row_mask:0xf bank_mask:0xf
v_mfma_f32_16x16x32_bf16 v[40:43], v[28:31], a[152:155], v[40:43]
s_nop 0
v_mfma_f32_16x16x32_bf16 v[40:43], v[32:35], a[156:159], v[40:43]
s_waitcnt vmcnt(36)
v_mfma_f32_16x16x32_bf16 v[28:31], v[16:19], a[160:163], 0
v_mfma_f32_16x16x32_bf16 v[32:35], v[16:19], a[168:171], 0
v_mfma_f32_16x16x32_bf16 v[28:31], v[20:23], a[164:167], v[28:31]
v_mfma_f32_16x16x32_bf16 v[32:35], v[20:23], a[172:175], v[32:35]
s_nop 6
v_add_f32_dpp v44, v29, v28 quad_perm:[1,1,3,3] row_mask:0x3 bank_mask:0xf
s_nop 1
v_add_f32_dpp v44, v33, v32 quad_perm:[1,1,3,3] row_mask:0xc bank_mask:0xf
v_fma_f32 v45, v44, s38, v3
v_cmp_lt_f32_e32 vcc, 0, v45
s_nop 0
s_and_b64 s[48:49], vcc, s[40:41]
s_cbranch_scc0 Lc175_%=
s_swappc_b64 s[42:43], s[46:47]
Lc175_%=:
v_exp_f32_e32 v45, v45
s_nop 0
v_cndmask_b32_e64 v45, 0, v45, s[40:41]
v_add_f32_e32 v47, v47, v45
v_cvt_pk_bf16_f32 v44, v45, v45
s_nop 1
v_mov_b32_dpp v44, v44 row_ror:8 row_mask:0xa bank_mask:0xf
s_waitcnt vmcnt(32)
s_nop 0
v_and_b32_dpp v28, v44, v4 row_newbcast:0 row_mask:0xf bank_mask:0xf
v_and_b32_dpp v29, v44, v5 row_newbcast:0 row_mask:0xf bank_mask:0xf
v_and_b32_dpp v30, v44, v6 row_newbcast:0 row_mask:0xf bank_mask:0xf
v_and_b32_dpp v31, v44, v7 row_newbcast:0 row_mask:0xf bank_mask:0xf
v_and_b32_dpp v32, v44, v4 row_newbcast:2 row_mask:0xf bank_mask:0xf
v_and_b32_dpp v33, v44, v5 row_newbcast:2 row_mask:0xf bank_mask:0xf
v_and_b32_dpp v34, v44, v6 row_newbcast:2 row_mask:0xf bank_mask:0xf
v_and_b32_dpp v35, v44, v7 row_newbcast:2 row_mask:0xf bank_mask:0xf
v_mfma_f32_16x16x32_bf16 v[40:43], v[28:31], a[176:179], v[40:43]
v_and_b32_dpp v28, v44, v4 row_newbcast:4 row_mask:0xf bank_mask:0xf
v_and_b32_dpp v29, v44, v5 row_newbcast:4 row_mask:0xf bank_mask:0xf
v_and_b32_dpp v30, v44, v6 row_newbcast:4 row_mask:0xf bank_mask:0xf
v_and_b32_dpp v31, v44, v7 row_newbcast:4 row_mask:0xf bank_mask:0xf
v_mfma_f32_16x16x32_bf16 v[40:43], v[32:35], a[180:183], v[40:43]
v_and_b32_dpp v32, v44, v4 row_newbcast:6 row_mask:0xf bank_mask:0xf
v_and_b32_dpp v33, v44, v5 row_newbcast:6 row_mask:0xf bank_mask:0xf
v_and_b32_dpp v34, v44, v6 row_newbcast:6 row_mask:0xf bank_mask:0xf
v_and_b32_dpp v35, v44, v7 row_newbcast:6 row_mask:0xf bank_mask:0xf
v_mfma_f32_16x16x32_bf16 v[40:43], v[28:31], a[184:187], v[40:43]
s_nop 0
v_mfma_f32_16x16x32_bf16 v[40:43], v[32:35], a[188:191], v[40:43]
s_waitcnt vmcnt(28)
v_mfma_f32_16x16x32_bf16 v[28:31], v[16:19], a[192:195], 0
v_mfma_f32_16x16x32_bf16 v[32:35], v[16:19], a[200:203], 0
v_mfma_f32_16x16x32_bf16 v[28:31], v[20:23], a[196:199], v[28:31]
v_mfma_f32_16x16x32_bf16 v[32:35], v[20:23], a[204:207], v[32:35]
s_nop 6
v_add_f32_dpp v44, v29, v28 quad_perm:[1,1,3,3] row_mask:0x3 bank_mask:0xf
s_nop 1
v_add_f32_dpp v44, v33, v32 quad_perm:[1,1,3,3] row_mask:0xc bank_mask:0xf
v_fma_f32 v45, v44, s38, v3
v_cmp_lt_f32_e32 vcc, 0, v45
s_nop 0
s_and_b64 s[48:49], vcc, s[40:41]
s_cbranch_scc0 Lc176_%=
s_swappc_b64 s[42:43], s[46:47]
Lc176_%=:
v_exp_f32_e32 v45, v45
s_nop 0
v_cndmask_b32_e64 v45, 0, v45, s[40:41]
v_add_f32_e32 v47, v47, v45
v_cvt_pk_bf16_f32 v44, v45, v45
s_nop 1
v_mov_b32_dpp v44, v44 row_ror:8 row_mask:0xa bank_mask:0xf
s_waitcnt vmcnt(24)
s_nop 0
v_and_b32_dpp v28, v44, v4 row_newbcast:0 row_mask:0xf bank_mask:0xf
v_and_b32_dpp v29, v44, v5 row_newbcast:0 row_mask:0xf bank_mask:0xf
v_and_b32_dpp v30, v44, v6 row_newbcast:0 row_mask:0xf bank_mask:0xf
v_and_b32_dpp v31, v44, v7 row_newbcast:0 row_mask:0xf bank_mask:0xf
v_and_b32_dpp v32, v44, v4 row_newbcast:2 row_mask:0xf bank_mask:0xf
v_and_b32_dpp v33, v44, v5 row_newbcast:2 row_mask:0xf bank_mask:0xf
v_and_b32_dpp v34, v44, v6 row_newbcast:2 row_mask:0xf bank_mask:0xf
v_and_b32_dpp v35, v44, v7 row_newbcast:2 row_mask:0xf bank_mask:0xf
v_mfma_f32_16x16x32_bf16 v[40:43], v[28:31], a[208:211], v[40:43]
v_and_b32_dpp v28, v44, v4 row_newbcast:4 row_mask:0xf bank_mask:0xf
v_and_b32_dpp v29, v44, v5 row_newbcast:4 row_mask:0xf bank_mask:0xf
v_and_b32_dpp v30, v44, v6 row_newbcast:4 row_mask:0xf bank_mask:0xf
v_and_b32_dpp v31, v44, v7 row_newbcast:4 row_mask:0xf bank_mask:0xf
v_mfma_f32_16x16x32_bf16 v[40:43], v[32:35], a[212:215], v[40:43]
v_and_b32_dpp v32, v44, v4 row_newbcast:6 row_mask:0xf bank_mask:0xf
v_and_b32_dpp v33, v44, v5 row_newbcast:6 row_mask:0xf bank_mask:0xf
v_and_b32_dpp v34, v44, v6 row_newbcast:6 row_mask:0xf bank_mask:0xf
v_and_b32_dpp v35, v44, v7 row_newbcast:6 row_mask:0xf bank_mask:0xf
v_mfma_f32_16x16x32_bf16 v[40:43], v[28:31], a[216:219], v[40:43]
s_nop 0
v_mfma_f32_16x16x32_bf16 v[40:43], v[32:35], a[220:223], v[40:43]
s_waitcnt vmcnt(20)
v_mfma_f32_16x16x32_bf16 v[28:31], v[16:19], a[224:227], 0
v_mfma_f32_16x16x32_bf16 v[32:35], v[16:19], a[232:235], 0
v_mfma_f32_16x16x32_bf16 v[28:31], v[20:23], a[228:231], v[28:31]
v_mfma_f32_16x16x32_bf16 v[32:35], v[20:23], a[236:239], v[32:35]
s_nop 6
v_add_f32_dpp v44, v29, v28 quad_perm:[1,1,3,3] row_mask:0x3 bank_mask:0xf
s_nop 1
v_add_f32_dpp v44, v33, v32 quad_perm:[1,1,3,3] row_mask:0xc bank_mask:0xf
v_fma_f32 v45, v44, s38, v3
v_cmp_lt_f32_e32 vcc, 0, v45
s_nop 0
s_and_b64 s[48:49], vcc, s[40:41]
s_cbranch_scc0 Lc177_%=
s_swappc_b64 s[42:43], s[46:47]
Lc177_%=:
v_exp_f32_e32 v45, v45
s_nop 0
v_cndmask_b32_e64 v45, 0, v45, s[40:41]
v_add_f32_e32 v47, v47, v45
v_cvt_pk_bf16_f32 v44, v45, v45
s_nop 1
v_mov_b32_dpp v44, v44 row_ror:8 row_mask:0xa bank_mask:0xf
s_waitcnt vmcnt(16)
s_nop 0
v_and_b32_dpp v28, v44, v4 row_newbcast:0 row_mask:0xf bank_mask:0xf
v_and_b32_dpp v29, v44, v5 row_newbcast:0 row_mask:0xf bank_mask:0xf
v_and_b32_dpp v30, v44, v6 row_newbcast:0 row_mask:0xf bank_mask:0xf
v_and_b32_dpp v31, v44, v7 row_newbcast:0 row_mask:0xf bank_mask:0xf
v_and_b32_dpp v32, v44, v4 row_newbcast:2 row_mask:0xf bank_mask:0xf
v_and_b32_dpp v33, v44, v5 row_newbcast:2 row_mask:0xf bank_mask:0xf
v_and_b32_dpp v34, v44, v6 row_newbcast:2 row_mask:0xf bank_mask:0xf
v_and_b32_dpp v35, v44, v7 row_newbcast:2 row_mask:0xf bank_mask:0xf
v_mfma_f32_16x16x32_bf16 v[40:43], v[28:31], a[240:243], v[40:43]
v_and_b32_dpp v28, v44, v4 row_newbcast:4 row_mask:0xf bank_mask:0xf
v_and_b32_dpp v29, v44, v5 row_newbcast:4 row_mask:0xf bank_mask:0xf
v_and_b32_dpp v30, v44, v6 row_newbcast:4 row_mask:0xf bank_mask:0xf
v_and_b32_dpp v31, v44, v7 row_newbcast:4 row_mask:0xf bank_mask:0xf
v_mfma_f32_16x16x32_bf16 v[40:43], v[32:35], a[244:247], v[40:43]
v_and_b32_dpp v32, v44, v4 row_newbcast:6 row_mask:0xf bank_mask:0xf
v_and_b32_dpp v33, v44, v5 row_newbcast:6 row_mask:0xf bank_mask:0xf
v_and_b32_dpp v34, v44, v6 row_newbcast:6 row_mask:0xf bank_mask:0xf
v_and_b32_dpp v35, v44, v7 row_newbcast:6 row_mask:0xf bank_mask:0xf
v_mfma_f32_16x16x32_bf16 v[40:43], v[28:31], a[248:251], v[40:43]
s_nop 0
v_mfma_f32_16x16x32_bf16 v[40:43], v[32:35], a[252:255], v[40:43]
s_waitcnt vmcnt(12)
v_mfma_f32_16x16x32_bf16 v[28:31], v[16:19], v[96:99], 0
v_mfma_f32_16x16x32_bf16 v[32:35], v[16:19], v[104:107], 0
v_mfma_f32_16x16x32_bf16 v[28:31], v[20:23], v[100:103], v[28:31]
v_mfma_f32_16x16x32_bf16 v[32:35], v[20:23], v[108:111], v[32:35]
s_nop 6
v_add_f32_dpp v44, v29, v28 quad_perm:[1,1,3,3] row_mask:0x3 bank_mask:0xf
s_nop 1
v_add_f32_dpp v44, v33, v32 quad_perm:[1,1,3,3] row_mask:0xc bank_mask:0xf
v_fma_f32 v45, v44, s38, v3
v_cmp_lt_f32_e32 vcc, 0, v45
s_nop 0
s_and_b64 s[48:49], vcc, s[40:41]
s_cbranch_scc0 Lc178_%=
s_swappc_b64 s[42:43], s[46:47]
Lc178_%=:
v_exp_f32_e32 v45, v45
s_nop 0
v_cndmask_b32_e64 v45, 0, v45, s[40:41]
v_add_f32_e32 v47, v47, v45
v_cvt_pk_bf16_f32 v44, v45, v45
s_nop 1
v_mov_b32_dpp v44, v44 row_ror:8 row_mask:0xa bank_mask:0xf
s_waitcnt vmcnt(8)
s_nop 0
v_and_b32_dpp v28, v44, v4 row_newbcast:0 row_mask:0xf bank_mask:0xf
v_and_b32_dpp v29, v44, v5 row_newbcast:0 row_mask:0xf bank_mask:0xf
v_and_b32_dpp v30, v44, v6 row_newbcast:0 row_mask:0xf bank_mask:0xf
v_and_b32_dpp v31, v44, v7 row_newbcast:0 row_mask:0xf bank_mask:0xf
v_and_b32_dpp v32, v44, v4 row_newbcast:2 row_mask:0xf bank_mask:0xf
v_and_b32_dpp v33, v44, v5 row_newbcast:2 row_mask:0xf bank_mask:0xf
v_and_b32_dpp v34, v44, v6 row_newbcast:2 row_mask:0xf bank_mask:0xf
v_and_b32_dpp v35, v44, v7 row_newbcast:2 row_mask:0xf bank_mask:0xf
v_mfma_f32_16x16x32_bf16 v[40:43], v[28:31], v[112:115], v[40:43]
v_and_b32_dpp v28, v44, v4 row_newbcast:4 row_mask:0xf bank_mask:0xf
v_and_b32_dpp v29, v44, v5 row_newbcast:4 row_mask:0xf bank_mask:0xf
v_and_b32_dpp v30, v44, v6 row_newbcast:4 row_mask:0xf bank_mask:0xf
v_and_b32_dpp v31, v44, v7 row_newbcast:4 row_mask:0xf bank_mask:0xf
v_mfma_f32_16x16x32_bf16 v[40:43], v[32:35], v[116:119], v[40:43]
v_and_b32_dpp v32, v44, v4 row_newbcast:6 row_mask:0xf bank_mask:0xf
v_and_b32_dpp v33, v44, v5 row_newbcast:6 row_mask:0xf bank_mask:0xf
v_and_b32_dpp v34, v44, v6 row_newbcast:6 row_mask:0xf bank_mask:0xf
v_and_b32_dpp v35, v44, v7 row_newbcast:6 row_mask:0xf bank_mask:0xf
v_mfma_f32_16x16x32_bf16 v[40:43], v[28:31], v[120:123], v[40:43]
s_nop 0
v_mfma_f32_16x16x32_bf16 v[40:43], v[32:35], v[124:127], v[40:43]
v_mfma_f32_16x16x32_bf16 v[28:31], v[16:19], v[48:51], 0
v_mfma_f32_16x16x32_bf16 v[32:35], v[16:19], v[56:59], 0
v_mfma_f32_16x16x32_bf16 v[28:31], v[20:23], v[52:55], v[28:31]
v_mfma_f32_16x16x32_bf16 v[32:35], v[20:23], v[60:63], v[32:35]
v_mbcnt_lo_u32_b32 v45, -1, 0
v_mbcnt_hi_u32_b32 v45, -1, v45
v_lshlrev_b32_e32 v45, 4, v45
v_add_u32_e32 v45, s57, v45
ds_write_b128 v45, v[48:51] offset:8192
ds_write_b128 v45, v[52:55] offset:9216
ds_write_b128 v45, v[56:59] offset:10240
ds_write_b128 v45, v[60:63] offset:11264
s_waitcnt lgkmcnt(0)
s_add_u32 s58, s26, 1720320
s_addc_u32 s59, s27, 0
global_load_dwordx4 v[48:51], v1, s[58:59] offset:0 nt
global_load_dwordx4 v[52:55], v1, s[58:59] offset:256 nt
global_load_dwordx4 v[56:59], v1, s[58:59] offset:512 nt
global_load_dwordx4 v[60:63], v1, s[58:59] offset:768 nt
v_add_f32_dpp v44, v29, v28 quad_perm:[1,1,3,3] row_mask:0x3 bank_mask:0xf
s_nop 1
v_add_f32_dpp v44, v33, v32 quad_perm:[1,1,3,3] row_mask:0xc bank_mask:0xf
v_fma_f32 v45, v44, s38, v3
v_cmp_lt_f32_e32 vcc, 0, v45
s_nop 0
s_and_b64 s[48:49], vcc, s[40:41]
s_cbranch_scc0 Lc179_%=
s_swappc_b64 s[42:43], s[46:47]
Lc179_%=:
v_exp_f32_e32 v45, v45
s_nop 0
v_cndmask_b32_e64 v45, 0, v45, s[40:41]
v_add_f32_e32 v47, v47, v45
v_cvt_pk_bf16_f32 v44, v45, v45
s_nop 1
v_mov_b32_dpp v44, v44 row_ror:8 row_mask:0xa bank_mask:0xf
s_waitcnt vmcnt(8)
s_nop 0
v_and_b32_dpp v28, v44, v4 row_newbcast:0 row_mask:0xf bank_mask:0xf
v_and_b32_dpp v29, v44, v5 row_newbcast:0 row_mask:0xf bank_mask:0xf
v_and_b32_dpp v30, v44, v6 row_newbcast:0 row_mask:0xf bank_mask:0xf
v_and_b32_dpp v31, v44, v7 row_newbcast:0 row_mask:0xf bank_mask:0xf
v_and_b32_dpp v32, v44, v4 row_newbcast:2 row_mask:0xf bank_mask:0xf
v_and_b32_dpp v33, v44, v5 row_newbcast:2 row_mask:0xf bank_mask:0xf
v_and_b32_dpp v34, v44, v6 row_newbcast:2 row_mask:0xf bank_mask:0xf
v_and_b32_dpp v35, v44, v7 row_newbcast:2 row_mask:0xf bank_mask:0xf
v_mfma_f32_16x16x32_bf16 v[40:43], v[28:31], v[64:67], v[40:43]
v_and_b32_dpp v28, v44, v4 row_newbcast:4 row_mask:0xf bank_mask:0xf
v_and_b32_dpp v29, v44, v5 row_newbcast:4 row_mask:0xf bank_mask:0xf
v_and_b32_dpp v30, v44, v6 row_newbcast:4 row_mask:0xf bank_mask:0xf
v_and_b32_dpp v31, v44, v7 row_newbcast:4 row_mask:0xf bank_mask:0xf
v_mfma_f32_16x16x32_bf16 v[40:43], v[32:35], v[68:71], v[40:43]
v_and_b32_dpp v32, v44, v4 row_newbcast:6 row_mask:0xf bank_mask:0xf
v_and_b32_dpp v33, v44, v5 row_newbcast:6 row_mask:0xf bank_mask:0xf
v_and_b32_dpp v34, v44, v6 row_newbcast:6 row_mask:0xf bank_mask:0xf
v_and_b32_dpp v35, v44, v7 row_newbcast:6 row_mask:0xf bank_mask:0xf
v_mfma_f32_16x16x32_bf16 v[40:43], v[28:31], v[72:75], v[40:43]
s_nop 0
v_mfma_f32_16x16x32_bf16 v[40:43], v[32:35], v[76:79], v[40:43]
v_mbcnt_lo_u32_b32 v45, -1, 0
v_mbcnt_hi_u32_b32 v45, -1, v45
v_lshlrev_b32_e32 v45, 4, v45
v_add_u32_e32 v45, s57, v45
ds_write_b128 v45, v[64:67] offset:12288
ds_write_b128 v45, v[68:71] offset:13312
ds_write_b128 v45, v[72:75] offset:14336
ds_write_b128 v45, v[76:79] offset:15360
s_waitcnt lgkmcnt(0)
s_add_u32 s58, s24, 1835008
s_addc_u32 s59, s25, 0
Lp180_%=:
s_memrealtime s[70:71]
s_waitcnt lgkmcnt(0)
s_sub_u32 s72, s68, s70
s_cmp_gt_i32 s72, 0
s_cbranch_scc0 Lg181_%=
s_sleep 1
s_branch Lp180_%=
Lg181_%=:
s_add_u32 s68, s68, s69
global_load_dwordx4 v[64:67], v0, s[58:59] offset:0 nt
global_load_dwordx4 v[68:71], v0, s[58:59] offset:128 nt
global_load_dwordx4 v[72:75], v0, s[58:59] offset:2048 nt
global_load_dwordx4 v[76:79], v0, s[58:59] offset:2176 nt
s_waitcnt vmcnt(8)
v_mfma_f32_16x16x32_bf16 v[28:31], v[16:19], v[80:83], 0
v_mfma_f32_16x16x32_bf16 v[32:35], v[16:19], v[88:91], 0
v_mfma_f32_16x16x32_bf16 v[28:31], v[20:23], v[84:87], v[28:31]
v_mfma_f32_16x16x32_bf16 v[32:35], v[20:23], v[92:95], v[32:35]
v_mbcnt_lo_u32_b32 v45, -1, 0
v_mbcnt_hi_u32_b32 v45, -1, v45
v_lshlrev_b32_e32 v45, 4, v45
v_add_u32_e32 v45, s57, v45
ds_write_b128 v45, v[80:83] offset:16384
ds_write_b128 v45, v[84:87] offset:17408
ds_write_b128 v45, v[88:91] offset:18432
ds_write_b128 v45, v[92:95] offset:19456
s_waitcnt lgkmcnt(0)
s_add_u32 s58, s26, 1835008
s_addc_u32 s59, s27, 0
global_load_dwordx4 v[80:83], v1, s[58:59] offset:0 nt
global_load_dwordx4 v[84:87], v1, s[58:59] offset:256 nt
global_load_dwordx4 v[88:91], v1, s[58:59] offset:512 nt
global_load_dwordx4 v[92:95], v1, s[58:59] offset:768 nt
v_add_f32_dpp v44, v29, v28 quad_perm:[1,1,3,3] row_mask:0x3 bank_mask:0xf
s_nop 1
v_add_f32_dpp v44, v33, v32 quad_perm:[1,1,3,3] row_mask:0xc bank_mask:0xf
v_fma_f32 v45, v44, s38, v3
v_cmp_lt_f32_e32 vcc, 0, v45
s_nop 0
s_and_b64 s[48:49], vcc, s[40:41]
s_cbranch_scc0 Lc182_%=
s_swappc_b64 s[42:43], s[46:47]
Lc182_%=:
v_exp_f32_e32 v45, v45
s_nop 0
v_cndmask_b32_e64 v45, 0, v45, s[40:41]
v_add_f32_e32 v47, v47, v45
v_cvt_pk_bf16_f32 v44, v45, v45
s_nop 1
v_mov_b32_dpp v44, v44 row_ror:8 row_mask:0xa bank_mask:0xf
s_waitcnt vmcnt(8)
s_nop 0
v_and_b32_dpp v28, v44, v4 row_newbcast:0 row_mask:0xf bank_mask:0xf
v_and_b32_dpp v29, v44, v5 row_newbcast:0 row_mask:0xf bank_mask:0xf
v_and_b32_dpp v30, v44, v6 row_newbcast:0 row_mask:0xf bank_mask:0xf
v_and_b32_dpp v31, v44, v7 row_newbcast:0 row_mask:0xf bank_mask:0xf
v_and_b32_dpp v32, v44, v4 row_newbcast:2 row_mask:0xf bank_mask:0xf
v_and_b32_dpp v33, v44, v5 row_newbcast:2 row_mask:0xf bank_mask:0xf
v_and_b32_dpp v34, v44, v6 row_newbcast:2 row_mask:0xf bank_mask:0xf
v_and_b32_dpp v35, v44, v7 row_newbcast:2 row_mask:0xf bank_mask:0xf
v_mfma_f32_16x16x32_bf16 v[40:43], v[28:31], v[48:51], v[40:43]
v_and_b32_dpp v28, v44, v4 row_newbcast:4 row_mask:0xf bank_mask:0xf
v_and_b32_dpp v29, v44, v5 row_newbcast:4 row_mask:0xf bank_mask:0xf
v_and_b32_dpp v30, v44, v6 row_newbcast:4 row_mask:0xf bank_mask:0xf
v_and_b32_dpp v31, v44, v7 row_newbcast:4 row_mask:0xf bank_mask:0xf
v_mfma_f32_16x16x32_bf16 v[40:43], v[32:35], v[52:55], v[40:43]
v_and_b32_dpp v32, v44, v4 row_newbcast:6 row_mask:0xf bank_mask:0xf
v_and_b32_dpp v33, v44, v5 row_newbcast:6 row_mask:0xf bank_mask:0xf
v_and_b32_dpp v34, v44, v6 row_newbcast:6 row_mask:0xf bank_mask:0xf
v_and_b32_dpp v35, v44, v7 row_newbcast:6 row_mask:0xf bank_mask:0xf
v_mfma_f32_16x16x32_bf16 v[40:43], v[28:31], v[56:59], v[40:43]
s_nop 0
v_mfma_f32_16x16x32_bf16 v[40:43], v[32:35], v[60:63], v[40:43]
v_mbcnt_lo_u32_b32 v45, -1, 0
v_mbcnt_hi_u32_b32 v45, -1, v45
v_lshlrev_b32_e32 v45, 4, v45
v_add_u32_e32 v45, s57, v45
ds_write_b128 v45, v[48:51] offset:20480
ds_write_b128 v45, v[52:55] offset:21504
ds_write_b128 v45, v[56:59] offset:22528
ds_write_b128 v45, v[60:63] offset:23552
s_waitcnt lgkmcnt(0)
s_add_u32 s58, s24, 1949696
s_addc_u32 s59, s25, 0
Lp183_%=:
s_memrealtime s[70:71]
s_waitcnt lgkmcnt(0)
s_sub_u32 s72, s68, s70
s_cmp_gt_i32 s72, 0
s_cbranch_scc0 Lg184_%=
s_sleep 1
s_branch Lp183_%=
Lg184_%=:
s_add_u32 s68, s68, s69
global_load_dwordx4 v[48:51], v0, s[58:59] offset:0 nt
global_load_dwordx4 v[52:55], v0, s[58:59] offset:128 nt
global_load_dwordx4 v[56:59], v0, s[58:59] offset:2048 nt
global_load_dwordx4 v[60:63], v0, s[58:59] offset:2176 nt
s_waitcnt vmcnt(8)
v_mfma_f32_16x16x32_bf16 v[28:31], v[16:19], v[64:67], 0
v_mfma_f32_16x16x32_bf16 v[32:35], v[16:19], v[72:75], 0
v_mfma_f32_16x16x32_bf16 v[28:31], v[20:23], v[68:71], v[28:31]
v_mfma_f32_16x16x32_bf16 v[32:35], v[20:23], v[76:79], v[32:35]
v_mbcnt_lo_u32_b32 v45, -1, 0
v_mbcnt_hi_u32_b32 v45, -1, v45
v_lshlrev_b32_e32 v45, 4, v45
v_add_u32_e32 v45, s57, v45
ds_write_b128 v45, v[64:67] offset:24576
ds_write_b128 v45, v[68:71] offset:25600
ds_write_b128 v45, v[72:75] offset:26624
ds_write_b128 v45, v[76:79] offset:27648
s_waitcnt lgkmcnt(0)
s_add_u32 s58, s26, 1949696
s_addc_u32 s59, s27, 0
global_load_dwordx4 v[64:67], v1, s[58:59] offset:0 nt
global_load_dwordx4 v[68:71], v1, s[58:59] offset:256 nt
global_load_dwordx4 v[72:75], v1, s[58:59] offset:512 nt
global_load_dwordx4 v[76:79], v1, s[58:59] offset:768 nt
v_add_f32_dpp v44, v29, v28 quad_perm:[1,1,3,3] row_mask:0x3 bank_mask:0xf
s_nop 1
v_add_f32_dpp v44, v33, v32 quad_perm:[1,1,3,3] row_mask:0xc bank_mask:0xf
v_fma_f32 v45, v44, s38, v3
v_cmp_lt_f32_e32 vcc, 0, v45
s_nop 0
s_and_b64 s[48:49], vcc, s[40:41]
s_cbranch_scc0 Lc185_%=
s_swappc_b64 s[42:43], s[46:47]
Lc185_%=:
v_exp_f32_e32 v45, v45
s_nop 0
v_cndmask_b32_e64 v45, 0, v45, s[40:41]
v_add_f32_e32 v47, v47, v45
v_cvt_pk_bf16_f32 v44, v45, v45
s_nop 1
v_mov_b32_dpp v44, v44 row_ror:8 row_mask:0xa bank_mask:0xf
s_waitcnt vmcnt(8)
s_nop 0
v_and_b32_dpp v28, v44, v4 row_newbcast:0 row_mask:0xf bank_mask:0xf
v_and_b32_dpp v29, v44, v5 row_newbcast:0 row_mask:0xf bank_mask:0xf
v_and_b32_dpp v30, v44, v6 row_newbcast:0 row_mask:0xf bank_mask:0xf
v_and_b32_dpp v31, v44, v7 row_newbcast:0 row_mask:0xf bank_mask:0xf
v_and_b32_dpp v32, v44, v4 row_newbcast:2 row_mask:0xf bank_mask:0xf
v_and_b32_dpp v33, v44, v5 row_newbcast:2 row_mask:0xf bank_mask:0xf
v_and_b32_dpp v34, v44, v6 row_newbcast:2 row_mask:0xf bank_mask:0xf
v_and_b32_dpp v35, v44, v7 row_newbcast:2 row_mask:0xf bank_mask:0xf
v_mfma_f32_16x16x32_bf16 v[40:43], v[28:31], v[80:83], v[40:43]
v_and_b32_dpp v28, v44, v4 row_newbcast:4 row_mask:0xf bank_mask:0xf
v_and_b32_dpp v29, v44, v5 row_newbcast:4 row_mask:0xf bank_mask:0xf
v_and_b32_dpp v30, v44, v6 row_newbcast:4 row_mask:0xf bank_mask:0xf
v_and_b32_dpp v31, v44, v7 row_newbcast:4 row_mask:0xf bank_mask:0xf
v_mfma_f32_16x16x32_bf16 v[40:43], v[32:35], v[84:87], v[40:43]
v_and_b32_dpp v32, v44, v4 row_newbcast:6 row_mask:0xf bank_mask:0xf
v_and_b32_dpp v33, v44, v5 row_newbcast:6 row_mask:0xf bank_mask:0xf
v_and_b32_dpp v34, v44, v6 row_newbcast:6 row_mask:0xf bank_mask:0xf
v_and_b32_dpp v35, v44, v7 row_newbcast:6 row_mask:0xf bank_mask:0xf
v_mfma_f32_16x16x32_bf16 v[40:43], v[28:31], v[88:91], v[40:43]
s_nop 0
v_mfma_f32_16x16x32_bf16 v[40:43], v[32:35], v[92:95], v[40:43]
v_mbcnt_lo_u32_b32 v45, -1, 0
v_mbcnt_hi_u32_b32 v45, -1, v45
v_lshlrev_b32_e32 v45, 4, v45
v_add_u32_e32 v45, s57, v45
ds_write_b128 v45, v[80:83] offset:28672
ds_write_b128 v45, v[84:87] offset:29696
ds_write_b128 v45, v[88:91] offset:30720
ds_write_b128 v45, v[92:95] offset:31744
s_waitcnt lgkmcnt(0)
s_add_u32 s58, s20, 2064384
s_addc_u32 s59, s21, 0
global_load_dwordx4 v[80:83], v0, s[58:59] offset:0 nt
global_load_dwordx4 v[84:87], v0, s[58:59] offset:128 nt
global_load_dwordx4 v[88:91], v0, s[58:59] offset:2048 nt
global_load_dwordx4 v[92:95], v0, s[58:59] offset:2176 nt
s_waitcnt vmcnt(8)
v_mfma_f32_16x16x32_bf16 v[28:31], v[16:19], v[48:51], 0
v_mfma_f32_16x16x32_bf16 v[32:35], v[16:19], v[56:59], 0
v_mfma_f32_16x16x32_bf16 v[28:31], v[20:23], v[52:55], v[28:31]
v_mfma_f32_16x16x32_bf16 v[32:35], v[20:23], v[60:63], v[32:35]
v_mbcnt_lo_u32_b32 v45, -1, 0
v_mbcnt_hi_u32_b32 v45, -1, v45
v_lshlrev_b32_e32 v45, 4, v45
v_add_u32_e32 v45, s57, v45
ds_write_b128 v45, v[48:51] offset:32768
ds_write_b128 v45, v[52:55] offset:33792
ds_write_b128 v45, v[56:59] offset:34816
ds_write_b128 v45, v[60:63] offset:35840
s_waitcnt lgkmcnt(0)
s_add_u32 s58, s22, 2064384
s_addc_u32 s59, s23, 0
global_load_dwordx4 v[48:51], v1, s[58:59] offset:0 nt
global_load_dwordx4 v[52:55], v1, s[58:59] offset:256 nt
global_load_dwordx4 v[56:59], v1, s[58:59] offset:512 nt
global_load_dwordx4 v[60:63], v1, s[58:59] offset:768 nt
v_add_f32_dpp v44, v29, v28 quad_perm:[1,1,3,3] row_mask:0x3 bank_mask:0xf
s_nop 1
v_add_f32_dpp v44, v33, v32 quad_perm:[1,1,3,3] row_mask:0xc bank_mask:0xf
v_fma_f32 v45, v44, s38, v3
v_cmp_lt_f32_e32 vcc, 0, v45
s_nop 0
s_and_b64 s[48:49], vcc, s[40:41]
s_cbranch_scc0 Lc186_%=
s_swappc_b64 s[42:43], s[46:47]
Lc186_%=:
v_exp_f32_e32 v45, v45
s_nop 0
v_cndmask_b32_e64 v45, 0, v45, s[40:41]
v_add_f32_e32 v47, v47, v45
v_cvt_pk_bf16_f32 v44, v45, v45
s_nop 1
v_mov_b32_dpp v44, v44 row_ror:8 row_mask:0xa bank_mask:0xf
s_waitcnt vmcnt(8)
s_nop 0
v_and_b32_dpp v28, v44, v4 row_newbcast:0 row_mask:0xf bank_mask:0xf
v_and_b32_dpp v29, v44, v5 row_newbcast:0 row_mask:0xf bank_mask:0xf
v_and_b32_dpp v30, v44, v6 row_newbcast:0 row_mask:0xf bank_mask:0xf
v_and_b32_dpp v31, v44, v7 row_newbcast:0 row_mask:0xf bank_mask:0xf
v_and_b32_dpp v32, v44, v4 row_newbcast:2 row_mask:0xf bank_mask:0xf
v_and_b32_dpp v33, v44, v5 row_newbcast:2 row_mask:0xf bank_mask:0xf
v_and_b32_dpp v34, v44, v6 row_newbcast:2 row_mask:0xf bank_mask:0xf
v_and_b32_dpp v35, v44, v7 row_newbcast:2 row_mask:0xf bank_mask:0xf
v_mfma_f32_16x16x32_bf16 v[40:43], v[28:31], v[64:67], v[40:43]
v_and_b32_dpp v28, v44, v4 row_newbcast:4 row_mask:0xf bank_mask:0xf
v_and_b32_dpp v29, v44, v5 row_newbcast:4 row_mask:0xf bank_mask:0xf
v_and_b32_dpp v30, v44, v6 row_newbcast:4 row_mask:0xf bank_mask:0xf
v_and_b32_dpp v31, v44, v7 row_newbcast:4 row_mask:0xf bank_mask:0xf
v_mfma_f32_16x16x32_bf16 v[40:43], v[32:35], v[68:71], v[40:43]
v_and_b32_dpp v32, v44, v4 row_newbcast:6 row_mask:0xf bank_mask:0xf
v_and_b32_dpp v33, v44, v5 row_newbcast:6 row_mask:0xf bank_mask:0xf
v_and_b32_dpp v34, v44, v6 row_newbcast:6 row_mask:0xf bank_mask:0xf
v_and_b32_dpp v35, v44, v7 row_newbcast:6 row_mask:0xf bank_mask:0xf
v_mfma_f32_16x16x32_bf16 v[40:43], v[28:31], v[72:75], v[40:43]
s_nop 0
v_mfma_f32_16x16x32_bf16 v[40:43], v[32:35], v[76:79], v[40:43]
v_mbcnt_lo_u32_b32 v45, -1, 0
v_mbcnt_hi_u32_b32 v45, -1, v45
v_lshlrev_b32_e32 v45, 4, v45
v_add_u32_e32 v45, s57, v45
ds_write_b128 v45, v[64:67] offset:36864
ds_write_b128 v45, v[68:71] offset:37888
ds_write_b128 v45, v[72:75] offset:38912
v_mov_b32_e32 v24, v76
v_mov_b32_e32 v25, v77
v_mov_b32_e32 v26, v78
v_mov_b32_e32 v27, v79
s_waitcnt lgkmcnt(0)
s_add_u32 s58, s20, 2179072
s_addc_u32 s59, s21, 0
Lp187_%=:
s_memrealtime s[70:71]
s_waitcnt lgkmcnt(0)
s_sub_u32 s72, s68, s70
s_cmp_gt_i32 s72, 0
s_cbranch_scc0 Lg188_%=
s_sleep 1
s_branch Lp187_%=
Lg188_%=:
s_add_u32 s68, s68, s69
global_load_dwordx4 v[64:67], v0, s[58:59] offset:0 nt
global_load_dwordx4 v[68:71], v0, s[58:59] offset:128 nt
global_load_dwordx4 v[72:75], v0, s[58:59] offset:2048 nt
global_load_dwordx4 v[76:79], v0, s[58:59] offset:2176 nt
v_add_f32_dpp v29, v47, v47 row_ror:8 row_mask:0xf bank_mask:0xf
s_nop 1
v_add_f32_dpp v29, v29, v29 row_ror:4 row_mask:0xf bank_mask:0xf
s_nop 1
v_add_f32_dpp v29, v29, v29 row_ror:2 row_mask:0xf bank_mask:0xf
s_nop 1
v_add_f32_dpp v29, v29, v29 row_ror:1 row_mask:0xf bank_mask:0xf
s_nop 0
v_readlane_b32 s48, v29, 0
v_readlane_b32 s49, v29, 16
v_readlane_b32 s50, v29, 32
v_readlane_b32 s51, v29, 48
v_mov_b32_e32 v29, s48
v_add_f32_e32 v29, s49, v29
v_add_f32_e32 v29, s50, v29
v_add_f32_e32 v29, s51, v29
v_mov_b32_e32 v45, s74
s_nop 1
Ld189_%=:
ds_read_b32 v44, v45 offset:2116
s_waitcnt lgkmcnt(0)
v_readfirstlane_b32 s48, v44
s_nop 3
s_cmp_ge_u32 s48, 1
s_cbranch_scc1 Ldg190_%=
s_sub_u32 s56, s56, 1
s_cbranch_scc0 Ld189_%=
s_mov_b32 s56, 0
Ldg190_%=:
s_nop 1
v_mbcnt_lo_u32_b32 v45, -1, 0
v_mbcnt_hi_u32_b32 v45, -1, v45
v_lshlrev_b32_e32 v45, 4, v45
v_add_u32_e32 v45, s75, v45
v_sub_f32_e32 v28, 0, v3
s_sub_u32 s49, s75, s74
s_lshr_b32 s49, s49, 6
s_add_u32 s49, s49, s74
v_mov_b32_e32 v44, s49
s_mov_b32 exec_hi, 0
ds_write_b128 v45, v[40:43]
s_mov_b32 exec_lo, 1
ds_write_b64 v44, v[28:29] offset:2048
s_waitcnt lgkmcnt(0)
v_mov_b32_e32 v45, s74
v_mov_b32_e32 v44, 1
ds_add_rtn_u32 v44, v45, v44 offset:2112
s_waitcnt lgkmcnt(0)
v_readfirstlane_b32 s48, v44
s_mov_b64 exec, -1
s_and_b32 s48, s48, 3
s_cmp_eq_u32 s48, 3
s_nop 1
s_cbranch_scc0 Lnp191_%=
v_mbcnt_lo_u32_b32 v45, -1, 0
v_mbcnt_hi_u32_b32 v45, -1, v45
v_lshlrev_b32_e32 v45, 4, v45
v_add_u32_e32 v45, s74, v45
ds_read_b128 v[28:31], v45
ds_read_b128 v[16:19], v45 offset:512
ds_read_b128 v[20:23], v45 offset:1024
v_mbcnt_lo_u32_b32 v44, -1, 0
v_mbcnt_hi_u32_b32 v44, -1, v44
v_and_b32_e32 v44, 3, v44
v_lshlrev_b32_e32 v44, 3, v44
v_add_u32_e32 v44, s74, v44
ds_read_b64 v[32:33], v44 offset:2048
s_waitcnt lgkmcnt(0)
v_max_f32_dpp v34, v32, v32 quad_perm:[1,0,3,2] row_mask:0xf bank_mask:0xf
s_nop 1
v_max_f32_dpp v34, v34, v34 quad_perm:[2,3,0,1] row_mask:0xf bank_mask:0xf
v_sub_f32_e32 v44, v32, v34
v_exp_f32_e32 v44, v44
s_nop 0
v_mul_f32_e32 v35, v44, v33
s_nop 1
v_add_f32_dpp v35, v35, v35 quad_perm:[1,0,3,2] row_mask:0xf bank_mask:0xf
s_nop 1
v_add_f32_dpp v35, v35, v35 quad_perm:[2,3,0,1] row_mask:0xf bank_mask:0xf
v_mul_f32_dpp v40, v44, v28 quad_perm:[0,0,0,0] row_mask:0xf bank_mask:0xf
v_mul_f32_dpp v41, v44, v29 quad_perm:[0,0,0,0] row_mask:0xf bank_mask:0xf
v_mul_f32_dpp v42, v44, v30 quad_perm:[0,0,0,0] row_mask:0xf bank_mask:0xf
v_mul_f32_dpp v43, v44, v31 quad_perm:[0,0,0,0] row_mask:0xf bank_mask:0xf
ds_read_b128 v[28:31], v45 offset:1536
v_fmac_f32_dpp v40, v44, v16 quad_perm:[1,1,1,1] row_mask:0xf bank_mask:0xf
v_fmac_f32_dpp v41, v44, v17 quad_perm:[1,1,1,1] row_mask:0xf bank_mask:0xf
v_fmac_f32_dpp v42, v44, v18 quad_perm:[1,1,1,1] row_mask:0xf bank_mask:0xf
v_fmac_f32_dpp v43, v44, v19 quad_perm:[1,1,1,1] row_mask:0xf bank_mask:0xf
v_fmac_f32_dpp v40, v44, v20 quad_perm:[2,2,2,2] row_mask:0xf bank_mask:0xf
v_fmac_f32_dpp v41, v44, v21 quad_perm:[2,2,2,2] row_mask:0xf bank_mask:0xf
v_fmac_f32_dpp v42, v44, v22 quad_perm:[2,2,2,2] row_mask:0xf bank_mask:0xf
v_fmac_f32_dpp v43, v44, v23 quad_perm:[2,2,2,2] row_mask:0xf bank_mask:0xf
s_waitcnt lgkmcnt(0)
v_fmac_f32_dpp v40, v44, v28 quad_perm:[3,3,3,3] row_mask:0xf bank_mask:0xf
v_fmac_f32_dpp v41, v44, v29 quad_perm:[3,3,3,3] row_mask:0xf bank_mask:0xf
v_fmac_f32_dpp v42, v44, v30 quad_perm:[3,3,3,3] row_mask:0xf bank_mask:0xf
v_fmac_f32_dpp v43, v44, v31 quad_perm:[3,3,3,3] row_mask:0xf bank_mask:0xf
v_mov_b32_e32 v45, s74
v_mov_b32_e32 v44, 2
ds_write_b32 v45, v44 offset:2116
s_add_u32 s58, s66, 294912
s_addc_u32 s59, s67, 0
v_permlane32_swap_b32 v40, v42
v_permlane32_swap_b32 v41, v43
v_mov_b32_e32 v28, v40
v_mov_b32_e32 v29, s37
v_mov_b32_e32 v30, v41
v_mov_b32_e32 v31, s37
v_mov_b32_e32 v16, v34
v_mov_b32_e32 v17, s37
v_mov_b32_e32 v18, v35
v_mov_b32_e32 v19, s37
s_waitcnt lgkmcnt(0)
v_mbcnt_lo_u32_b32 v45, -1, 0
v_mbcnt_hi_u32_b32 v45, -1, v45
v_lshlrev_b32_e32 v45, 4, v45
global_store_dwordx4 v45, v[28:31], s[58:59] offset:0 sc1
s_mov_b64 exec, 0xff
global_store_dwordx4 v45, v[16:19], s[58:59] offset:1024 sc1
s_mov_b64 exec, -1
s_nop 3
Lnp191_%=:
s_nop 1
v_mov_b32_e32 v40, 0
v_mov_b32_e32 v41, 0
v_mov_b32_e32 v42, 0
v_mov_b32_e32 v43, 0
v_mov_b32_e32 v47, 0
v_mov_b32_e32 v3, 0x7f800000
s_add_u32 s30, s22, 2179072
s_addc_u32 s31, s23, 0
s_add_u32 s28, s20, 2293760
s_addc_u32 s29, s21, 0
s_waitcnt vmcnt(8)
v_mfma_f32_16x16x32_bf16 v[28:31], v[8:11], v[80:83], 0
v_mfma_f32_16x16x32_bf16 v[32:35], v[8:11], v[88:91], 0
v_mfma_f32_16x16x32_bf16 v[28:31], v[12:15], v[84:87], v[28:31]
v_mfma_f32_16x16x32_bf16 v[32:35], v[12:15], v[92:95], v[32:35]
global_load_dwordx4 v[80:83], v1, s[30:31] offset:0 nt
global_load_dwordx4 v[84:87], v1, s[30:31] offset:256 nt
global_load_dwordx4 v[88:91], v1, s[30:31] offset:512 nt
global_load_dwordx4 v[92:95], v1, s[30:31] offset:768 nt
s_add_u32 s30, s30, 114688
s_addc_u32 s31, s31, 0
s_nop 0
v_add_f32_dpp v44, v29, v28 quad_perm:[1,1,3,3] row_mask:0x3 bank_mask:0xf
s_nop 1
v_add_f32_dpp v44, v33, v32 quad_perm:[1,1,3,3] row_mask:0xc bank_mask:0xf
v_fma_f32 v45, v44, s38, v2
v_cmp_lt_f32_e32 vcc, 0, v45
s_nop 0
s_and_b64 s[48:49], vcc, s[40:41]
s_cbranch_scc0 Lc192_%=
s_swappc_b64 s[42:43], s[44:45]
Lc192_%=:
v_exp_f32_e32 v45, v45
s_nop 0
v_cndmask_b32_e64 v45, 0, v45, s[40:41]
v_add_f32_e32 v46, v46, v45
v_cvt_pk_bf16_f32 v44, v45, v45
s_nop 1
v_mov_b32_dpp v44, v44 row_ror:8 row_mask:0xa bank_mask:0xf
s_waitcnt vmcnt(8)
s_nop 0
v_and_b32_dpp v28, v44, v4 row_newbcast:0 row_mask:0xf bank_mask:0xf
v_and_b32_dpp v29, v44, v5 row_newbcast:0 row_mask:0xf bank_mask:0xf
v_and_b32_dpp v30, v44, v6 row_newbcast:0 row_mask:0xf bank_mask:0xf
v_and_b32_dpp v31, v44, v7 row_newbcast:0 row_mask:0xf bank_mask:0xf
v_and_b32_dpp v32, v44, v4 row_newbcast:2 row_mask:0xf bank_mask:0xf
v_and_b32_dpp v33, v44, v5 row_newbcast:2 row_mask:0xf bank_mask:0xf
v_and_b32_dpp v34, v44, v6 row_newbcast:2 row_mask:0xf bank_mask:0xf
v_and_b32_dpp v35, v44, v7 row_newbcast:2 row_mask:0xf bank_mask:0xf
v_mfma_f32_16x16x32_bf16 v[36:39], v[28:31], v[48:51], v[36:39]
v_and_b32_dpp v28, v44, v4 row_newbcast:4 row_mask:0xf bank_mask:0xf
v_and_b32_dpp v29, v44, v5 row_newbcast:4 row_mask:0xf bank_mask:0xf
v_and_b32_dpp v30, v44, v6 row_newbcast:4 row_mask:0xf bank_mask:0xf
v_and_b32_dpp v31, v44, v7 row_newbcast:4 row_mask:0xf bank_mask:0xf
v_mfma_f32_16x16x32_bf16 v[36:39], v[32:35], v[52:55], v[36:39]
v_and_b32_dpp v32, v44, v4 row_newbcast:6 row_mask:0xf bank_mask:0xf
v_and_b32_dpp v33, v44, v5 row_newbcast:6 row_mask:0xf bank_mask:0xf
v_and_b32_dpp v34, v44, v6 row_newbcast:6 row_mask:0xf bank_mask:0xf
v_and_b32_dpp v35, v44, v7 row_newbcast:6 row_mask:0xf bank_mask:0xf
v_mfma_f32_16x16x32_bf16 v[36:39], v[28:31], v[56:59], v[36:39]
s_nop 0
v_mfma_f32_16x16x32_bf16 v[36:39], v[32:35], v[60:63], v[36:39]
Lp193_%=:
s_memrealtime s[70:71]
s_waitcnt lgkmcnt(0)
s_sub_u32 s72, s68, s70
s_cmp_gt_i32 s72, 0
s_cbranch_scc0 Lg194_%=
s_sleep 1
s_branch Lp193_%=
Lg194_%=:
s_add_u32 s68, s68, s69
global_load_dwordx4 v[48:51], v0, s[28:29] offset:0 nt
global_load_dwordx4 v[52:55], v0, s[28:29] offset:128 nt
global_load_dwordx4 v[56:59], v0, s[28:29] offset:2048 nt
global_load_dwordx4 v[60:63], v0, s[28:29] offset:2176 nt
s_add_u32 s28, s28, 114688
s_addc_u32 s29, s29, 0
s_mov_b32 s39, 4
s_nop 3
Lloop195_%=:
s_waitcnt vmcnt(8)
v_mfma_f32_16x16x32_bf16 v[28:31], v[8:11], v[64:67], 0
v_mfma_f32_16x16x32_bf16 v[32:35], v[8:11], v[72:75], 0
v_mfma_f32_16x16x32_bf16 v[28:31], v[12:15], v[68:71], v[28:31]
v_mfma_f32_16x16x32_bf16 v[32:35], v[12:15], v[76:79], v[32:35]
global_load_dwordx4 v[64:67], v1, s[30:31] offset:0 nt
global_load_dwordx4 v[68:71], v1, s[30:31] offset:256 nt
global_load_dwordx4 v[72:75], v1, s[30:31] offset:512 nt
global_load_dwordx4 v[76:79], v1, s[30:31] offset:768 nt
s_add_u32 s30, s30, 114688
s_addc_u32 s31, s31, 0
s_nop 0
v_add_f32_dpp v44, v29, v28 quad_perm:[1,1,3,3] row_mask:0x3 bank_mask:0xf
s_nop 1
v_add_f32_dpp v44, v33, v32 quad_perm:[1,1,3,3] row_mask:0xc bank_mask:0xf
v_fma_f32 v45, v44, s38, v2
v_cmp_lt_f32_e32 vcc, 0, v45
s_nop 0
s_and_b64 s[48:49], vcc, s[40:41]
s_cbranch_scc0 Lc196_%=
s_swappc_b64 s[42:43], s[44:45]
Lc196_%=:
v_exp_f32_e32 v45, v45
s_nop 0
v_cndmask_b32_e64 v45, 0, v45, s[40:41]
v_add_f32_e32 v46, v46, v45
v_cvt_pk_bf16_f32 v44, v45, v45
s_nop 1
v_mov_b32_dpp v44, v44 row_ror:8 row_mask:0xa bank_mask:0xf
s_waitcnt vmcnt(8)
s_nop 0
v_and_b32_dpp v28, v44, v4 row_newbcast:0 row_mask:0xf bank_mask:0xf
v_and_b32_dpp v29, v44, v5 row_newbcast:0 row_mask:0xf bank_mask:0xf
v_and_b32_dpp v30, v44, v6 row_newbcast:0 row_mask:0xf bank_mask:0xf
v_and_b32_dpp v31, v44, v7 row_newbcast:0 row_mask:0xf bank_mask:0xf
v_and_b32_dpp v32, v44, v4 row_newbcast:2 row_mask:0xf bank_mask:0xf
v_and_b32_dpp v33, v44, v5 row_newbcast:2 row_mask:0xf bank_mask:0xf
v_and_b32_dpp v34, v44, v6 row_newbcast:2 row_mask:0xf bank_mask:0xf
v_and_b32_dpp v35, v44, v7 row_newbcast:2 row_mask:0xf bank_mask:0xf
v_mfma_f32_16x16x32_bf16 v[36:39], v[28:31], v[80:83], v[36:39]
v_and_b32_dpp v28, v44, v4 row_newbcast:4 row_mask:0xf bank_mask:0xf
v_and_b32_dpp v29, v44, v5 row_newbcast:4 row_mask:0xf bank_mask:0xf
v_and_b32_dpp v30, v44, v6 row_newbcast:4 row_mask:0xf bank_mask:0xf
v_and_b32_dpp v31, v44, v7 row_newbcast:4 row_mask:0xf bank_mask:0xf
v_mfma_f32_16x16x32_bf16 v[36:39], v[32:35], v[84:87], v[36:39]
v_and_b32_dpp v32, v44, v4 row_newbcast:6 row_mask:0xf bank_mask:0xf
v_and_b32_dpp v33, v44, v5 row_newbcast:6 row_mask:0xf bank_mask:0xf
v_and_b32_dpp v34, v44, v6 row_newbcast:6 row_mask:0xf bank_mask:0xf
v_and_b32_dpp v35, v44, v7 row_newbcast:6 row_mask:0xf bank_mask:0xf
v_mfma_f32_16x16x32_bf16 v[36:39], v[28:31], v[88:91], v[36:39]
s_nop 0
v_mfma_f32_16x16x32_bf16 v[36:39], v[32:35], v[92:95], v[36:39]
global_load_dwordx4 v[80:83], v0, s[28:29] offset:0 nt
global_load_dwordx4 v[84:87], v0, s[28:29] offset:128 nt
global_load_dwordx4 v[88:91], v0, s[28:29] offset:2048 nt
global_load_dwordx4 v[92:95], v0, s[28:29] offset:2176 nt
s_add_u32 s28, s28, 114688
s_addc_u32 s29, s29, 0
s_waitcnt vmcnt(8)
v_mfma_f32_16x16x32_bf16 v[28:31], v[8:11], v[48:51], 0
v_mfma_f32_16x16x32_bf16 v[32:35], v[8:11], v[56:59], 0
v_mfma_f32_16x16x32_bf16 v[28:31], v[12:15], v[52:55], v[28:31]
v_mfma_f32_16x16x32_bf16 v[32:35], v[12:15], v[60:63], v[32:35]
global_load_dwordx4 v[48:51], v1, s[30:31] offset:0 nt
global_load_dwordx4 v[52:55], v1, s[30:31] offset:256 nt
global_load_dwordx4 v[56:59], v1, s[30:31] offset:512 nt
global_load_dwordx4 v[60:63], v1, s[30:31] offset:768 nt
s_add_u32 s30, s30, 114688
s_addc_u32 s31, s31, 0
s_nop 0
v_add_f32_dpp v44, v29, v28 quad_perm:[1,1,3,3] row_mask:0x3 bank_mask:0xf
s_nop 1
v_add_f32_dpp v44, v33, v32 quad_perm:[1,1,3,3] row_mask:0xc bank_mask:0xf
v_fma_f32 v45, v44, s38, v2
v_cmp_lt_f32_e32 vcc, 0, v45
s_nop 0
s_and_b64 s[48:49], vcc, s[40:41]
s_cbranch_scc0 Lc197_%=
s_swappc_b64 s[42:43], s[44:45]
Lc197_%=:
v_exp_f32_e32 v45, v45
s_nop 0
v_cndmask_b32_e64 v45, 0, v45, s[40:41]
v_add_f32_e32 v46, v46, v45
v_cvt_pk_bf16_f32 v44, v45, v45
s_nop 1
v_mov_b32_dpp v44, v44 row_ror:8 row_mask:0xa bank_mask:0xf
s_waitcnt vmcnt(8)
s_nop 0
v_and_b32_dpp v28, v44, v4 row_newbcast:0 row_mask:0xf bank_mask:0xf
v_and_b32_dpp v29, v44, v5 row_newbcast:0 row_mask:0xf bank_mask:0xf
v_and_b32_dpp v30, v44, v6 row_newbcast:0 row_mask:0xf bank_mask:0xf
v_and_b32_dpp v31, v44, v7 row_newbcast:0 row_mask:0xf bank_mask:0xf
v_and_b32_dpp v32, v44, v4 row_newbcast:2 row_mask:0xf bank_mask:0xf
v_and_b32_dpp v33, v44, v5 row_newbcast:2 row_mask:0xf bank_mask:0xf
v_and_b32_dpp v34, v44, v6 row_newbcast:2 row_mask:0xf bank_mask:0xf
v_and_b32_dpp v35, v44, v7 row_newbcast:2 row_mask:0xf bank_mask:0xf
v_mfma_f32_16x16x32_bf16 v[36:39], v[28:31], v[64:67], v[36:39]
v_and_b32_dpp v28, v44, v4 row_newbcast:4 row_mask:0xf bank_mask:0xf
v_and_b32_dpp v29, v44, v5 row_newbcast:4 row_mask:0xf bank_mask:0xf
v_and_b32_dpp v30, v44, v6 row_newbcast:4 row_mask:0xf bank_mask:0xf
v_and_b32_dpp v31, v44, v7 row_newbcast:4 row_mask:0xf bank_mask:0xf
v_mfma_f32_16x16x32_bf16 v[36:39], v[32:35], v[68:71], v[36:39]
v_and_b32_dpp v32, v44, v4 row_newbcast:6 row_mask:0xf bank_mask:0xf
v_and_b32_dpp v33, v44, v5 row_newbcast:6 row_mask:0xf bank_mask:0xf
v_and_b32_dpp v34, v44, v6 row_newbcast:6 row_mask:0xf bank_mask:0xf
v_and_b32_dpp v35, v44, v7 row_newbcast:6 row_mask:0xf bank_mask:0xf
v_mfma_f32_16x16x32_bf16 v[36:39], v[28:31], v[72:75], v[36:39]
s_nop 0
v_mfma_f32_16x16x32_bf16 v[36:39], v[32:35], v[76:79], v[36:39]
Lp198_%=:
s_memrealtime s[70:71]
s_waitcnt lgkmcnt(0)
s_sub_u32 s72, s68, s70
s_cmp_gt_i32 s72, 0
s_cbranch_scc0 Lg199_%=
s_sleep 1
s_branch Lp198_%=
Lg199_%=:
s_add_u32 s68, s68, s69
global_load_dwordx4 v[64:67], v0, s[28:29] offset:0 nt
global_load_dwordx4 v[68:71], v0, s[28:29] offset:128 nt
global_load_dwordx4 v[72:75], v0, s[28:29] offset:2048 nt
global_load_dwordx4 v[76:79], v0, s[28:29] offset:2176 nt
s_add_u32 s28, s28, 114688
s_addc_u32 s29, s29, 0
s_waitcnt vmcnt(8)
v_mfma_f32_16x16x32_bf16 v[28:31], v[8:11], v[80:83], 0
v_mfma_f32_16x16x32_bf16 v[32:35], v[8:11], v[88:91], 0
v_mfma_f32_16x16x32_bf16 v[28:31], v[12:15], v[84:87], v[28:31]
v_mfma_f32_16x16x32_bf16 v[32:35], v[12:15], v[92:95], v[32:35]
global_load_dwordx4 v[80:83], v1, s[30:31] offset:0 nt
global_load_dwordx4 v[84:87], v1, s[30:31] offset:256 nt
global_load_dwordx4 v[88:91], v1, s[30:31] offset:512 nt
global_load_dwordx4 v[92:95], v1, s[30:31] offset:768 nt
s_add_u32 s30, s30, 114688
s_addc_u32 s31, s31, 0
s_nop 0
v_add_f32_dpp v44, v29, v28 quad_perm:[1,1,3,3] row_mask:0x3 bank_mask:0xf
s_nop 1
v_add_f32_dpp v44, v33, v32 quad_perm:[1,1,3,3] row_mask:0xc bank_mask:0xf
v_fma_f32 v45, v44, s38, v2
v_cmp_lt_f32_e32 vcc, 0, v45
s_nop 0
s_and_b64 s[48:49], vcc, s[40:41]
s_cbranch_scc0 Lc200_%=
s_swappc_b64 s[42:43], s[44:45]
Lc200_%=:
v_exp_f32_e32 v45, v45
s_nop 0
v_cndmask_b32_e64 v45, 0, v45, s[40:41]
v_add_f32_e32 v46, v46, v45
v_cvt_pk_bf16_f32 v44, v45, v45
s_nop 1
v_mov_b32_dpp v44, v44 row_ror:8 row_mask:0xa bank_mask:0xf
s_waitcnt vmcnt(8)
s_nop 0
v_and_b32_dpp v28, v44, v4 row_newbcast:0 row_mask:0xf bank_mask:0xf
v_and_b32_dpp v29, v44, v5 row_newbcast:0 row_mask:0xf bank_mask:0xf
v_and_b32_dpp v30, v44, v6 row_newbcast:0 row_mask:0xf bank_mask:0xf
v_and_b32_dpp v31, v44, v7 row_newbcast:0 row_mask:0xf bank_mask:0xf
v_and_b32_dpp v32, v44, v4 row_newbcast:2 row_mask:0xf bank_mask:0xf
v_and_b32_dpp v33, v44, v5 row_newbcast:2 row_mask:0xf bank_mask:0xf
v_and_b32_dpp v34, v44, v6 row_newbcast:2 row_mask:0xf bank_mask:0xf
v_and_b32_dpp v35, v44, v7 row_newbcast:2 row_mask:0xf bank_mask:0xf
v_mfma_f32_16x16x32_bf16 v[36:39], v[28:31], v[48:51], v[36:39]
v_and_b32_dpp v28, v44, v4 row_newbcast:4 row_mask:0xf bank_mask:0xf
v_and_b32_dpp v29, v44, v5 row_newbcast:4 row_mask:0xf bank_mask:0xf
v_and_b32_dpp v30, v44, v6 row_newbcast:4 row_mask:0xf bank_mask:0xf
v_and_b32_dpp v31, v44, v7 row_newbcast:4 row_mask:0xf bank_mask:0xf
v_mfma_f32_16x16x32_bf16 v[36:39], v[32:35], v[52:55], v[36:39]
v_and_b32_dpp v32, v44, v4 row_newbcast:6 row_mask:0xf bank_mask:0xf
v_and_b32_dpp v33, v44, v5 row_newbcast:6 row_mask:0xf bank_mask:0xf
v_and_b32_dpp v34, v44, v6 row_newbcast:6 row_mask:0xf bank_mask:0xf
v_and_b32_dpp v35, v44, v7 row_newbcast:6 row_mask:0xf bank_mask:0xf
v_mfma_f32_16x16x32_bf16 v[36:39], v[28:31], v[56:59], v[36:39]
s_nop 0
v_mfma_f32_16x16x32_bf16 v[36:39], v[32:35], v[60:63], v[36:39]
Lp201_%=:
s_memrealtime s[70:71]
s_waitcnt lgkmcnt(0)
s_sub_u32 s72, s68, s70
s_cmp_gt_i32 s72, 0
s_cbranch_scc0 Lg202_%=
s_sleep 1
s_branch Lp201_%=
Lg202_%=:
s_add_u32 s68, s68, s69
global_load_dwordx4 v[48:51], v0, s[28:29] offset:0 nt
global_load_dwordx4 v[52:55], v0, s[28:29] offset:128 nt
global_load_dwordx4 v[56:59], v0, s[28:29] offset:2048 nt
global_load_dwordx4 v[60:63], v0, s[28:29] offset:2176 nt
s_add_u32 s28, s28, 114688
s_addc_u32 s29, s29, 0
s_sub_u32 s39, s39, 1
s_cmp_lg_u32 s39, 0
s_nop 3
s_cbranch_scc1 Lloop195_%=
s_waitcnt vmcnt(8)
v_mfma_f32_16x16x32_bf16 v[28:31], v[8:11], v[64:67], 0
v_mfma_f32_16x16x32_bf16 v[32:35], v[8:11], v[72:75], 0
v_mfma_f32_16x16x32_bf16 v[28:31], v[12:15], v[68:71], v[28:31]
v_mfma_f32_16x16x32_bf16 v[32:35], v[12:15], v[76:79], v[32:35]
s_add_u32 s58, s22, 3670016
s_addc_u32 s59, s23, 0
global_load_dwordx4 v[64:67], v1, s[58:59] offset:0 nt
global_load_dwordx4 v[68:71], v1, s[58:59] offset:256 nt
global_load_dwordx4 v[72:75], v1, s[58:59] offset:512 nt
global_load_dwordx4 v[76:79], v1, s[58:59] offset:768 nt
s_nop 0
v_add_f32_dpp v44, v29, v28 quad_perm:[1,1,3,3] row_mask:0x3 bank_mask:0xf
s_nop 1
v_add_f32_dpp v44, v33, v32 quad_perm:[1,1,3,3] row_mask:0xc bank_mask:0xf
v_fma_f32 v45, v44, s38, v2
v_cmp_lt_f32_e32 vcc, 0, v45
s_nop 0
s_and_b64 s[48:49], vcc, s[40:41]
s_cbranch_scc0 Lc203_%=
s_swappc_b64 s[42:43], s[44:45]
Lc203_%=:
v_exp_f32_e32 v45, v45
s_nop 0
v_cndmask_b32_e64 v45, 0, v45, s[40:41]
v_add_f32_e32 v46, v46, v45
v_cvt_pk_bf16_f32 v44, v45, v45
s_nop 1
v_mov_b32_dpp v44, v44 row_ror:8 row_mask:0xa bank_mask:0xf
s_waitcnt vmcnt(8)
s_nop 0
v_and_b32_dpp v28, v44, v4 row_newbcast:0 row_mask:0xf bank_mask:0xf
v_and_b32_dpp v29, v44, v5 row_newbcast:0 row_mask:0xf bank_mask:0xf
v_and_b32_dpp v30, v44, v6 row_newbcast:0 row_mask:0xf bank_mask:0xf
v_and_b32_dpp v31, v44, v7 row_newbcast:0 row_mask:0xf bank_mask:0xf
v_and_b32_dpp v32, v44, v4 row_newbcast:2 row_mask:0xf bank_mask:0xf
v_and_b32_dpp v33, v44, v5 row_newbcast:2 row_mask:0xf bank_mask:0xf
v_and_b32_dpp v34, v44, v6 row_newbcast:2 row_mask:0xf bank_mask:0xf
v_and_b32_dpp v35, v44, v7 row_newbcast:2 row_mask:0xf bank_mask:0xf
v_mfma_f32_16x16x32_bf16 v[36:39], v[28:31], v[80:83], v[36:39]
v_and_b32_dpp v28, v44, v4 row_newbcast:4 row_mask:0xf bank_mask:0xf
v_and_b32_dpp v29, v44, v5 row_newbcast:4 row_mask:0xf bank_mask:0xf
v_and_b32_dpp v30, v44, v6 row_newbcast:4 row_mask:0xf bank_mask:0xf
v_and_b32_dpp v31, v44, v7 row_newbcast:4 row_mask:0xf bank_mask:0xf
v_mfma_f32_16x16x32_bf16 v[36:39], v[32:35], v[84:87], v[36:39]
v_and_b32_dpp v32, v44, v4 row_newbcast:6 row_mask:0xf bank_mask:0xf
v_and_b32_dpp v33, v44, v5 row_newbcast:6 row_mask:0xf bank_mask:0xf
v_and_b32_dpp v34, v44, v6 row_newbcast:6 row_mask:0xf bank_mask:0xf
v_and_b32_dpp v35, v44, v7 row_newbcast:6 row_mask:0xf bank_mask:0xf
v_mfma_f32_16x16x32_bf16 v[36:39], v[28:31], v[88:91], v[36:39]
s_nop 0
v_mfma_f32_16x16x32_bf16 v[36:39], v[32:35], v[92:95], v[36:39]
s_add_u32 s58, s20, 3784704
s_addc_u32 s59, s21, 0
global_load_dwordx4 v[80:83], v0, s[58:59] offset:0 nt
global_load_dwordx4 v[84:87], v0, s[58:59] offset:128 nt
global_load_dwordx4 v[88:91], v0, s[58:59] offset:2048 nt
global_load_dwordx4 v[92:95], v0, s[58:59] offset:2176 nt
s_waitcnt vmcnt(8)
v_mfma_f32_16x16x32_bf16 v[28:31], v[8:11], v[48:51], 0
v_mfma_f32_16x16x32_bf16 v[32:35], v[8:11], v[56:59], 0
v_mfma_f32_16x16x32_bf16 v[28:31], v[12:15], v[52:55], v[28:31]
v_mfma_f32_16x16x32_bf16 v[32:35], v[12:15], v[60:63], v[32:35]
s_add_u32 s58, s22, 3784704
s_addc_u32 s59, s23, 0
global_load_dwordx4 v[48:51], v1, s[58:59] offset:0 nt
global_load_dwordx4 v[52:55], v1, s[58:59] offset:256 nt
global_load_dwordx4 v[56:59], v1, s[58:59] offset:512 nt
global_load_dwordx4 v[60:63], v1, s[58:59] offset:768 nt
s_nop 0
v_add_f32_dpp v44, v29, v28 quad_perm:[1,1,3,3] row_mask:0x3 bank_mask:0xf
s_nop 1
v_add_f32_dpp v44, v33, v32 quad_perm:[1,1,3,3] row_mask:0xc bank_mask:0xf
v_fma_f32 v45, v44, s38, v2
v_cmp_lt_f32_e32 vcc, 0, v45
s_nop 0
s_and_b64 s[48:49], vcc, s[40:41]
s_cbranch_scc0 Lc204_%=
s_swappc_b64 s[42:43], s[44:45]
Lc204_%=:
v_exp_f32_e32 v45, v45
s_nop 0
v_cndmask_b32_e64 v45, 0, v45, s[40:41]
v_add_f32_e32 v46, v46, v45
v_cvt_pk_bf16_f32 v44, v45, v45
s_nop 1
v_mov_b32_dpp v44, v44 row_ror:8 row_mask:0xa bank_mask:0xf
s_waitcnt vmcnt(8)
s_nop 0
v_and_b32_dpp v28, v44, v4 row_newbcast:0 row_mask:0xf bank_mask:0xf
v_and_b32_dpp v29, v44, v5 row_newbcast:0 row_mask:0xf bank_mask:0xf
v_and_b32_dpp v30, v44, v6 row_newbcast:0 row_mask:0xf bank_mask:0xf
v_and_b32_dpp v31, v44, v7 row_newbcast:0 row_mask:0xf bank_mask:0xf
v_and_b32_dpp v32, v44, v4 row_newbcast:2 row_mask:0xf bank_mask:0xf
v_and_b32_dpp v33, v44, v5 row_newbcast:2 row_mask:0xf bank_mask:0xf
v_and_b32_dpp v34, v44, v6 row_newbcast:2 row_mask:0xf bank_mask:0xf
v_and_b32_dpp v35, v44, v7 row_newbcast:2 row_mask:0xf bank_mask:0xf
v_mfma_f32_16x16x32_bf16 v[36:39], v[28:31], v[64:67], v[36:39]
v_and_b32_dpp v28, v44, v4 row_newbcast:4 row_mask:0xf bank_mask:0xf
v_and_b32_dpp v29, v44, v5 row_newbcast:4 row_mask:0xf bank_mask:0xf
v_and_b32_dpp v30, v44, v6 row_newbcast:4 row_mask:0xf bank_mask:0xf
v_and_b32_dpp v31, v44, v7 row_newbcast:4 row_mask:0xf bank_mask:0xf
v_mfma_f32_16x16x32_bf16 v[36:39], v[32:35], v[68:71], v[36:39]
v_and_b32_dpp v32, v44, v4 row_newbcast:6 row_mask:0xf bank_mask:0xf
v_and_b32_dpp v33, v44, v5 row_newbcast:6 row_mask:0xf bank_mask:0xf
v_and_b32_dpp v34, v44, v6 row_newbcast:6 row_mask:0xf bank_mask:0xf
v_and_b32_dpp v35, v44, v7 row_newbcast:6 row_mask:0xf bank_mask:0xf
v_mfma_f32_16x16x32_bf16 v[36:39], v[28:31], v[72:75], v[36:39]
s_nop 0
v_mfma_f32_16x16x32_bf16 v[36:39], v[32:35], v[76:79], v[36:39]
s_add_u32 s58, s20, 3899392
s_addc_u32 s59, s21, 0
Lp205_%=:
s_memrealtime s[70:71]
s_waitcnt lgkmcnt(0)
s_sub_u32 s72, s68, s70
s_cmp_gt_i32 s72, 0
s_cbranch_scc0 Lg206_%=
s_sleep 1
s_branch Lp205_%=
Lg206_%=:
s_add_u32 s68, s68, s69
global_load_dwordx4 v[64:67], v0, s[58:59] offset:0 nt
global_load_dwordx4 v[68:71], v0, s[58:59] offset:128 nt
global_load_dwordx4 v[72:75], v0, s[58:59] offset:2048 nt
global_load_dwordx4 v[76:79], v0, s[58:59] offset:2176 nt
s_waitcnt vmcnt(8)
v_mfma_f32_16x16x32_bf16 v[28:31], v[8:11], v[80:83], 0
v_mfma_f32_16x16x32_bf16 v[32:35], v[8:11], v[88:91], 0
v_mfma_f32_16x16x32_bf16 v[28:31], v[12:15], v[84:87], v[28:31]
v_mfma_f32_16x16x32_bf16 v[32:35], v[12:15], v[92:95], v[32:35]
s_add_u32 s58, s22, 3899392
s_addc_u32 s59, s23, 0
global_load_dwordx4 v[80:83], v1, s[58:59] offset:0 nt
global_load_dwordx4 v[84:87], v1, s[58:59] offset:256 nt
global_load_dwordx4 v[88:91], v1, s[58:59] offset:512 nt
global_load_dwordx4 v[92:95], v1, s[58:59] offset:768 nt
s_nop 0
v_add_f32_dpp v44, v29, v28 quad_perm:[1,1,3,3] row_mask:0x3 bank_mask:0xf
s_nop 1
v_add_f32_dpp v44, v33, v32 quad_perm:[1,1,3,3] row_mask:0xc bank_mask:0xf
v_fma_f32 v45, v44, s38, v2
v_cmp_lt_f32_e32 vcc, 0, v45
s_nop 0
s_and_b64 s[48:49], vcc, s[40:41]
s_cbranch_scc0 Lc207_%=
s_swappc_b64 s[42:43], s[44:45]
Lc207_%=:
v_exp_f32_e32 v45, v45
s_nop 0
v_cndmask_b32_e64 v45, 0, v45, s[40:41]
v_add_f32_e32 v46, v46, v45
v_cvt_pk_bf16_f32 v44, v45, v45
s_nop 1
v_mov_b32_dpp v44, v44 row_ror:8 row_mask:0xa bank_mask:0xf
s_waitcnt vmcnt(8)
s_nop 0
v_and_b32_dpp v28, v44, v4 row_newbcast:0 row_mask:0xf bank_mask:0xf
v_and_b32_dpp v29, v44, v5 row_newbcast:0 row_mask:0xf bank_mask:0xf
v_and_b32_dpp v30, v44, v6 row_newbcast:0 row_mask:0xf bank_mask:0xf
v_and_b32_dpp v31, v44, v7 row_newbcast:0 row_mask:0xf bank_mask:0xf
v_and_b32_dpp v32, v44, v4 row_newbcast:2 row_mask:0xf bank_mask:0xf
v_and_b32_dpp v33, v44, v5 row_newbcast:2 row_mask:0xf bank_mask:0xf
v_and_b32_dpp v34, v44, v6 row_newbcast:2 row_mask:0xf bank_mask:0xf
v_and_b32_dpp v35, v44, v7 row_newbcast:2 row_mask:0xf bank_mask:0xf
v_mfma_f32_16x16x32_bf16 v[36:39], v[28:31], v[48:51], v[36:39]
v_and_b32_dpp v28, v44, v4 row_newbcast:4 row_mask:0xf bank_mask:0xf
v_and_b32_dpp v29, v44, v5 row_newbcast:4 row_mask:0xf bank_mask:0xf
v_and_b32_dpp v30, v44, v6 row_newbcast:4 row_mask:0xf bank_mask:0xf
v_and_b32_dpp v31, v44, v7 row_newbcast:4 row_mask:0xf bank_mask:0xf
v_mfma_f32_16x16x32_bf16 v[36:39], v[32:35], v[52:55], v[36:39]
v_and_b32_dpp v32, v44, v4 row_newbcast:6 row_mask:0xf bank_mask:0xf
v_and_b32_dpp v33, v44, v5 row_newbcast:6 row_mask:0xf bank_mask:0xf
v_and_b32_dpp v34, v44, v6 row_newbcast:6 row_mask:0xf bank_mask:0xf
v_and_b32_dpp v35, v44, v7 row_newbcast:6 row_mask:0xf bank_mask:0xf
v_mfma_f32_16x16x32_bf16 v[36:39], v[28:31], v[56:59], v[36:39]
s_nop 0
v_mfma_f32_16x16x32_bf16 v[36:39], v[32:35], v[60:63], v[36:39]
s_add_u32 s58, s20, 4014080
s_addc_u32 s59, s21, 0
Lp208_%=:
s_memrealtime s[70:71]
s_waitcnt lgkmcnt(0)
s_sub_u32 s72, s68, s70
s_cmp_gt_i32 s72, 0
s_cbranch_scc0 Lg209_%=
s_sleep 1
s_branch Lp208_%=
Lg209_%=:
s_add_u32 s68, s68, s69
global_load_dwordx4 v[48:51], v0, s[58:59] offset:0 nt
global_load_dwordx4 v[52:55], v0, s[58:59] offset:128 nt
global_load_dwordx4 v[56:59], v0, s[58:59] offset:2048 nt
global_load_dwordx4 v[60:63], v0, s[58:59] offset:2176 nt
s_add_u32 s58, s64, 49152
s_addc_u32 s59, s65, 0
v_mbcnt_lo_u32_b32 v18, -1, 0
v_mbcnt_hi_u32_b32 v18, -1, v18
v_lshlrev_b32_e32 v18, 3, v18
global_load_dwordx2 v[16:17], v18, s[58:59] offset:0 sc1
s_waitcnt vmcnt(9)
v_mfma_f32_16x16x32_bf16 v[28:31], v[8:11], v[64:67], 0
v_mfma_f32_16x16x32_bf16 v[32:35], v[8:11], v[72:75], 0
v_mfma_f32_16x16x32_bf16 v[28:31], v[12:15], v[68:71], v[28:31]
v_mfma_f32_16x16x32_bf16 v[32:35], v[12:15], v[76:79], v[32:35]
s_add_u32 s58, s22, 4014080
s_addc_u32 s59, s23, 0
global_load_dwordx4 v[64:67], v1, s[58:59] offset:0 nt
global_load_dwordx4 v[68:71], v1, s[58:59] offset:256 nt
global_load_dwordx4 v[72:75], v1, s[58:59] offset:512 nt
global_load_dwordx4 v[76:79], v1, s[58:59] offset:768 nt
s_nop 0
v_add_f32_dpp v44, v29, v28 quad_perm:[1,1,3,3] row_mask:0x3 bank_mask:0xf
s_nop 1
v_add_f32_dpp v44, v33, v32 quad_perm:[1,1,3,3] row_mask:0xc bank_mask:0xf
v_fma_f32 v45, v44, s38, v2
v_cmp_lt_f32_e32 vcc, 0, v45
s_nop 0
s_and_b64 s[48:49], vcc, s[40:41]
s_cbranch_scc0 Lc210_%=
s_swappc_b64 s[42:43], s[44:45]
Lc210_%=:
v_exp_f32_e32 v45, v45
s_nop 0
v_cndmask_b32_e64 v45, 0, v45, s[40:41]
v_add_f32_e32 v46, v46, v45
v_cvt_pk_bf16_f32 v44, v45, v45
s_nop 1
v_mov_b32_dpp v44, v44 row_ror:8 row_mask:0xa bank_mask:0xf
s_waitcnt vmcnt(9)
s_nop 0
v_and_b32_dpp v28, v44, v4 row_newbcast:0 row_mask:0xf bank_mask:0xf
v_and_b32_dpp v29, v44, v5 row_newbcast:0 row_mask:0xf bank_mask:0xf
v_and_b32_dpp v30, v44, v6 row_newbcast:0 row_mask:0xf bank_mask:0xf
v_and_b32_dpp v31, v44, v7 row_newbcast:0 row_mask:0xf bank_mask:0xf
v_and_b32_dpp v32, v44, v4 row_newbcast:2 row_mask:0xf bank_mask:0xf
v_and_b32_dpp v33, v44, v5 row_newbcast:2 row_mask:0xf bank_mask:0xf
v_and_b32_dpp v34, v44, v6 row_newbcast:2 row_mask:0xf bank_mask:0xf
v_and_b32_dpp v35, v44, v7 row_newbcast:2 row_mask:0xf bank_mask:0xf
v_mfma_f32_16x16x32_bf16 v[36:39], v[28:31], v[80:83], v[36:39]
v_and_b32_dpp v28, v44, v4 row_newbcast:4 row_mask:0xf bank_mask:0xf
v_and_b32_dpp v29, v44, v5 row_newbcast:4 row_mask:0xf bank_mask:0xf
v_and_b32_dpp v30, v44, v6 row_newbcast:4 row_mask:0xf bank_mask:0xf
v_and_b32_dpp v31, v44, v7 row_newbcast:4 row_mask:0xf bank_mask:0xf
v_mfma_f32_16x16x32_bf16 v[36:39], v[32:35], v[84:87], v[36:39]
v_and_b32_dpp v32, v44, v4 row_newbcast:6 row_mask:0xf bank_mask:0xf
v_and_b32_dpp v33, v44, v5 row_newbcast:6 row_mask:0xf bank_mask:0xf
v_and_b32_dpp v34, v44, v6 row_newbcast:6 row_mask:0xf bank_mask:0xf
v_and_b32_dpp v35, v44, v7 row_newbcast:6 row_mask:0xf bank_mask:0xf
v_mfma_f32_16x16x32_bf16 v[36:39], v[28:31], v[88:91], v[36:39]
s_nop 0
v_mfma_f32_16x16x32_bf16 v[36:39], v[32:35], v[92:95], v[36:39]
s_add_u32 s58, s24, 2064384
s_addc_u32 s59, s25, 0
global_load_dwordx4 v[80:83], v0, s[58:59] offset:0 nt
global_load_dwordx4 v[84:87], v0, s[58:59] offset:128 nt
global_load_dwordx4 v[88:91], v0, s[58:59] offset:2048 nt
global_load_dwordx4 v[92:95], v0, s[58:59] offset:2176 nt
s_waitcnt vmcnt(9)
v_mfma_f32_16x16x32_bf16 v[28:31], v[8:11], v[48:51], 0
v_mfma_f32_16x16x32_bf16 v[32:35], v[8:11], v[56:59], 0
v_mfma_f32_16x16x32_bf16 v[28:31], v[12:15], v[52:55], v[28:31]
v_mfma_f32_16x16x32_bf16 v[32:35], v[12:15], v[60:63], v[32:35]
s_add_u32 s58, s26, 2064384
s_addc_u32 s59, s27, 0
global_load_dwordx4 v[48:51], v1, s[58:59] offset:0 nt
global_load_dwordx4 v[52:55], v1, s[58:59] offset:256 nt
global_load_dwordx4 v[56:59], v1, s[58:59] offset:512 nt
global_load_dwordx4 v[60:63], v1, s[58:59] offset:768 nt
s_nop 0
v_add_f32_dpp v44, v29, v28 quad_perm:[1,1,3,3] row_mask:0x3 bank_mask:0xf
s_nop 1
v_add_f32_dpp v44, v33, v32 quad_perm:[1,1,3,3] row_mask:0xc bank_mask:0xf
v_fma_f32 v45, v44, s38, v2
v_cmp_lt_f32_e32 vcc, 0, v45
s_nop 0
s_and_b64 s[48:49], vcc, s[40:41]
s_cbranch_scc0 Lc211_%=
s_swappc_b64 s[42:43], s[44:45]
Lc211_%=:
v_exp_f32_e32 v45, v45
s_nop 0
v_cndmask_b32_e64 v45, 0, v45, s[40:41]
v_add_f32_e32 v46, v46, v45
v_cvt_pk_bf16_f32 v44, v45, v45
s_nop 1
v_mov_b32_dpp v44, v44 row_ror:8 row_mask:0xa bank_mask:0xf
s_waitcnt vmcnt(8)
s_nop 0
v_and_b32_dpp v28, v44, v4 row_newbcast:0 row_mask:0xf bank_mask:0xf
v_and_b32_dpp v29, v44, v5 row_newbcast:0 row_mask:0xf bank_mask:0xf
v_and_b32_dpp v30, v44, v6 row_newbcast:0 row_mask:0xf bank_mask:0xf
v_and_b32_dpp v31, v44, v7 row_newbcast:0 row_mask:0xf bank_mask:0xf
v_and_b32_dpp v32, v44, v4 row_newbcast:2 row_mask:0xf bank_mask:0xf
v_and_b32_dpp v33, v44, v5 row_newbcast:2 row_mask:0xf bank_mask:0xf
v_and_b32_dpp v34, v44, v6 row_newbcast:2 row_mask:0xf bank_mask:0xf
v_and_b32_dpp v35, v44, v7 row_newbcast:2 row_mask:0xf bank_mask:0xf
v_mfma_f32_16x16x32_bf16 v[36:39], v[28:31], v[64:67], v[36:39]
v_and_b32_dpp v28, v44, v4 row_newbcast:4 row_mask:0xf bank_mask:0xf
v_and_b32_dpp v29, v44, v5 row_newbcast:4 row_mask:0xf bank_mask:0xf
v_and_b32_dpp v30, v44, v6 row_newbcast:4 row_mask:0xf bank_mask:0xf
v_and_b32_dpp v31, v44, v7 row_newbcast:4 row_mask:0xf bank_mask:0xf
v_mfma_f32_16x16x32_bf16 v[36:39], v[32:35], v[68:71], v[36:39]
v_and_b32_dpp v32, v44, v4 row_newbcast:6 row_mask:0xf bank_mask:0xf
v_and_b32_dpp v33, v44, v5 row_newbcast:6 row_mask:0xf bank_mask:0xf
v_and_b32_dpp v34, v44, v6 row_newbcast:6 row_mask:0xf bank_mask:0xf
v_and_b32_dpp v35, v44, v7 row_newbcast:6 row_mask:0xf bank_mask:0xf
v_mfma_f32_16x16x32_bf16 v[36:39], v[28:31], v[72:75], v[36:39]
s_nop 0
v_mfma_f32_16x16x32_bf16 v[36:39], v[32:35], v[76:79], v[36:39]
s_add_u32 s58, s24, 2179072
s_addc_u32 s59, s25, 0
Lp212_%=:
s_memrealtime s[70:71]
s_waitcnt lgkmcnt(0)
s_sub_u32 s72, s68, s70
s_cmp_gt_i32 s72, 0
s_cbranch_scc0 Lg213_%=
s_sleep 1
s_branch Lp212_%=
Lg213_%=:
s_add_u32 s68, s68, s69
global_load_dwordx4 v[64:67], v0, s[58:59] offset:0 nt
global_load_dwordx4 v[68:71], v0, s[58:59] offset:128 nt
global_load_dwordx4 v[72:75], v0, s[58:59] offset:2048 nt
global_load_dwordx4 v[76:79], v0, s[58:59] offset:2176 nt
v_add_f32_dpp v29, v46, v46 row_ror:8 row_mask:0xf bank_mask:0xf
s_nop 1
v_add_f32_dpp v29, v29, v29 row_ror:4 row_mask:0xf bank_mask:0xf
s_nop 1
v_add_f32_dpp v29, v29, v29 row_ror:2 row_mask:0xf bank_mask:0xf
s_nop 1
v_add_f32_dpp v29, v29, v29 row_ror:1 row_mask:0xf bank_mask:0xf
s_nop 0
v_readlane_b32 s48, v29, 0
v_readlane_b32 s49, v29, 16
v_readlane_b32 s50, v29, 32
v_readlane_b32 s51, v29, 48
v_mov_b32_e32 v29, s48
v_add_f32_e32 v29, s49, v29
v_add_f32_e32 v29, s50, v29
v_add_f32_e32 v29, s51, v29
v_mov_b32_e32 v45, s74
s_nop 1
Ld214_%=:
ds_read_b32 v44, v45 offset:2116
s_waitcnt lgkmcnt(0)
v_readfirstlane_b32 s48, v44
s_nop 3
s_cmp_ge_u32 s48, 2
s_cbranch_scc1 Ldg215_%=
s_sub_u32 s56, s56, 1
s_cbranch_scc0 Ld214_%=
s_mov_b32 s56, 0
Ldg215_%=:
s_nop 1
v_mbcnt_lo_u32_b32 v45, -1, 0
v_mbcnt_hi_u32_b32 v45, -1, v45
v_lshlrev_b32_e32 v45, 4, v45
v_add_u32_e32 v45, s75, v45
v_sub_f32_e32 v28, 0, v2
s_sub_u32 s49, s75, s74
s_lshr_b32 s49, s49, 6
s_add_u32 s49, s49, s74
v_mov_b32_e32 v44, s49
s_mov_b32 exec_hi, 0
ds_write_b128 v45, v[36:39]
s_mov_b32 exec_lo, 1
ds_write_b64 v44, v[28:29] offset:2048
s_waitcnt lgkmcnt(0)
v_mov_b32_e32 v45, s74
v_mov_b32_e32 v44, 1
ds_add_rtn_u32 v44, v45, v44 offset:2112
s_waitcnt lgkmcnt(0)
v_readfirstlane_b32 s48, v44
s_mov_b64 exec, -1
s_and_b32 s48, s48, 3
s_cmp_eq_u32 s48, 3
s_nop 1
s_cbranch_scc0 Lnp216_%=
v_mbcnt_lo_u32_b32 v45, -1, 0
v_mbcnt_hi_u32_b32 v45, -1, v45
v_lshlrev_b32_e32 v45, 4, v45
v_add_u32_e32 v45, s74, v45
ds_read_b128 v[28:31], v45
ds_read_b128 v[8:11], v45 offset:512
ds_read_b128 v[12:15], v45 offset:1024
v_mbcnt_lo_u32_b32 v44, -1, 0
v_mbcnt_hi_u32_b32 v44, -1, v44
v_and_b32_e32 v44, 3, v44
v_lshlrev_b32_e32 v44, 3, v44
v_add_u32_e32 v44, s74, v44
ds_read_b64 v[32:33], v44 offset:2048
s_waitcnt lgkmcnt(0)
v_max_f32_dpp v34, v32, v32 quad_perm:[1,0,3,2] row_mask:0xf bank_mask:0xf
s_nop 1
v_max_f32_dpp v34, v34, v34 quad_perm:[2,3,0,1] row_mask:0xf bank_mask:0xf
v_sub_f32_e32 v44, v32, v34
v_exp_f32_e32 v44, v44
s_nop 0
v_mul_f32_e32 v35, v44, v33
s_nop 1
v_add_f32_dpp v35, v35, v35 quad_perm:[1,0,3,2] row_mask:0xf bank_mask:0xf
s_nop 1
v_add_f32_dpp v35, v35, v35 quad_perm:[2,3,0,1] row_mask:0xf bank_mask:0xf
v_mul_f32_dpp v36, v44, v28 quad_perm:[0,0,0,0] row_mask:0xf bank_mask:0xf
v_mul_f32_dpp v37, v44, v29 quad_perm:[0,0,0,0] row_mask:0xf bank_mask:0xf
v_mul_f32_dpp v38, v44, v30 quad_perm:[0,0,0,0] row_mask:0xf bank_mask:0xf
v_mul_f32_dpp v39, v44, v31 quad_perm:[0,0,0,0] row_mask:0xf bank_mask:0xf
ds_read_b128 v[28:31], v45 offset:1536
v_fmac_f32_dpp v36, v44, v8 quad_perm:[1,1,1,1] row_mask:0xf bank_mask:0xf
v_fmac_f32_dpp v37, v44, v9 quad_perm:[1,1,1,1] row_mask:0xf bank_mask:0xf
v_fmac_f32_dpp v38, v44, v10 quad_perm:[1,1,1,1] row_mask:0xf bank_mask:0xf
v_fmac_f32_dpp v39, v44, v11 quad_perm:[1,1,1,1] row_mask:0xf bank_mask:0xf
v_fmac_f32_dpp v36, v44, v12 quad_perm:[2,2,2,2] row_mask:0xf bank_mask:0xf
v_fmac_f32_dpp v37, v44, v13 quad_perm:[2,2,2,2] row_mask:0xf bank_mask:0xf
v_fmac_f32_dpp v38, v44, v14 quad_perm:[2,2,2,2] row_mask:0xf bank_mask:0xf
v_fmac_f32_dpp v39, v44, v15 quad_perm:[2,2,2,2] row_mask:0xf bank_mask:0xf
s_waitcnt lgkmcnt(0)
v_fmac_f32_dpp v36, v44, v28 quad_perm:[3,3,3,3] row_mask:0xf bank_mask:0xf
v_fmac_f32_dpp v37, v44, v29 quad_perm:[3,3,3,3] row_mask:0xf bank_mask:0xf
v_fmac_f32_dpp v38, v44, v30 quad_perm:[3,3,3,3] row_mask:0xf bank_mask:0xf
v_fmac_f32_dpp v39, v44, v31 quad_perm:[3,3,3,3] row_mask:0xf bank_mask:0xf
v_mov_b32_e32 v45, s74
v_mov_b32_e32 v44, 3
ds_write_b32 v45, v44 offset:2116
s_add_u32 s58, s66, 589824
s_addc_u32 s59, s67, 0
v_permlane32_swap_b32 v36, v38
v_permlane32_swap_b32 v37, v39
v_mov_b32_e32 v28, v36
v_mov_b32_e32 v29, s37
v_mov_b32_e32 v30, v37
v_mov_b32_e32 v31, s37
v_mov_b32_e32 v8, v34
v_mov_b32_e32 v9, s37
v_mov_b32_e32 v10, v35
v_mov_b32_e32 v11, s37
s_waitcnt lgkmcnt(0)
v_mbcnt_lo_u32_b32 v45, -1, 0
v_mbcnt_hi_u32_b32 v45, -1, v45
v_lshlrev_b32_e32 v45, 4, v45
global_store_dwordx4 v45, v[28:31], s[58:59] offset:0 sc1
s_mov_b64 exec, 0xff
global_store_dwordx4 v45, v[8:11], s[58:59] offset:1024 sc1
s_mov_b64 exec, -1
s_nop 3
Lnp216_%=:
s_nop 1
v_mov_b32_e32 v36, 0
v_mov_b32_e32 v37, 0
v_mov_b32_e32 v38, 0
v_mov_b32_e32 v39, 0
v_mov_b32_e32 v46, 0
v_mov_b32_e32 v2, 0x7f800000
v_cmp_ne_u32_e32 vcc, s37, v17
s_nop 0
s_cbranch_vccz Lok217_%=
s_add_u32 s58, s64, 49152
s_addc_u32 s59, s65, 0
Lag218_%=:
s_sleep 2
global_load_dwordx2 v[16:17], v18, s[58:59] offset:0 sc1
s_waitcnt vmcnt(0)
v_cmp_ne_u32_e32 vcc, s37, v17
s_cbranch_vccz Lok217_%=
s_sub_u32 s56, s56, 1
s_cbranch_scc0 Lag218_%=
s_mov_b32 s56, 0
Lok217_%=:
s_nop 1
v_mov_b32_e32 v45, v16
v_mbcnt_lo_u32_b32 v44, -1, 0
v_mbcnt_hi_u32_b32 v44, -1, v44
v_and_b32_e32 v16, 1, v44
v_lshlrev_b32_e32 v16, 6, v16
v_and_b32_e32 v44, 48, v44
v_add_u32_e32 v44, v16, v44
ds_bpermute_b32 v16, v44, v45
ds_bpermute_b32 v17, v44, v45 offset:4
ds_bpermute_b32 v18, v44, v45 offset:8
ds_bpermute_b32 v19, v44, v45 offset:12
ds_bpermute_b32 v20, v44, v45 offset:128
ds_bpermute_b32 v21, v44, v45 offset:132
ds_bpermute_b32 v22, v44, v45 offset:136
ds_bpermute_b32 v23, v44, v45 offset:140
s_waitcnt lgkmcnt(0)
v_mfma_f32_16x16x32_bf16 v[28:31], v[16:19], v[128:131], 0
v_mfma_f32_16x16x32_bf16 v[32:35], v[16:19], v[136:139], 0
v_mfma_f32_16x16x32_bf16 v[28:31], v[20:23], v[132:135], v[28:31]
v_mfma_f32_16x16x32_bf16 v[32:35], v[20:23], v[140:143], v[32:35]
s_add_u32 s58, s24, 2293760
s_addc_u32 s59, s25, 0
Lp219_%=:
s_memrealtime s[70:71]
s_waitcnt lgkmcnt(0)
s_sub_u32 s72, s68, s70
s_cmp_gt_i32 s72, 0
s_cbranch_scc0 Lg220_%=
s_sleep 1
s_branch Lp219_%=
Lg220_%=:
s_add_u32 s68, s68, s69
global_load_dwordx4 v[128:131], v0, s[58:59] offset:0 nt
global_load_dwordx4 v[132:135], v0, s[58:59] offset:128 nt
global_load_dwordx4 v[136:139], v0, s[58:59] offset:2048 nt
global_load_dwordx4 v[140:143], v0, s[58:59] offset:2176 nt
v_add_f32_dpp v44, v29, v28 quad_perm:[1,1,3,3] row_mask:0x3 bank_mask:0xf
s_nop 1
v_add_f32_dpp v44, v33, v32 quad_perm:[1,1,3,3] row_mask:0xc bank_mask:0xf
v_fma_f32 v45, v44, s38, v3
v_cmp_lt_f32_e32 vcc, 0, v45
s_nop 0
s_and_b64 s[48:49], vcc, s[40:41]
s_cbranch_scc0 Lc221_%=
s_swappc_b64 s[42:43], s[46:47]
Lc221_%=:
v_exp_f32_e32 v45, v45
s_nop 0
v_cndmask_b32_e64 v45, 0, v45, s[40:41]
v_add_f32_e32 v47, v47, v45
v_cvt_pk_bf16_f32 v44, v45, v45
s_nop 1
v_mov_b32_dpp v44, v44 row_ror:8 row_mask:0xa bank_mask:0xf
s_nop 1
v_and_b32_dpp v28, v44, v4 row_newbcast:0 row_mask:0xf bank_mask:0xf
v_and_b32_dpp v29, v44, v5 row_newbcast:0 row_mask:0xf bank_mask:0xf
v_and_b32_dpp v30, v44, v6 row_newbcast:0 row_mask:0xf bank_mask:0xf
v_and_b32_dpp v31, v44, v7 row_newbcast:0 row_mask:0xf bank_mask:0xf
v_and_b32_dpp v32, v44, v4 row_newbcast:2 row_mask:0xf bank_mask:0xf
v_and_b32_dpp v33, v44, v5 row_newbcast:2 row_mask:0xf bank_mask:0xf
v_and_b32_dpp v34, v44, v6 row_newbcast:2 row_mask:0xf bank_mask:0xf
v_and_b32_dpp v35, v44, v7 row_newbcast:2 row_mask:0xf bank_mask:0xf
v_mfma_f32_16x16x32_bf16 v[40:43], v[28:31], v[144:147], v[40:43]
v_and_b32_dpp v28, v44, v4 row_newbcast:4 row_mask:0xf bank_mask:0xf
v_and_b32_dpp v29, v44, v5 row_newbcast:4 row_mask:0xf bank_mask:0xf
v_and_b32_dpp v30, v44, v6 row_newbcast:4 row_mask:0xf bank_mask:0xf
v_and_b32_dpp v31, v44, v7 row_newbcast:4 row_mask:0xf bank_mask:0xf
v_mfma_f32_16x16x32_bf16 v[40:43], v[32:35], v[148:151], v[40:43]
v_and_b32_dpp v32, v44, v4 row_newbcast:6 row_mask:0xf bank_mask:0xf
v_and_b32_dpp v33, v44, v5 row_newbcast:6 row_mask:0xf bank_mask:0xf
v_and_b32_dpp v34, v44, v6 row_newbcast:6 row_mask:0xf bank_mask:0xf
v_and_b32_dpp v35, v44, v7 row_newbcast:6 row_mask:0xf bank_mask:0xf
v_mfma_f32_16x16x32_bf16 v[40:43], v[28:31], v[152:155], v[40:43]
s_nop 0
v_mfma_f32_16x16x32_bf16 v[40:43], v[32:35], v[156:159], v[40:43]
s_add_u32 s58, s26, 2293760
s_addc_u32 s59, s27, 0
global_load_dwordx4 v[144:147], v1, s[58:59] offset:0 nt
global_load_dwordx4 v[148:151], v1, s[58:59] offset:256 nt
global_load_dwordx4 v[152:155], v1, s[58:59] offset:512 nt
global_load_dwordx4 v[156:159], v1, s[58:59] offset:768 nt
v_mfma_f32_16x16x32_bf16 v[28:31], v[16:19], v[160:163], 0
v_mfma_f32_16x16x32_bf16 v[32:35], v[16:19], v[168:171], 0
v_mfma_f32_16x16x32_bf16 v[28:31], v[20:23], v[164:167], v[28:31]
v_mfma_f32_16x16x32_bf16 v[32:35], v[20:23], v[172:175], v[32:35]
s_add_u32 s58, s24, 2408448
s_addc_u32 s59, s25, 0
global_load_dwordx4 v[160:163], v0, s[58:59] offset:0 nt
global_load_dwordx4 v[164:167], v0, s[58:59] offset:128 nt
global_load_dwordx4 v[168:171], v0, s[58:59] offset:2048 nt
global_load_dwordx4 v[172:175], v0, s[58:59] offset:2176 nt
s_nop 0
v_add_f32_dpp v44, v29, v28 quad_perm:[1,1,3,3] row_mask:0x3 bank_mask:0xf
s_nop 1
v_add_f32_dpp v44, v33, v32 quad_perm:[1,1,3,3] row_mask:0xc bank_mask:0xf
v_fma_f32 v45, v44, s38, v3
v_cmp_lt_f32_e32 vcc, 0, v45
s_nop 0
s_and_b64 s[48:49], vcc, s[40:41]
s_cbranch_scc0 Lc222_%=
s_swappc_b64 s[42:43], s[46:47]
Lc222_%=:
v_exp_f32_e32 v45, v45
s_nop 0
v_cndmask_b32_e64 v45, 0, v45, s[40:41]
v_add_f32_e32 v47, v47, v45
v_cvt_pk_bf16_f32 v44, v45, v45
s_nop 1
v_mov_b32_dpp v44, v44 row_ror:8 row_mask:0xa bank_mask:0xf
s_nop 1
v_and_b32_dpp v28, v44, v4 row_newbcast:0 row_mask:0xf bank_mask:0xf
v_and_b32_dpp v29, v44, v5 row_newbcast:0 row_mask:0xf bank_mask:0xf
v_and_b32_dpp v30, v44, v6 row_newbcast:0 row_mask:0xf bank_mask:0xf
v_and_b32_dpp v31, v44, v7 row_newbcast:0 row_mask:0xf bank_mask:0xf
v_and_b32_dpp v32, v44, v4 row_newbcast:2 row_mask:0xf bank_mask:0xf
v_and_b32_dpp v33, v44, v5 row_newbcast:2 row_mask:0xf bank_mask:0xf
v_and_b32_dpp v34, v44, v6 row_newbcast:2 row_mask:0xf bank_mask:0xf
v_and_b32_dpp v35, v44, v7 row_newbcast:2 row_mask:0xf bank_mask:0xf
v_mfma_f32_16x16x32_bf16 v[40:43], v[28:31], v[176:179], v[40:43]
v_and_b32_dpp v28, v44, v4 row_newbcast:4 row_mask:0xf bank_mask:0xf
v_and_b32_dpp v29, v44, v5 row_newbcast:4 row_mask:0xf bank_mask:0xf
v_and_b32_dpp v30, v44, v6 row_newbcast:4 row_mask:0xf bank_mask:0xf
v_and_b32_dpp v31, v44, v7 row_newbcast:4 row_mask:0xf bank_mask:0xf
v_mfma_f32_16x16x32_bf16 v[40:43], v[32:35], v[180:183], v[40:43]
v_and_b32_dpp v32, v44, v4 row_newbcast:6 row_mask:0xf bank_mask:0xf
v_and_b32_dpp v33, v44, v5 row_newbcast:6 row_mask:0xf bank_mask:0xf
v_and_b32_dpp v34, v44, v6 row_newbcast:6 row_mask:0xf bank_mask:0xf
v_and_b32_dpp v35, v44, v7 row_newbcast:6 row_mask:0xf bank_mask:0xf
v_mfma_f32_16x16x32_bf16 v[40:43], v[28:31], v[184:187], v[40:43]
s_nop 0
v_mfma_f32_16x16x32_bf16 v[40:43], v[32:35], v[188:191], v[40:43]
s_add_u32 s58, s26, 2408448
s_addc_u32 s59, s27, 0
global_load_dwordx4 v[176:179], v1, s[58:59] offset:0 nt
global_load_dwordx4 v[180:183], v1, s[58:59] offset:256 nt
global_load_dwordx4 v[184:187], v1, s[58:59] offset:512 nt
global_load_dwordx4 v[188:191], v1, s[58:59] offset:768 nt
v_mfma_f32_16x16x32_bf16 v[28:31], v[16:19], v[192:195], 0
v_mfma_f32_16x16x32_bf16 v[32:35], v[16:19], v[200:203], 0
v_mfma_f32_16x16x32_bf16 v[28:31], v[20:23], v[196:199], v[28:31]
v_mfma_f32_16x16x32_bf16 v[32:35], v[20:23], v[204:207], v[32:35]
s_add_u32 s58, s24, 2523136
s_addc_u32 s59, s25, 0
Lp223_%=:
s_memrealtime s[70:71]
s_waitcnt lgkmcnt(0)
s_sub_u32 s72, s68, s70
s_cmp_gt_i32 s72, 0
s_cbranch_scc0 Lg224_%=
s_sleep 1
s_branch Lp223_%=
Lg224_%=:
s_add_u32 s68, s68, s69
global_load_dwordx4 v[192:195], v0, s[58:59] offset:0 nt
global_load_dwordx4 v[196:199], v0, s[58:59] offset:128 nt
global_load_dwordx4 v[200:203], v0, s[58:59] offset:2048 nt
global_load_dwordx4 v[204:207], v0, s[58:59] offset:2176 nt
v_add_f32_dpp v44, v29, v28 quad_perm:[1,1,3,3] row_mask:0x3 bank_mask:0xf
s_nop 1
v_add_f32_dpp v44, v33, v32 quad_perm:[1,1,3,3] row_mask:0xc bank_mask:0xf
v_fma_f32 v45, v44, s38, v3
v_cmp_lt_f32_e32 vcc, 0, v45
s_nop 0
s_and_b64 s[48:49], vcc, s[40:41]
s_cbranch_scc0 Lc225_%=
s_swappc_b64 s[42:43], s[46:47]
Lc225_%=:
v_exp_f32_e32 v45, v45
s_nop 0
v_cndmask_b32_e64 v45, 0, v45, s[40:41]
v_add_f32_e32 v47, v47, v45
v_cvt_pk_bf16_f32 v44, v45, v45
s_nop 1
v_mov_b32_dpp v44, v44 row_ror:8 row_mask:0xa bank_mask:0xf
s_nop 1
v_and_b32_dpp v28, v44, v4 row_newbcast:0 row_mask:0xf bank_mask:0xf
v_and_b32_dpp v29, v44, v5 row_newbcast:0 row_mask:0xf bank_mask:0xf
v_and_b32_dpp v30, v44, v6 row_newbcast:0 row_mask:0xf bank_mask:0xf
v_and_b32_dpp v31, v44, v7 row_newbcast:0 row_mask:0xf bank_mask:0xf
v_and_b32_dpp v32, v44, v4 row_newbcast:2 row_mask:0xf bank_mask:0xf
v_and_b32_dpp v33, v44, v5 row_newbcast:2 row_mask:0xf bank_mask:0xf
v_and_b32_dpp v34, v44, v6 row_newbcast:2 row_mask:0xf bank_mask:0xf
v_and_b32_dpp v35, v44, v7 row_newbcast:2 row_mask:0xf bank_mask:0xf
v_mfma_f32_16x16x32_bf16 v[40:43], v[28:31], v[208:211], v[40:43]
v_and_b32_dpp v28, v44, v4 row_newbcast:4 row_mask:0xf bank_mask:0xf
v_and_b32_dpp v29, v44, v5 row_newbcast:4 row_mask:0xf bank_mask:0xf
v_and_b32_dpp v30, v44, v6 row_newbcast:4 row_mask:0xf bank_mask:0xf
v_and_b32_dpp v31, v44, v7 row_newbcast:4 row_mask:0xf bank_mask:0xf
v_mfma_f32_16x16x32_bf16 v[40:43], v[32:35], v[212:215], v[40:43]
v_and_b32_dpp v32, v44, v4 row_newbcast:6 row_mask:0xf bank_mask:0xf
v_and_b32_dpp v33, v44, v5 row_newbcast:6 row_mask:0xf bank_mask:0xf
v_and_b32_dpp v34, v44, v6 row_newbcast:6 row_mask:0xf bank_mask:0xf
v_and_b32_dpp v35, v44, v7 row_newbcast:6 row_mask:0xf bank_mask:0xf
v_mfma_f32_16x16x32_bf16 v[40:43], v[28:31], v[216:219], v[40:43]
s_nop 0
v_mfma_f32_16x16x32_bf16 v[40:43], v[32:35], v[220:223], v[40:43]
s_add_u32 s58, s26, 2523136
s_addc_u32 s59, s27, 0
global_load_dwordx4 v[208:211], v1, s[58:59] offset:0 nt
global_load_dwordx4 v[212:215], v1, s[58:59] offset:256 nt
global_load_dwordx4 v[216:219], v1, s[58:59] offset:512 nt
global_load_dwordx4 v[220:223], v1, s[58:59] offset:768 nt
v_mfma_f32_16x16x32_bf16 v[28:31], v[16:19], v[224:227], 0
v_mfma_f32_16x16x32_bf16 v[32:35], v[16:19], v[232:235], 0
v_mfma_f32_16x16x32_bf16 v[28:31], v[20:23], v[228:231], v[28:31]
v_mfma_f32_16x16x32_bf16 v[32:35], v[20:23], v[236:239], v[32:35]
s_add_u32 s58, s24, 2637824
s_addc_u32 s59, s25, 0
Lp226_%=:
s_memrealtime s[70:71]
s_waitcnt lgkmcnt(0)
s_sub_u32 s72, s68, s70
s_cmp_gt_i32 s72, 0
s_cbranch_scc0 Lg227_%=
s_sleep 1
s_branch Lp226_%=
Lg227_%=:
s_add_u32 s68, s68, s69
global_load_dwordx4 v[224:227], v0, s[58:59] offset:0 nt
global_load_dwordx4 v[228:231], v0, s[58:59] offset:128 nt
global_load_dwordx4 v[232:235], v0, s[58:59] offset:2048 nt
global_load_dwordx4 v[236:239], v0, s[58:59] offset:2176 nt
v_add_f32_dpp v44, v29, v28 quad_perm:[1,1,3,3] row_mask:0x3 bank_mask:0xf
s_nop 1
v_add_f32_dpp v44, v33, v32 quad_perm:[1,1,3,3] row_mask:0xc bank_mask:0xf
v_fma_f32 v45, v44, s38, v3
v_cmp_lt_f32_e32 vcc, 0, v45
s_nop 0
s_and_b64 s[48:49], vcc, s[40:41]
s_cbranch_scc0 Lc228_%=
s_swappc_b64 s[42:43], s[46:47]
Lc228_%=:
v_exp_f32_e32 v45, v45
s_nop 0
v_cndmask_b32_e64 v45, 0, v45, s[40:41]
v_add_f32_e32 v47, v47, v45
v_cvt_pk_bf16_f32 v44, v45, v45
s_nop 1
v_mov_b32_dpp v44, v44 row_ror:8 row_mask:0xa bank_mask:0xf
s_nop 1
v_and_b32_dpp v28, v44, v4 row_newbcast:0 row_mask:0xf bank_mask:0xf
v_and_b32_dpp v29, v44, v5 row_newbcast:0 row_mask:0xf bank_mask:0xf
v_and_b32_dpp v30, v44, v6 row_newbcast:0 row_mask:0xf bank_mask:0xf
v_and_b32_dpp v31, v44, v7 row_newbcast:0 row_mask:0xf bank_mask:0xf
v_and_b32_dpp v32, v44, v4 row_newbcast:2 row_mask:0xf bank_mask:0xf
v_and_b32_dpp v33, v44, v5 row_newbcast:2 row_mask:0xf bank_mask:0xf
v_and_b32_dpp v34, v44, v6 row_newbcast:2 row_mask:0xf bank_mask:0xf
v_and_b32_dpp v35, v44, v7 row_newbcast:2 row_mask:0xf bank_mask:0xf
v_mfma_f32_16x16x32_bf16 v[40:43], v[28:31], v[240:243], v[40:43]
v_and_b32_dpp v28, v44, v4 row_newbcast:4 row_mask:0xf bank_mask:0xf
v_and_b32_dpp v29, v44, v5 row_newbcast:4 row_mask:0xf bank_mask:0xf
v_and_b32_dpp v30, v44, v6 row_newbcast:4 row_mask:0xf bank_mask:0xf
v_and_b32_dpp v31, v44, v7 row_newbcast:4 row_mask:0xf bank_mask:0xf
v_mfma_f32_16x16x32_bf16 v[40:43], v[32:35], v[244:247], v[40:43]
v_and_b32_dpp v32, v44, v4 row_newbcast:6 row_mask:0xf bank_mask:0xf
v_and_b32_dpp v33, v44, v5 row_newbcast:6 row_mask:0xf bank_mask:0xf
v_and_b32_dpp v34, v44, v6 row_newbcast:6 row_mask:0xf bank_mask:0xf
v_and_b32_dpp v35, v44, v7 row_newbcast:6 row_mask:0xf bank_mask:0xf
v_mfma_f32_16x16x32_bf16 v[40:43], v[28:31], v[248:251], v[40:43]
s_nop 0
v_mfma_f32_16x16x32_bf16 v[40:43], v[32:35], v[252:255], v[40:43]
s_add_u32 s58, s26, 2637824
s_addc_u32 s59, s27, 0
global_load_dwordx4 v[240:243], v1, s[58:59] offset:0 nt
global_load_dwordx4 v[244:247], v1, s[58:59] offset:256 nt
global_load_dwordx4 v[248:251], v1, s[58:59] offset:512 nt
global_load_dwordx4 v[252:255], v1, s[58:59] offset:768 nt
v_mfma_f32_16x16x32_bf16 v[28:31], v[16:19], a[0:3], 0
v_mfma_f32_16x16x32_bf16 v[32:35], v[16:19], a[8:11], 0
v_mfma_f32_16x16x32_bf16 v[28:31], v[20:23], a[4:7], v[28:31]
v_mfma_f32_16x16x32_bf16 v[32:35], v[20:23], a[12:15], v[32:35]
s_add_u32 s58, s24, 2752512
s_addc_u32 s59, s25, 0
global_load_dwordx4 a[0:3], v0, s[58:59] offset:0 nt
global_load_dwordx4 a[4:7], v0, s[58:59] offset:128 nt
global_load_dwordx4 a[8:11], v0, s[58:59] offset:2048 nt
global_load_dwordx4 a[12:15], v0, s[58:59] offset:2176 nt
s_nop 0
v_add_f32_dpp v44, v29, v28 quad_perm:[1,1,3,3] row_mask:0x3 bank_mask:0xf
s_nop 1
v_add_f32_dpp v44, v33, v32 quad_perm:[1,1,3,3] row_mask:0xc bank_mask:0xf
v_fma_f32 v45, v44, s38, v3
v_cmp_lt_f32_e32 vcc, 0, v45
s_nop 0
s_and_b64 s[48:49], vcc, s[40:41]
s_cbranch_scc0 Lc229_%=
s_swappc_b64 s[42:43], s[46:47]
Lc229_%=:
v_exp_f32_e32 v45, v45
s_nop 0
v_cndmask_b32_e64 v45, 0, v45, s[40:41]
v_add_f32_e32 v47, v47, v45
v_cvt_pk_bf16_f32 v44, v45, v45
s_nop 1
v_mov_b32_dpp v44, v44 row_ror:8 row_mask:0xa bank_mask:0xf
s_nop 1
v_and_b32_dpp v28, v44, v4 row_newbcast:0 row_mask:0xf bank_mask:0xf
v_and_b32_dpp v29, v44, v5 row_newbcast:0 row_mask:0xf bank_mask:0xf
v_and_b32_dpp v30, v44, v6 row_newbcast:0 row_mask:0xf bank_mask:0xf
v_and_b32_dpp v31, v44, v7 row_newbcast:0 row_mask:0xf bank_mask:0xf
v_and_b32_dpp v32, v44, v4 row_newbcast:2 row_mask:0xf bank_mask:0xf
v_and_b32_dpp v33, v44, v5 row_newbcast:2 row_mask:0xf bank_mask:0xf
v_and_b32_dpp v34, v44, v6 row_newbcast:2 row_mask:0xf bank_mask:0xf
v_and_b32_dpp v35, v44, v7 row_newbcast:2 row_mask:0xf bank_mask:0xf
v_mfma_f32_16x16x32_bf16 v[40:43], v[28:31], a[16:19], v[40:43]
v_and_b32_dpp v28, v44, v4 row_newbcast:4 row_mask:0xf bank_mask:0xf
v_and_b32_dpp v29, v44, v5 row_newbcast:4 row_mask:0xf bank_mask:0xf
v_and_b32_dpp v30, v44, v6 row_newbcast:4 row_mask:0xf bank_mask:0xf
v_and_b32_dpp v31, v44, v7 row_newbcast:4 row_mask:0xf bank_mask:0xf
v_mfma_f32_16x16x32_bf16 v[40:43], v[32:35], a[20:23], v[40:43]
v_and_b32_dpp v32, v44, v4 row_newbcast:6 row_mask:0xf bank_mask:0xf
v_and_b32_dpp v33, v44, v5 row_newbcast:6 row_mask:0xf bank_mask:0xf
v_and_b32_dpp v34, v44, v6 row_newbcast:6 row_mask:0xf bank_mask:0xf
v_and_b32_dpp v35, v44, v7 row_newbcast:6 row_mask:0xf bank_mask:0xf
v_mfma_f32_16x16x32_bf16 v[40:43], v[28:31], a[24:27], v[40:43]
s_nop 0
v_mfma_f32_16x16x32_bf16 v[40:43], v[32:35], a[28:31], v[40:43]
s_add_u32 s58, s26, 2752512
s_addc_u32 s59, s27, 0
global_load_dwordx4 a[16:19], v1, s[58:59] offset:0 nt
global_load_dwordx4 a[20:23], v1, s[58:59] offset:256 nt
global_load_dwordx4 a[24:27], v1, s[58:59] offset:512 nt
global_load_dwordx4 a[28:31], v1, s[58:59] offset:768 nt
v_mfma_f32_16x16x32_bf16 v[28:31], v[16:19], a[32:35], 0
v_mfma_f32_16x16x32_bf16 v[32:35], v[16:19], a[40:43], 0
v_mfma_f32_16x16x32_bf16 v[28:31], v[20:23], a[36:39], v[28:31]
v_mfma_f32_16x16x32_bf16 v[32:35], v[20:23], a[44:47], v[32:35]
s_add_u32 s58, s24, 2867200
s_addc_u32 s59, s25, 0
Lp230_%=:
s_memrealtime s[70:71]
s_waitcnt lgkmcnt(0)
s_sub_u32 s72, s68, s70
s_cmp_gt_i32 s72, 0
s_cbranch_scc0 Lg231_%=
s_sleep 1
s_branch Lp230_%=
Lg231_%=:
s_add_u32 s68, s68, s69
global_load_dwordx4 a[32:35], v0, s[58:59] offset:0 nt
global_load_dwordx4 a[36:39], v0, s[58:59] offset:128 nt
global_load_dwordx4 a[40:43], v0, s[58:59] offset:2048 nt
global_load_dwordx4 a[44:47], v0, s[58:59] offset:2176 nt
v_add_f32_dpp v44, v29, v28 quad_perm:[1,1,3,3] row_mask:0x3 bank_mask:0xf
s_nop 1
v_add_f32_dpp v44, v33, v32 quad_perm:[1,1,3,3] row_mask:0xc bank_mask:0xf
v_fma_f32 v45, v44, s38, v3
v_cmp_lt_f32_e32 vcc, 0, v45
s_nop 0
s_and_b64 s[48:49], vcc, s[40:41]
s_cbranch_scc0 Lc232_%=
s_swappc_b64 s[42:43], s[46:47]
Lc232_%=:
v_exp_f32_e32 v45, v45
s_nop 0
v_cndmask_b32_e64 v45, 0, v45, s[40:41]
v_add_f32_e32 v47, v47, v45
v_cvt_pk_bf16_f32 v44, v45, v45
s_nop 1
v_mov_b32_dpp v44, v44 row_ror:8 row_mask:0xa bank_mask:0xf
s_nop 1
v_and_b32_dpp v28, v44, v4 row_newbcast:0 row_mask:0xf bank_mask:0xf
v_and_b32_dpp v29, v44, v5 row_newbcast:0 row_mask:0xf bank_mask:0xf
v_and_b32_dpp v30, v44, v6 row_newbcast:0 row_mask:0xf bank_mask:0xf
v_and_b32_dpp v31, v44, v7 row_newbcast:0 row_mask:0xf bank_mask:0xf
v_and_b32_dpp v32, v44, v4 row_newbcast:2 row_mask:0xf bank_mask:0xf
v_and_b32_dpp v33, v44, v5 row_newbcast:2 row_mask:0xf bank_mask:0xf
v_and_b32_dpp v34, v44, v6 row_newbcast:2 row_mask:0xf bank_mask:0xf
v_and_b32_dpp v35, v44, v7 row_newbcast:2 row_mask:0xf bank_mask:0xf
v_mfma_f32_16x16x32_bf16 v[40:43], v[28:31], a[48:51], v[40:43]
v_and_b32_dpp v28, v44, v4 row_newbcast:4 row_mask:0xf bank_mask:0xf
v_and_b32_dpp v29, v44, v5 row_newbcast:4 row_mask:0xf bank_mask:0xf
v_and_b32_dpp v30, v44, v6 row_newbcast:4 row_mask:0xf bank_mask:0xf
v_and_b32_dpp v31, v44, v7 row_newbcast:4 row_mask:0xf bank_mask:0xf
v_mfma_f32_16x16x32_bf16 v[40:43], v[32:35], a[52:55], v[40:43]
v_and_b32_dpp v32, v44, v4 row_newbcast:6 row_mask:0xf bank_mask:0xf
v_and_b32_dpp v33, v44, v5 row_newbcast:6 row_mask:0xf bank_mask:0xf
v_and_b32_dpp v34, v44, v6 row_newbcast:6 row_mask:0xf bank_mask:0xf
v_and_b32_dpp v35, v44, v7 row_newbcast:6 row_mask:0xf bank_mask:0xf
v_mfma_f32_16x16x32_bf16 v[40:43], v[28:31], a[56:59], v[40:43]
s_nop 0
v_mfma_f32_16x16x32_bf16 v[40:43], v[32:35], a[60:63], v[40:43]
s_add_u32 s58, s26, 2867200
s_addc_u32 s59, s27, 0
global_load_dwordx4 a[48:51], v1, s[58:59] offset:0 nt
global_load_dwordx4 a[52:55], v1, s[58:59] offset:256 nt
global_load_dwordx4 a[56:59], v1, s[58:59] offset:512 nt
global_load_dwordx4 a[60:63], v1, s[58:59] offset:768 nt
v_mfma_f32_16x16x32_bf16 v[28:31], v[16:19], a[64:67], 0
v_mfma_f32_16x16x32_bf16 v[32:35], v[16:19], a[72:75], 0
v_mfma_f32_16x16x32_bf16 v[28:31], v[20:23], a[68:71], v[28:31]
v_mfma_f32_16x16x32_bf16 v[32:35], v[20:23], a[76:79], v[32:35]
s_add_u32 s58, s24, 2981888
s_addc_u32 s59, s25, 0
Lp233_%=:
s_memrealtime s[70:71]
s_waitcnt lgkmcnt(0)
s_sub_u32 s72, s68, s70
s_cmp_gt_i32 s72, 0
s_cbranch_scc0 Lg234_%=
s_sleep 1
s_branch Lp233_%=
Lg234_%=:
s_add_u32 s68, s68, s69
s_waitcnt vmcnt(56)
global_load_dwordx4 a[64:67], v0, s[58:59] offset:0 nt
global_load_dwordx4 a[68:71], v0, s[58:59] offset:128 nt
global_load_dwordx4 a[72:75], v0, s[58:59] offset:2048 nt
global_load_dwordx4 a[76:79], v0, s[58:59] offset:2176 nt
v_add_f32_dpp v44, v29, v28 quad_perm:[1,1,3,3] row_mask:0x3 bank_mask:0xf
s_nop 1
v_add_f32_dpp v44, v33, v32 quad_perm:[1,1,3,3] row_mask:0xc bank_mask:0xf
v_fma_f32 v45, v44, s38, v3
v_cmp_lt_f32_e32 vcc, 0, v45
s_nop 0
s_and_b64 s[48:49], vcc, s[40:41]
s_cbranch_scc0 Lc235_%=
s_swappc_b64 s[42:43], s[46:47]
Lc235_%=:
v_exp_f32_e32 v45, v45
s_nop 0
v_cndmask_b32_e64 v45, 0, v45, s[40:41]
v_add_f32_e32 v47, v47, v45
v_cvt_pk_bf16_f32 v44, v45, v45
s_nop 1
v_mov_b32_dpp v44, v44 row_ror:8 row_mask:0xa bank_mask:0xf
s_nop 1
v_and_b32_dpp v28, v44, v4 row_newbcast:0 row_mask:0xf bank_mask:0xf
v_and_b32_dpp v29, v44, v5 row_newbcast:0 row_mask:0xf bank_mask:0xf
v_and_b32_dpp v30, v44, v6 row_newbcast:0 row_mask:0xf bank_mask:0xf
v_and_b32_dpp v31, v44, v7 row_newbcast:0 row_mask:0xf bank_mask:0xf
v_and_b32_dpp v32, v44, v4 row_newbcast:2 row_mask:0xf bank_mask:0xf
v_and_b32_dpp v33, v44, v5 row_newbcast:2 row_mask:0xf bank_mask:0xf
v_and_b32_dpp v34, v44, v6 row_newbcast:2 row_mask:0xf bank_mask:0xf
v_and_b32_dpp v35, v44, v7 row_newbcast:2 row_mask:0xf bank_mask:0xf
v_mfma_f32_16x16x32_bf16 v[40:43], v[28:31], a[80:83], v[40:43]
v_and_b32_dpp v28, v44, v4 row_newbcast:4 row_mask:0xf bank_mask:0xf
v_and_b32_dpp v29, v44, v5 row_newbcast:4 row_mask:0xf bank_mask:0xf
v_and_b32_dpp v30, v44, v6 row_newbcast:4 row_mask:0xf bank_mask:0xf
v_and_b32_dpp v31, v44, v7 row_newbcast:4 row_mask:0xf bank_mask:0xf
v_mfma_f32_16x16x32_bf16 v[40:43], v[32:35], a[84:87], v[40:43]
v_and_b32_dpp v32, v44, v4 row_newbcast:6 row_mask:0xf bank_mask:0xf
v_and_b32_dpp v33, v44, v5 row_newbcast:6 row_mask:0xf bank_mask:0xf
v_and_b32_dpp v34, v44, v6 row_newbcast:6 row_mask:0xf bank_mask:0xf
v_and_b32_dpp v35, v44, v7 row_newbcast:6 row_mask:0xf bank_mask:0xf
v_mfma_f32_16x16x32_bf16 v[40:43], v[28:31], a[88:91], v[40:43]
s_nop 0
v_mfma_f32_16x16x32_bf16 v[40:43], v[32:35], a[92:95], v[40:43]
s_add_u32 s58, s26, 2981888
s_addc_u32 s59, s27, 0
s_waitcnt vmcnt(56)
global_load_dwordx4 a[80:83], v1, s[58:59] offset:0 nt
global_load_dwordx4 a[84:87], v1, s[58:59] offset:256 nt
global_load_dwordx4 a[88:91], v1, s[58:59] offset:512 nt
global_load_dwordx4 a[92:95], v1, s[58:59] offset:768 nt
v_mfma_f32_16x16x32_bf16 v[28:31], v[16:19], a[96:99], 0
v_mfma_f32_16x16x32_bf16 v[32:35], v[16:19], a[104:107], 0
v_mfma_f32_16x16x32_bf16 v[28:31], v[20:23], a[100:103], v[28:31]
v_mfma_f32_16x16x32_bf16 v[32:35], v[20:23], a[108:111], v[32:35]
s_add_u32 s58, s24, 3096576
s_addc_u32 s59, s25, 0
s_waitcnt vmcnt(56)
global_load_dwordx4 a[96:99], v0, s[58:59] offset:0 nt
global_load_dwordx4 a[100:103], v0, s[58:59] offset:128 nt
global_load_dwordx4 a[104:107], v0, s[58:59] offset:2048 nt
global_load_dwordx4 a[108:111], v0, s[58:59] offset:2176 nt
v_add_f32_dpp v44, v29, v28 quad_perm:[1,1,3,3] row_mask:0x3 bank_mask:0xf
s_nop 1
v_add_f32_dpp v44, v33, v32 quad_perm:[1,1,3,3] row_mask:0xc bank_mask:0xf
v_fma_f32 v45, v44, s38, v3
v_cmp_lt_f32_e32 vcc, 0, v45
s_nop 0
s_and_b64 s[48:49], vcc, s[40:41]
s_cbranch_scc0 Lc236_%=
s_swappc_b64 s[42:43], s[46:47]
Lc236_%=:
v_exp_f32_e32 v45, v45
s_nop 0
v_cndmask_b32_e64 v45, 0, v45, s[40:41]
v_add_f32_e32 v47, v47, v45
v_cvt_pk_bf16_f32 v44, v45, v45
s_nop 1
v_mov_b32_dpp v44, v44 row_ror:8 row_mask:0xa bank_mask:0xf
s_nop 1
v_and_b32_dpp v28, v44, v4 row_newbcast:0 row_mask:0xf bank_mask:0xf
v_and_b32_dpp v29, v44, v5 row_newbcast:0 row_mask:0xf bank_mask:0xf
v_and_b32_dpp v30, v44, v6 row_newbcast:0 row_mask:0xf bank_mask:0xf
v_and_b32_dpp v31, v44, v7 row_newbcast:0 row_mask:0xf bank_mask:0xf
v_and_b32_dpp v32, v44, v4 row_newbcast:2 row_mask:0xf bank_mask:0xf
v_and_b32_dpp v33, v44, v5 row_newbcast:2 row_mask:0xf bank_mask:0xf
v_and_b32_dpp v34, v44, v6 row_newbcast:2 row_mask:0xf bank_mask:0xf
v_and_b32_dpp v35, v44, v7 row_newbcast:2 row_mask:0xf bank_mask:0xf
v_mfma_f32_16x16x32_bf16 v[40:43], v[28:31], a[112:115], v[40:43]
v_and_b32_dpp v28, v44, v4 row_newbcast:4 row_mask:0xf bank_mask:0xf
v_and_b32_dpp v29, v44, v5 row_newbcast:4 row_mask:0xf bank_mask:0xf
v_and_b32_dpp v30, v44, v6 row_newbcast:4 row_mask:0xf bank_mask:0xf
v_and_b32_dpp v31, v44, v7 row_newbcast:4 row_mask:0xf bank_mask:0xf
v_mfma_f32_16x16x32_bf16 v[40:43], v[32:35], a[116:119], v[40:43]
v_and_b32_dpp v32, v44, v4 row_newbcast:6 row_mask:0xf bank_mask:0xf
v_and_b32_dpp v33, v44, v5 row_newbcast:6 row_mask:0xf bank_mask:0xf
v_and_b32_dpp v34, v44, v6 row_newbcast:6 row_mask:0xf bank_mask:0xf
v_and_b32_dpp v35, v44, v7 row_newbcast:6 row_mask:0xf bank_mask:0xf
v_mfma_f32_16x16x32_bf16 v[40:43], v[28:31], a[120:123], v[40:43]
s_nop 0
v_mfma_f32_16x16x32_bf16 v[40:43], v[32:35], a[124:127], v[40:43]
s_add_u32 s58, s26, 3096576
s_addc_u32 s59, s27, 0
s_waitcnt vmcnt(56)
global_load_dwordx4 a[112:115], v1, s[58:59] offset:0 nt
global_load_dwordx4 a[116:119], v1, s[58:59] offset:256 nt
global_load_dwordx4 a[120:123], v1, s[58:59] offset:512 nt
global_load_dwordx4 a[124:127], v1, s[58:59] offset:768 nt
v_mfma_f32_16x16x32_bf16 v[28:31], v[16:19], a[128:131], 0
v_mfma_f32_16x16x32_bf16 v[32:35], v[16:19], a[136:139], 0
v_mfma_f32_16x16x32_bf16 v[28:31], v[20:23], a[132:135], v[28:31]
v_mfma_f32_16x16x32_bf16 v[32:35], v[20:23], a[140:143], v[32:35]
s_add_u32 s58, s24, 3211264
s_addc_u32 s59, s25, 0
Lp237_%=:
s_memrealtime s[70:71]
s_waitcnt lgkmcnt(0)
s_sub_u32 s72, s68, s70
s_cmp_gt_i32 s72, 0
s_cbranch_scc0 Lg238_%=
s_sleep 1
s_branch Lp237_%=
Lg238_%=:
s_add_u32 s68, s68, s69
s_waitcnt vmcnt(56)
global_load_dwordx4 a[128:131], v0, s[58:59] offset:0 nt
global_load_dwordx4 a[132:135], v0, s[58:59] offset:128 nt
global_load_dwordx4 a[136:139], v0, s[58:59] offset:2048 nt
global_load_dwordx4 a[140:143], v0, s[58:59] offset:2176 nt
v_add_f32_dpp v44, v29, v28 quad_perm:[1,1,3,3] row_mask:0x3 bank_mask:0xf
s_nop 1
v_add_f32_dpp v44, v33, v32 quad_perm:[1,1,3,3] row_mask:0xc bank_mask:0xf
v_fma_f32 v45, v44, s38, v3
v_cmp_lt_f32_e32 vcc, 0, v45
s_nop 0
s_and_b64 s[48:49], vcc, s[40:41]
s_cbranch_scc0 Lc239_%=
s_swappc_b64 s[42:43], s[46:47]
Lc239_%=:
v_exp_f32_e32 v45, v45
s_nop 0
v_cndmask_b32_e64 v45, 0, v45, s[40:41]
v_add_f32_e32 v47, v47, v45
v_cvt_pk_bf16_f32 v44, v45, v45
s_nop 1
v_mov_b32_dpp v44, v44 row_ror:8 row_mask:0xa bank_mask:0xf
s_nop 1
v_and_b32_dpp v28, v44, v4 row_newbcast:0 row_mask:0xf bank_mask:0xf
v_and_b32_dpp v29, v44, v5 row_newbcast:0 row_mask:0xf bank_mask:0xf
v_and_b32_dpp v30, v44, v6 row_newbcast:0 row_mask:0xf bank_mask:0xf
v_and_b32_dpp v31, v44, v7 row_newbcast:0 row_mask:0xf bank_mask:0xf
v_and_b32_dpp v32, v44, v4 row_newbcast:2 row_mask:0xf bank_mask:0xf
v_and_b32_dpp v33, v44, v5 row_newbcast:2 row_mask:0xf bank_mask:0xf
v_and_b32_dpp v34, v44, v6 row_newbcast:2 row_mask:0xf bank_mask:0xf
v_and_b32_dpp v35, v44, v7 row_newbcast:2 row_mask:0xf bank_mask:0xf
v_mfma_f32_16x16x32_bf16 v[40:43], v[28:31], a[144:147], v[40:43]
v_and_b32_dpp v28, v44, v4 row_newbcast:4 row_mask:0xf bank_mask:0xf
v_and_b32_dpp v29, v44, v5 row_newbcast:4 row_mask:0xf bank_mask:0xf
v_and_b32_dpp v30, v44, v6 row_newbcast:4 row_mask:0xf bank_mask:0xf
v_and_b32_dpp v31, v44, v7 row_newbcast:4 row_mask:0xf bank_mask:0xf
v_mfma_f32_16x16x32_bf16 v[40:43], v[32:35], a[148:151], v[40:43]
v_and_b32_dpp v32, v44, v4 row_newbcast:6 row_mask:0xf bank_mask:0xf
v_and_b32_dpp v33, v44, v5 row_newbcast:6 row_mask:0xf bank_mask:0xf
v_and_b32_dpp v34, v44, v6 row_newbcast:6 row_mask:0xf bank_mask:0xf
v_and_b32_dpp v35, v44, v7 row_newbcast:6 row_mask:0xf bank_mask:0xf
v_mfma_f32_16x16x32_bf16 v[40:43], v[28:31], a[152:155], v[40:43]
s_nop 0
v_mfma_f32_16x16x32_bf16 v[40:43], v[32:35], a[156:159], v[40:43]
s_add_u32 s58, s26, 3211264
s_addc_u32 s59, s27, 0
s_waitcnt vmcnt(56)
global_load_dwordx4 a[144:147], v1, s[58:59] offset:0 nt
global_load_dwordx4 a[148:151], v1, s[58:59] offset:256 nt
global_load_dwordx4 a[152:155], v1, s[58:59] offset:512 nt
global_load_dwordx4 a[156:159], v1, s[58:59] offset:768 nt
v_mfma_f32_16x16x32_bf16 v[28:31], v[16:19], a[160:163], 0
v_mfma_f32_16x16x32_bf16 v[32:35], v[16:19], a[168:171], 0
v_mfma_f32_16x16x32_bf16 v[28:31], v[20:23], a[164:167], v[28:31]
v_mfma_f32_16x16x32_bf16 v[32:35], v[20:23], a[172:175], v[32:35]
s_add_u32 s58, s24, 3325952
s_addc_u32 s59, s25, 0
Lp240_%=:
s_memrealtime s[70:71]
s_waitcnt lgkmcnt(0)
s_sub_u32 s72, s68, s70
s_cmp_gt_i32 s72, 0
s_cbranch_scc0 Lg241_%=
s_sleep 1
s_branch Lp240_%=
Lg241_%=:
s_add_u32 s68, s68, s69
s_waitcnt vmcnt(56)
global_load_dwordx4 a[160:163], v0, s[58:59] offset:0 nt
global_load_dwordx4 a[164:167], v0, s[58:59] offset:128 nt
global_load_dwordx4 a[168:171], v0, s[58:59] offset:2048 nt
global_load_dwordx4 a[172:175], v0, s[58:59] offset:2176 nt
v_add_f32_dpp v44, v29, v28 quad_perm:[1,1,3,3] row_mask:0x3 bank_mask:0xf
s_nop 1
v_add_f32_dpp v44, v33, v32 quad_perm:[1,1,3,3] row_mask:0xc bank_mask:0xf
v_fma_f32 v45, v44, s38, v3
v_cmp_lt_f32_e32 vcc, 0, v45
s_nop 0
s_and_b64 s[48:49], vcc, s[40:41]
s_cbranch_scc0 Lc242_%=
s_swappc_b64 s[42:43], s[46:47]
Lc242_%=:
v_exp_f32_e32 v45, v45
s_nop 0
v_cndmask_b32_e64 v45, 0, v45, s[40:41]
v_add_f32_e32 v47, v47, v45
v_cvt_pk_bf16_f32 v44, v45, v45
s_nop 1
v_mov_b32_dpp v44, v44 row_ror:8 row_mask:0xa bank_mask:0xf
s_nop 1
v_and_b32_dpp v28, v44, v4 row_newbcast:0 row_mask:0xf bank_mask:0xf
v_and_b32_dpp v29, v44, v5 row_newbcast:0 row_mask:0xf bank_mask:0xf
v_and_b32_dpp v30, v44, v6 row_newbcast:0 row_mask:0xf bank_mask:0xf
v_and_b32_dpp v31, v44, v7 row_newbcast:0 row_mask:0xf bank_mask:0xf
v_and_b32_dpp v32, v44, v4 row_newbcast:2 row_mask:0xf bank_mask:0xf
v_and_b32_dpp v33, v44, v5 row_newbcast:2 row_mask:0xf bank_mask:0xf
v_and_b32_dpp v34, v44, v6 row_newbcast:2 row_mask:0xf bank_mask:0xf
v_and_b32_dpp v35, v44, v7 row_newbcast:2 row_mask:0xf bank_mask:0xf
v_mfma_f32_16x16x32_bf16 v[40:43], v[28:31], a[176:179], v[40:43]
v_and_b32_dpp v28, v44, v4 row_newbcast:4 row_mask:0xf bank_mask:0xf
v_and_b32_dpp v29, v44, v5 row_newbcast:4 row_mask:0xf bank_mask:0xf
v_and_b32_dpp v30, v44, v6 row_newbcast:4 row_mask:0xf bank_mask:0xf
v_and_b32_dpp v31, v44, v7 row_newbcast:4 row_mask:0xf bank_mask:0xf
v_mfma_f32_16x16x32_bf16 v[40:43], v[32:35], a[180:183], v[40:43]
v_and_b32_dpp v32, v44, v4 row_newbcast:6 row_mask:0xf bank_mask:0xf
v_and_b32_dpp v33, v44, v5 row_newbcast:6 row_mask:0xf bank_mask:0xf
v_and_b32_dpp v34, v44, v6 row_newbcast:6 row_mask:0xf bank_mask:0xf
v_and_b32_dpp v35, v44, v7 row_newbcast:6 row_mask:0xf bank_mask:0xf
v_mfma_f32_16x16x32_bf16 v[40:43], v[28:31], a[184:187], v[40:43]
s_nop 0
v_mfma_f32_16x16x32_bf16 v[40:43], v[32:35], a[188:191], v[40:43]
s_add_u32 s58, s26, 3325952
s_addc_u32 s59, s27, 0
s_waitcnt vmcnt(56)
global_load_dwordx4 a[176:179], v1, s[58:59] offset:0 nt
global_load_dwordx4 a[180:183], v1, s[58:59] offset:256 nt
global_load_dwordx4 a[184:187], v1, s[58:59] offset:512 nt
global_load_dwordx4 a[188:191], v1, s[58:59] offset:768 nt
v_mfma_f32_16x16x32_bf16 v[28:31], v[16:19], a[192:195], 0
v_mfma_f32_16x16x32_bf16 v[32:35], v[16:19], a[200:203], 0
v_mfma_f32_16x16x32_bf16 v[28:31], v[20:23], a[196:199], v[28:31]
v_mfma_f32_16x16x32_bf16 v[32:35], v[20:23], a[204:207], v[32:35]
s_add_u32 s58, s24, 3440640
s_addc_u32 s59, s25, 0
s_waitcnt vmcnt(56)
global_load_dwordx4 a[192:195], v0, s[58:59] offset:0 nt
global_load_dwordx4 a[196:199], v0, s[58:59] offset:128 nt
global_load_dwordx4 a[200:203], v0, s[58:59] offset:2048 nt
global_load_dwordx4 a[204:207], v0, s[58:59] offset:2176 nt
v_add_f32_dpp v44, v29, v28 quad_perm:[1,1,3,3] row_mask:0x3 bank_mask:0xf
s_nop 1
v_add_f32_dpp v44, v33, v32 quad_perm:[1,1,3,3] row_mask:0xc bank_mask:0xf
v_fma_f32 v45, v44, s38, v3
v_cmp_lt_f32_e32 vcc, 0, v45
s_nop 0
s_and_b64 s[48:49], vcc, s[40:41]
s_cbranch_scc0 Lc243_%=
s_swappc_b64 s[42:43], s[46:47]
Lc243_%=:
v_exp_f32_e32 v45, v45
s_nop 0
v_cndmask_b32_e64 v45, 0, v45, s[40:41]
v_add_f32_e32 v47, v47, v45
v_cvt_pk_bf16_f32 v44, v45, v45
s_nop 1
v_mov_b32_dpp v44, v44 row_ror:8 row_mask:0xa bank_mask:0xf
s_nop 1
v_and_b32_dpp v28, v44, v4 row_newbcast:0 row_mask:0xf bank_mask:0xf
v_and_b32_dpp v29, v44, v5 row_newbcast:0 row_mask:0xf bank_mask:0xf
v_and_b32_dpp v30, v44, v6 row_newbcast:0 row_mask:0xf bank_mask:0xf
v_and_b32_dpp v31, v44, v7 row_newbcast:0 row_mask:0xf bank_mask:0xf
v_and_b32_dpp v32, v44, v4 row_newbcast:2 row_mask:0xf bank_mask:0xf
v_and_b32_dpp v33, v44, v5 row_newbcast:2 row_mask:0xf bank_mask:0xf
v_and_b32_dpp v34, v44, v6 row_newbcast:2 row_mask:0xf bank_mask:0xf
v_and_b32_dpp v35, v44, v7 row_newbcast:2 row_mask:0xf bank_mask:0xf
v_mfma_f32_16x16x32_bf16 v[40:43], v[28:31], a[208:211], v[40:43]
v_and_b32_dpp v28, v44, v4 row_newbcast:4 row_mask:0xf bank_mask:0xf
v_and_b32_dpp v29, v44, v5 row_newbcast:4 row_mask:0xf bank_mask:0xf
v_and_b32_dpp v30, v44, v6 row_newbcast:4 row_mask:0xf bank_mask:0xf
v_and_b32_dpp v31, v44, v7 row_newbcast:4 row_mask:0xf bank_mask:0xf
v_mfma_f32_16x16x32_bf16 v[40:43], v[32:35], a[212:215], v[40:43]
v_and_b32_dpp v32, v44, v4 row_newbcast:6 row_mask:0xf bank_mask:0xf
v_and_b32_dpp v33, v44, v5 row_newbcast:6 row_mask:0xf bank_mask:0xf
v_and_b32_dpp v34, v44, v6 row_newbcast:6 row_mask:0xf bank_mask:0xf
v_and_b32_dpp v35, v44, v7 row_newbcast:6 row_mask:0xf bank_mask:0xf
v_mfma_f32_16x16x32_bf16 v[40:43], v[28:31], a[216:219], v[40:43]
s_nop 0
v_mfma_f32_16x16x32_bf16 v[40:43], v[32:35], a[220:223], v[40:43]
s_add_u32 s58, s26, 3440640
s_addc_u32 s59, s27, 0
s_waitcnt vmcnt(56)
global_load_dwordx4 a[208:211], v1, s[58:59] offset:0 nt
global_load_dwordx4 a[212:215], v1, s[58:59] offset:256 nt
global_load_dwordx4 a[216:219], v1, s[58:59] offset:512 nt
global_load_dwordx4 a[220:223], v1, s[58:59] offset:768 nt
v_mfma_f32_16x16x32_bf16 v[28:31], v[16:19], a[224:227], 0
v_mfma_f32_16x16x32_bf16 v[32:35], v[16:19], a[232:235], 0
v_mfma_f32_16x16x32_bf16 v[28:31], v[20:23], a[228:231], v[28:31]
v_mfma_f32_16x16x32_bf16 v[32:35], v[20:23], a[236:239], v[32:35]
s_add_u32 s58, s24, 3555328
s_addc_u32 s59, s25, 0
Lp244_%=:
s_memrealtime s[70:71]
s_waitcnt lgkmcnt(0)
s_sub_u32 s72, s68, s70
s_cmp_gt_i32 s72, 0
s_cbranch_scc0 Lg245_%=
s_sleep 1
s_branch Lp244_%=
Lg245_%=:
s_add_u32 s68, s68, s69
s_waitcnt vmcnt(56)
global_load_dwordx4 a[224:227], v0, s[58:59] offset:0 nt
global_load_dwordx4 a[228:231], v0, s[58:59] offset:128 nt
global_load_dwordx4 a[232:235], v0, s[58:59] offset:2048 nt
global_load_dwordx4 a[236:239], v0, s[58:59] offset:2176 nt
v_add_f32_dpp v44, v29, v28 quad_perm:[1,1,3,3] row_mask:0x3 bank_mask:0xf
s_nop 1
v_add_f32_dpp v44, v33, v32 quad_perm:[1,1,3,3] row_mask:0xc bank_mask:0xf
v_fma_f32 v45, v44, s38, v3
v_cmp_lt_f32_e32 vcc, 0, v45
s_nop 0
s_and_b64 s[48:49], vcc, s[40:41]
s_cbranch_scc0 Lc246_%=
s_swappc_b64 s[42:43], s[46:47]
Lc246_%=:
v_exp_f32_e32 v45, v45
s_nop 0
v_cndmask_b32_e64 v45, 0, v45, s[40:41]
v_add_f32_e32 v47, v47, v45
v_cvt_pk_bf16_f32 v44, v45, v45
s_nop 1
v_mov_b32_dpp v44, v44 row_ror:8 row_mask:0xa bank_mask:0xf
s_nop 1
v_and_b32_dpp v28, v44, v4 row_newbcast:0 row_mask:0xf bank_mask:0xf
v_and_b32_dpp v29, v44, v5 row_newbcast:0 row_mask:0xf bank_mask:0xf
v_and_b32_dpp v30, v44, v6 row_newbcast:0 row_mask:0xf bank_mask:0xf
v_and_b32_dpp v31, v44, v7 row_newbcast:0 row_mask:0xf bank_mask:0xf
v_and_b32_dpp v32, v44, v4 row_newbcast:2 row_mask:0xf bank_mask:0xf
v_and_b32_dpp v33, v44, v5 row_newbcast:2 row_mask:0xf bank_mask:0xf
v_and_b32_dpp v34, v44, v6 row_newbcast:2 row_mask:0xf bank_mask:0xf
v_and_b32_dpp v35, v44, v7 row_newbcast:2 row_mask:0xf bank_mask:0xf
v_mfma_f32_16x16x32_bf16 v[40:43], v[28:31], a[240:243], v[40:43]
v_and_b32_dpp v28, v44, v4 row_newbcast:4 row_mask:0xf bank_mask:0xf
v_and_b32_dpp v29, v44, v5 row_newbcast:4 row_mask:0xf bank_mask:0xf
v_and_b32_dpp v30, v44, v6 row_newbcast:4 row_mask:0xf bank_mask:0xf
v_and_b32_dpp v31, v44, v7 row_newbcast:4 row_mask:0xf bank_mask:0xf
v_mfma_f32_16x16x32_bf16 v[40:43], v[32:35], a[244:247], v[40:43]
v_and_b32_dpp v32, v44, v4 row_newbcast:6 row_mask:0xf bank_mask:0xf
v_and_b32_dpp v33, v44, v5 row_newbcast:6 row_mask:0xf bank_mask:0xf
v_and_b32_dpp v34, v44, v6 row_newbcast:6 row_mask:0xf bank_mask:0xf
v_and_b32_dpp v35, v44, v7 row_newbcast:6 row_mask:0xf bank_mask:0xf
v_mfma_f32_16x16x32_bf16 v[40:43], v[28:31], a[248:251], v[40:43]
s_nop 0
v_mfma_f32_16x16x32_bf16 v[40:43], v[32:35], a[252:255], v[40:43]
s_add_u32 s58, s26, 3555328
s_addc_u32 s59, s27, 0
s_waitcnt vmcnt(56)
global_load_dwordx4 a[240:243], v1, s[58:59] offset:0 nt
global_load_dwordx4 a[244:247], v1, s[58:59] offset:256 nt
global_load_dwordx4 a[248:251], v1, s[58:59] offset:512 nt
global_load_dwordx4 a[252:255], v1, s[58:59] offset:768 nt
v_mfma_f32_16x16x32_bf16 v[28:31], v[16:19], v[96:99], 0
v_mfma_f32_16x16x32_bf16 v[32:35], v[16:19], v[104:107], 0
v_mfma_f32_16x16x32_bf16 v[28:31], v[20:23], v[100:103], v[28:31]
v_mfma_f32_16x16x32_bf16 v[32:35], v[20:23], v[108:111], v[32:35]
s_nop 6
v_add_f32_dpp v44, v29, v28 quad_perm:[1,1,3,3] row_mask:0x3 bank_mask:0xf
s_nop 1
v_add_f32_dpp v44, v33, v32 quad_perm:[1,1,3,3] row_mask:0xc bank_mask:0xf
v_fma_f32 v45, v44, s38, v3
v_cmp_lt_f32_e32 vcc, 0, v45
s_nop 0
s_and_b64 s[48:49], vcc, s[40:41]
s_cbranch_scc0 Lc247_%=
s_swappc_b64 s[42:43], s[46:47]
Lc247_%=:
v_exp_f32_e32 v45, v45
s_nop 0
v_cndmask_b32_e64 v45, 0, v45, s[40:41]
v_add_f32_e32 v47, v47, v45
v_cvt_pk_bf16_f32 v44, v45, v45
s_nop 1
v_mov_b32_dpp v44, v44 row_ror:8 row_mask:0xa bank_mask:0xf
s_nop 1
v_and_b32_dpp v28, v44, v4 row_newbcast:0 row_mask:0xf bank_mask:0xf
v_and_b32_dpp v29, v44, v5 row_newbcast:0 row_mask:0xf bank_mask:0xf
v_and_b32_dpp v30, v44, v6 row_newbcast:0 row_mask:0xf bank_mask:0xf
v_and_b32_dpp v31, v44, v7 row_newbcast:0 row_mask:0xf bank_mask:0xf
v_and_b32_dpp v32, v44, v4 row_newbcast:2 row_mask:0xf bank_mask:0xf
v_and_b32_dpp v33, v44, v5 row_newbcast:2 row_mask:0xf bank_mask:0xf
v_and_b32_dpp v34, v44, v6 row_newbcast:2 row_mask:0xf bank_mask:0xf
v_and_b32_dpp v35, v44, v7 row_newbcast:2 row_mask:0xf bank_mask:0xf
v_mfma_f32_16x16x32_bf16 v[40:43], v[28:31], v[112:115], v[40:43]
v_and_b32_dpp v28, v44, v4 row_newbcast:4 row_mask:0xf bank_mask:0xf
v_and_b32_dpp v29, v44, v5 row_newbcast:4 row_mask:0xf bank_mask:0xf
v_and_b32_dpp v30, v44, v6 row_newbcast:4 row_mask:0xf bank_mask:0xf
v_and_b32_dpp v31, v44, v7 row_newbcast:4 row_mask:0xf bank_mask:0xf
v_mfma_f32_16x16x32_bf16 v[40:43], v[32:35], v[116:119], v[40:43]
v_and_b32_dpp v32, v44, v4 row_newbcast:6 row_mask:0xf bank_mask:0xf
v_and_b32_dpp v33, v44, v5 row_newbcast:6 row_mask:0xf bank_mask:0xf
v_and_b32_dpp v34, v44, v6 row_newbcast:6 row_mask:0xf bank_mask:0xf
v_and_b32_dpp v35, v44, v7 row_newbcast:6 row_mask:0xf bank_mask:0xf
v_mfma_f32_16x16x32_bf16 v[40:43], v[28:31], v[120:123], v[40:43]
s_nop 0
v_mfma_f32_16x16x32_bf16 v[40:43], v[32:35], v[124:127], v[40:43]
v_mbcnt_lo_u32_b32 v45, -1, 0
v_mbcnt_hi_u32_b32 v45, -1, v45
v_lshlrev_b32_e32 v45, 4, v45
v_add_u32_e32 v45, s57, v45
ds_read_b128 v[96:99], v45
ds_read_b128 v[100:103], v45 offset:1024
ds_read_b128 v[104:107], v45 offset:2048
ds_read_b128 v[108:111], v45 offset:3072
ds_read_b128 v[112:115], v45 offset:4096
ds_read_b128 v[116:119], v45 offset:5120
ds_read_b128 v[120:123], v45 offset:6144
ds_read_b128 v[124:127], v45 offset:7168
s_waitcnt lgkmcnt(4)
v_mfma_f32_16x16x32_bf16 v[28:31], v[16:19], v[96:99], 0
v_mfma_f32_16x16x32_bf16 v[32:35], v[16:19], v[104:107], 0
v_mfma_f32_16x16x32_bf16 v[28:31], v[20:23], v[100:103], v[28:31]
v_mfma_f32_16x16x32_bf16 v[32:35], v[20:23], v[108:111], v[32:35]
s_nop 6
v_add_f32_dpp v44, v29, v28 quad_perm:[1,1,3,3] row_mask:0x3 bank_mask:0xf
s_nop 1
v_add_f32_dpp v44, v33, v32 quad_perm:[1,1,3,3] row_mask:0xc bank_mask:0xf
v_fma_f32 v45, v44, s38, v3
v_cmp_lt_f32_e32 vcc, 0, v45
s_nop 0
s_and_b64 s[48:49], vcc, s[40:41]
s_cbranch_scc0 Lc248_%=
s_swappc_b64 s[42:43], s[46:47]
Lc248_%=:
v_exp_f32_e32 v45, v45
s_nop 0
v_cndmask_b32_e64 v45, 0, v45, s[40:41]
v_add_f32_e32 v47, v47, v45
v_cvt_pk_bf16_f32 v44, v45, v45
s_nop 1
v_mov_b32_dpp v44, v44 row_ror:8 row_mask:0xa bank_mask:0xf
s_waitcnt lgkmcnt(0)
s_nop 0
v_and_b32_dpp v28, v44, v4 row_newbcast:0 row_mask:0xf bank_mask:0xf
v_and_b32_dpp v29, v44, v5 row_newbcast:0 row_mask:0xf bank_mask:0xf
v_and_b32_dpp v30, v44, v6 row_newbcast:0 row_mask:0xf bank_mask:0xf
v_and_b32_dpp v31, v44, v7 row_newbcast:0 row_mask:0xf bank_mask:0xf
v_and_b32_dpp v32, v44, v4 row_newbcast:2 row_mask:0xf bank_mask:0xf
v_and_b32_dpp v33, v44, v5 row_newbcast:2 row_mask:0xf bank_mask:0xf
v_and_b32_dpp v34, v44, v6 row_newbcast:2 row_mask:0xf bank_mask:0xf
v_and_b32_dpp v35, v44, v7 row_newbcast:2 row_mask:0xf bank_mask:0xf
v_mfma_f32_16x16x32_bf16 v[40:43], v[28:31], v[112:115], v[40:43]
v_and_b32_dpp v28, v44, v4 row_newbcast:4 row_mask:0xf bank_mask:0xf
v_and_b32_dpp v29, v44, v5 row_newbcast:4 row_mask:0xf bank_mask:0xf
v_and_b32_dpp v30, v44, v6 row_newbcast:4 row_mask:0xf bank_mask:0xf
v_and_b32_dpp v31, v44, v7 row_newbcast:4 row_mask:0xf bank_mask:0xf
v_mfma_f32_16x16x32_bf16 v[40:43], v[32:35], v[116:119], v[40:43]
v_and_b32_dpp v32, v44, v4 row_newbcast:6 row_mask:0xf bank_mask:0xf
v_and_b32_dpp v33, v44, v5 row_newbcast:6 row_mask:0xf bank_mask:0xf
v_and_b32_dpp v34, v44, v6 row_newbcast:6 row_mask:0xf bank_mask:0xf
v_and_b32_dpp v35, v44, v7 row_newbcast:6 row_mask:0xf bank_mask:0xf
v_mfma_f32_16x16x32_bf16 v[40:43], v[28:31], v[120:123], v[40:43]
s_nop 0
v_mfma_f32_16x16x32_bf16 v[40:43], v[32:35], v[124:127], v[40:43]
v_mbcnt_lo_u32_b32 v45, -1, 0
v_mbcnt_hi_u32_b32 v45, -1, v45
v_lshlrev_b32_e32 v45, 4, v45
v_add_u32_e32 v45, s57, v45
ds_read_b128 v[96:99], v45 offset:8192
ds_read_b128 v[100:103], v45 offset:9216
ds_read_b128 v[104:107], v45 offset:10240
ds_read_b128 v[108:111], v45 offset:11264
ds_read_b128 v[112:115], v45 offset:12288
ds_read_b128 v[116:119], v45 offset:13312
ds_read_b128 v[120:123], v45 offset:14336
ds_read_b128 v[124:127], v45 offset:15360
s_waitcnt lgkmcnt(4)
v_mfma_f32_16x16x32_bf16 v[28:31], v[16:19], v[96:99], 0
v_mfma_f32_16x16x32_bf16 v[32:35], v[16:19], v[104:107], 0
v_mfma_f32_16x16x32_bf16 v[28:31], v[20:23], v[100:103], v[28:31]
v_mfma_f32_16x16x32_bf16 v[32:35], v[20:23], v[108:111], v[32:35]
s_nop 6
v_add_f32_dpp v44, v29, v28 quad_perm:[1,1,3,3] row_mask:0x3 bank_mask:0xf
s_nop 1
v_add_f32_dpp v44, v33, v32 quad_perm:[1,1,3,3] row_mask:0xc bank_mask:0xf
v_fma_f32 v45, v44, s38, v3
v_cmp_lt_f32_e32 vcc, 0, v45
s_nop 0
s_and_b64 s[48:49], vcc, s[40:41]
s_cbranch_scc0 Lc249_%=
s_swappc_b64 s[42:43], s[46:47]
Lc249_%=:
v_exp_f32_e32 v45, v45
s_nop 0
v_cndmask_b32_e64 v45, 0, v45, s[40:41]
v_add_f32_e32 v47, v47, v45
v_cvt_pk_bf16_f32 v44, v45, v45
s_nop 1
v_mov_b32_dpp v44, v44 row_ror:8 row_mask:0xa bank_mask:0xf
s_waitcnt lgkmcnt(0)
s_nop 0
v_and_b32_dpp v28, v44, v4 row_newbcast:0 row_mask:0xf bank_mask:0xf
v_and_b32_dpp v29, v44, v5 row_newbcast:0 row_mask:0xf bank_mask:0xf
v_and_b32_dpp v30, v44, v6 row_newbcast:0 row_mask:0xf bank_mask:0xf
v_and_b32_dpp v31, v44, v7 row_newbcast:0 row_mask:0xf bank_mask:0xf
v_and_b32_dpp v32, v44, v4 row_newbcast:2 row_mask:0xf bank_mask:0xf
v_and_b32_dpp v33, v44, v5 row_newbcast:2 row_mask:0xf bank_mask:0xf
v_and_b32_dpp v34, v44, v6 row_newbcast:2 row_mask:0xf bank_mask:0xf
v_and_b32_dpp v35, v44, v7 row_newbcast:2 row_mask:0xf bank_mask:0xf
v_mfma_f32_16x16x32_bf16 v[40:43], v[28:31], v[112:115], v[40:43]
v_and_b32_dpp v28, v44, v4 row_newbcast:4 row_mask:0xf bank_mask:0xf
v_and_b32_dpp v29, v44, v5 row_newbcast:4 row_mask:0xf bank_mask:0xf
v_and_b32_dpp v30, v44, v6 row_newbcast:4 row_mask:0xf bank_mask:0xf
v_and_b32_dpp v31, v44, v7 row_newbcast:4 row_mask:0xf bank_mask:0xf
v_mfma_f32_16x16x32_bf16 v[40:43], v[32:35], v[116:119], v[40:43]
v_and_b32_dpp v32, v44, v4 row_newbcast:6 row_mask:0xf bank_mask:0xf
v_and_b32_dpp v33, v44, v5 row_newbcast:6 row_mask:0xf bank_mask:0xf
v_and_b32_dpp v34, v44, v6 row_newbcast:6 row_mask:0xf bank_mask:0xf
v_and_b32_dpp v35, v44, v7 row_newbcast:6 row_mask:0xf bank_mask:0xf
v_mfma_f32_16x16x32_bf16 v[40:43], v[28:31], v[120:123], v[40:43]
s_nop 0
v_mfma_f32_16x16x32_bf16 v[40:43], v[32:35], v[124:127], v[40:43]
v_mbcnt_lo_u32_b32 v45, -1, 0
v_mbcnt_hi_u32_b32 v45, -1, v45
v_lshlrev_b32_e32 v45, 4, v45
v_add_u32_e32 v45, s57, v45
ds_read_b128 v[96:99], v45 offset:16384
ds_read_b128 v[100:103], v45 offset:17408
ds_read_b128 v[104:107], v45 offset:18432
ds_read_b128 v[108:111], v45 offset:19456
ds_read_b128 v[112:115], v45 offset:20480
ds_read_b128 v[116:119], v45 offset:21504
ds_read_b128 v[120:123], v45 offset:22528
ds_read_b128 v[124:127], v45 offset:23552
s_waitcnt lgkmcnt(4)
v_mfma_f32_16x16x32_bf16 v[28:31], v[16:19], v[96:99], 0
v_mfma_f32_16x16x32_bf16 v[32:35], v[16:19], v[104:107], 0
v_mfma_f32_16x16x32_bf16 v[28:31], v[20:23], v[100:103], v[28:31]
v_mfma_f32_16x16x32_bf16 v[32:35], v[20:23], v[108:111], v[32:35]
s_nop 6
v_add_f32_dpp v44, v29, v28 quad_perm:[1,1,3,3] row_mask:0x3 bank_mask:0xf
s_nop 1
v_add_f32_dpp v44, v33, v32 quad_perm:[1,1,3,3] row_mask:0xc bank_mask:0xf
v_fma_f32 v45, v44, s38, v3
v_cmp_lt_f32_e32 vcc, 0, v45
s_nop 0
s_and_b64 s[48:49], vcc, s[40:41]
s_cbranch_scc0 Lc250_%=
s_swappc_b64 s[42:43], s[46:47]
Lc250_%=:
v_exp_f32_e32 v45, v45
s_nop 0
v_cndmask_b32_e64 v45, 0, v45, s[40:41]
v_add_f32_e32 v47, v47, v45
v_cvt_pk_bf16_f32 v44, v45, v45
s_nop 1
v_mov_b32_dpp v44, v44 row_ror:8 row_mask:0xa bank_mask:0xf
s_waitcnt lgkmcnt(0)
s_nop 0
v_and_b32_dpp v28, v44, v4 row_newbcast:0 row_mask:0xf bank_mask:0xf
v_and_b32_dpp v29, v44, v5 row_newbcast:0 row_mask:0xf bank_mask:0xf
v_and_b32_dpp v30, v44, v6 row_newbcast:0 row_mask:0xf bank_mask:0xf
v_and_b32_dpp v31, v44, v7 row_newbcast:0 row_mask:0xf bank_mask:0xf
v_and_b32_dpp v32, v44, v4 row_newbcast:2 row_mask:0xf bank_mask:0xf
v_and_b32_dpp v33, v44, v5 row_newbcast:2 row_mask:0xf bank_mask:0xf
v_and_b32_dpp v34, v44, v6 row_newbcast:2 row_mask:0xf bank_mask:0xf
v_and_b32_dpp v35, v44, v7 row_newbcast:2 row_mask:0xf bank_mask:0xf
v_mfma_f32_16x16x32_bf16 v[40:43], v[28:31], v[112:115], v[40:43]
v_and_b32_dpp v28, v44, v4 row_newbcast:4 row_mask:0xf bank_mask:0xf
v_and_b32_dpp v29, v44, v5 row_newbcast:4 row_mask:0xf bank_mask:0xf
v_and_b32_dpp v30, v44, v6 row_newbcast:4 row_mask:0xf bank_mask:0xf
v_and_b32_dpp v31, v44, v7 row_newbcast:4 row_mask:0xf bank_mask:0xf
v_mfma_f32_16x16x32_bf16 v[40:43], v[32:35], v[116:119], v[40:43]
v_and_b32_dpp v32, v44, v4 row_newbcast:6 row_mask:0xf bank_mask:0xf
v_and_b32_dpp v33, v44, v5 row_newbcast:6 row_mask:0xf bank_mask:0xf
v_and_b32_dpp v34, v44, v6 row_newbcast:6 row_mask:0xf bank_mask:0xf
v_and_b32_dpp v35, v44, v7 row_newbcast:6 row_mask:0xf bank_mask:0xf
v_mfma_f32_16x16x32_bf16 v[40:43], v[28:31], v[120:123], v[40:43]
s_nop 0
v_mfma_f32_16x16x32_bf16 v[40:43], v[32:35], v[124:127], v[40:43]
v_mbcnt_lo_u32_b32 v45, -1, 0
v_mbcnt_hi_u32_b32 v45, -1, v45
v_lshlrev_b32_e32 v45, 4, v45
v_add_u32_e32 v45, s57, v45
ds_read_b128 v[96:99], v45 offset:24576
ds_read_b128 v[100:103], v45 offset:25600
ds_read_b128 v[104:107], v45 offset:26624
ds_read_b128 v[108:111], v45 offset:27648
ds_read_b128 v[112:115], v45 offset:28672
ds_read_b128 v[116:119], v45 offset:29696
ds_read_b128 v[120:123], v45 offset:30720
ds_read_b128 v[124:127], v45 offset:31744
s_waitcnt lgkmcnt(4)
v_mfma_f32_16x16x32_bf16 v[28:31], v[16:19], v[96:99], 0
v_mfma_f32_16x16x32_bf16 v[32:35], v[16:19], v[104:107], 0
v_mfma_f32_16x16x32_bf16 v[28:31], v[20:23], v[100:103], v[28:31]
v_mfma_f32_16x16x32_bf16 v[32:35], v[20:23], v[108:111], v[32:35]
s_nop 6
v_add_f32_dpp v44, v29, v28 quad_perm:[1,1,3,3] row_mask:0x3 bank_mask:0xf
s_nop 1
v_add_f32_dpp v44, v33, v32 quad_perm:[1,1,3,3] row_mask:0xc bank_mask:0xf
v_fma_f32 v45, v44, s38, v3
v_cmp_lt_f32_e32 vcc, 0, v45
s_nop 0
s_and_b64 s[48:49], vcc, s[40:41]
s_cbranch_scc0 Lc251_%=
s_swappc_b64 s[42:43], s[46:47]
Lc251_%=:
v_exp_f32_e32 v45, v45
s_nop 0
v_cndmask_b32_e64 v45, 0, v45, s[40:41]
v_add_f32_e32 v47, v47, v45
v_cvt_pk_bf16_f32 v44, v45, v45
s_nop 1
v_mov_b32_dpp v44, v44 row_ror:8 row_mask:0xa bank_mask:0xf
s_waitcnt lgkmcnt(0)
s_nop 0
v_and_b32_dpp v28, v44, v4 row_newbcast:0 row_mask:0xf bank_mask:0xf
v_and_b32_dpp v29, v44, v5 row_newbcast:0 row_mask:0xf bank_mask:0xf
v_and_b32_dpp v30, v44, v6 row_newbcast:0 row_mask:0xf bank_mask:0xf
v_and_b32_dpp v31, v44, v7 row_newbcast:0 row_mask:0xf bank_mask:0xf
v_and_b32_dpp v32, v44, v4 row_newbcast:2 row_mask:0xf bank_mask:0xf
v_and_b32_dpp v33, v44, v5 row_newbcast:2 row_mask:0xf bank_mask:0xf
v_and_b32_dpp v34, v44, v6 row_newbcast:2 row_mask:0xf bank_mask:0xf
v_and_b32_dpp v35, v44, v7 row_newbcast:2 row_mask:0xf bank_mask:0xf
v_mfma_f32_16x16x32_bf16 v[40:43], v[28:31], v[112:115], v[40:43]
v_and_b32_dpp v28, v44, v4 row_newbcast:4 row_mask:0xf bank_mask:0xf
v_and_b32_dpp v29, v44, v5 row_newbcast:4 row_mask:0xf bank_mask:0xf
v_and_b32_dpp v30, v44, v6 row_newbcast:4 row_mask:0xf bank_mask:0xf
v_and_b32_dpp v31, v44, v7 row_newbcast:4 row_mask:0xf bank_mask:0xf
v_mfma_f32_16x16x32_bf16 v[40:43], v[32:35], v[116:119], v[40:43]
v_and_b32_dpp v32, v44, v4 row_newbcast:6 row_mask:0xf bank_mask:0xf
v_and_b32_dpp v33, v44, v5 row_newbcast:6 row_mask:0xf bank_mask:0xf
v_and_b32_dpp v34, v44, v6 row_newbcast:6 row_mask:0xf bank_mask:0xf
v_and_b32_dpp v35, v44, v7 row_newbcast:6 row_mask:0xf bank_mask:0xf
v_mfma_f32_16x16x32_bf16 v[40:43], v[28:31], v[120:123], v[40:43]
s_nop 0
v_mfma_f32_16x16x32_bf16 v[40:43], v[32:35], v[124:127], v[40:43]
v_mbcnt_lo_u32_b32 v45, -1, 0
v_mbcnt_hi_u32_b32 v45, -1, v45
v_lshlrev_b32_e32 v45, 4, v45
v_add_u32_e32 v45, s57, v45
ds_read_b128 v[96:99], v45 offset:32768
ds_read_b128 v[100:103], v45 offset:33792
ds_read_b128 v[104:107], v45 offset:34816
ds_read_b128 v[108:111], v45 offset:35840
ds_read_b128 v[112:115], v45 offset:36864
ds_read_b128 v[116:119], v45 offset:37888
ds_read_b128 v[120:123], v45 offset:38912
s_waitcnt lgkmcnt(3)
v_mfma_f32_16x16x32_bf16 v[28:31], v[16:19], v[96:99], 0
v_mfma_f32_16x16x32_bf16 v[32:35], v[16:19], v[104:107], 0
v_mfma_f32_16x16x32_bf16 v[28:31], v[20:23], v[100:103], v[28:31]
v_mfma_f32_16x16x32_bf16 v[32:35], v[20:23], v[108:111], v[32:35]
s_nop 6
v_add_f32_dpp v44, v29, v28 quad_perm:[1,1,3,3] row_mask:0x3 bank_mask:0xf
s_nop 1
v_add_f32_dpp v44, v33, v32 quad_perm:[1,1,3,3] row_mask:0xc bank_mask:0xf
v_fma_f32 v45, v44, s38, v3
v_cmp_lt_f32_e32 vcc, 0, v45
s_nop 0
s_and_b64 s[48:49], vcc, s[40:41]
s_cbranch_scc0 Lc252_%=
s_swappc_b64 s[42:43], s[46:47]
Lc252_%=:
v_exp_f32_e32 v45, v45
s_nop 0
v_cndmask_b32_e64 v45, 0, v45, s[40:41]
v_add_f32_e32 v47, v47, v45
v_cvt_pk_bf16_f32 v44, v45, v45
s_nop 1
v_mov_b32_dpp v44, v44 row_ror:8 row_mask:0xa bank_mask:0xf
s_waitcnt lgkmcnt(0)
s_nop 0
v_and_b32_dpp v28, v44, v4 row_newbcast:0 row_mask:0xf bank_mask:0xf
v_and_b32_dpp v29, v44, v5 row_newbcast:0 row_mask:0xf bank_mask:0xf
v_and_b32_dpp v30, v44, v6 row_newbcast:0 row_mask:0xf bank_mask:0xf
v_and_b32_dpp v31, v44, v7 row_newbcast:0 row_mask:0xf bank_mask:0xf
v_and_b32_dpp v32, v44, v4 row_newbcast:2 row_mask:0xf bank_mask:0xf
v_and_b32_dpp v33, v44, v5 row_newbcast:2 row_mask:0xf bank_mask:0xf
v_and_b32_dpp v34, v44, v6 row_newbcast:2 row_mask:0xf bank_mask:0xf
v_and_b32_dpp v35, v44, v7 row_newbcast:2 row_mask:0xf bank_mask:0xf
v_mfma_f32_16x16x32_bf16 v[40:43], v[28:31], v[112:115], v[40:43]
v_and_b32_dpp v28, v44, v4 row_newbcast:4 row_mask:0xf bank_mask:0xf
v_and_b32_dpp v29, v44, v5 row_newbcast:4 row_mask:0xf bank_mask:0xf
v_and_b32_dpp v30, v44, v6 row_newbcast:4 row_mask:0xf bank_mask:0xf
v_and_b32_dpp v31, v44, v7 row_newbcast:4 row_mask:0xf bank_mask:0xf
v_mfma_f32_16x16x32_bf16 v[40:43], v[32:35], v[116:119], v[40:43]
v_and_b32_dpp v32, v44, v4 row_newbcast:6 row_mask:0xf bank_mask:0xf
v_and_b32_dpp v33, v44, v5 row_newbcast:6 row_mask:0xf bank_mask:0xf
v_and_b32_dpp v34, v44, v6 row_newbcast:6 row_mask:0xf bank_mask:0xf
v_and_b32_dpp v35, v44, v7 row_newbcast:6 row_mask:0xf bank_mask:0xf
v_mfma_f32_16x16x32_bf16 v[40:43], v[28:31], v[120:123], v[40:43]
s_nop 0
v_mfma_f32_16x16x32_bf16 v[40:43], v[32:35], v[24:27], v[40:43]
s_add_u32 s58, s24, 3670016
s_addc_u32 s59, s25, 0
Lp253_%=:
s_memrealtime s[70:71]
s_waitcnt lgkmcnt(0)
s_sub_u32 s72, s68, s70
s_cmp_gt_i32 s72, 0
s_cbranch_scc0 Lg254_%=
s_sleep 1
s_branch Lp253_%=
Lg254_%=:
s_add_u32 s68, s68, s69
s_waitcnt vmcnt(56)
global_load_dwordx4 v[96:99], v0, s[58:59] offset:0 nt
global_load_dwordx4 v[100:103], v0, s[58:59] offset:128 nt
global_load_dwordx4 v[104:107], v0, s[58:59] offset:2048 nt
global_load_dwordx4 v[108:111], v0, s[58:59] offset:2176 nt
s_add_u32 s58, s26, 3670016
s_addc_u32 s59, s27, 0
s_waitcnt vmcnt(56)
global_load_dwordx4 v[112:115], v1, s[58:59] offset:0 nt
global_load_dwordx4 v[116:119], v1, s[58:59] offset:256 nt
global_load_dwordx4 v[120:123], v1, s[58:59] offset:512 nt
global_load_dwordx4 v[124:127], v1, s[58:59] offset:768 nt
v_mfma_f32_16x16x32_bf16 v[28:31], v[16:19], v[80:83], 0
v_mfma_f32_16x16x32_bf16 v[32:35], v[16:19], v[88:91], 0
v_mfma_f32_16x16x32_bf16 v[28:31], v[20:23], v[84:87], v[28:31]
v_mfma_f32_16x16x32_bf16 v[32:35], v[20:23], v[92:95], v[32:35]
s_add_u32 s58, s26, 2179072
s_addc_u32 s59, s27, 0
s_waitcnt vmcnt(56)
global_load_dwordx4 v[80:83], v1, s[58:59] offset:0 nt
global_load_dwordx4 v[84:87], v1, s[58:59] offset:256 nt
global_load_dwordx4 v[88:91], v1, s[58:59] offset:512 nt
global_load_dwordx4 v[92:95], v1, s[58:59] offset:768 nt
v_add_f32_dpp v44, v29, v28 quad_perm:[1,1,3,3] row_mask:0x3 bank_mask:0xf
s_nop 1
v_add_f32_dpp v44, v33, v32 quad_perm:[1,1,3,3] row_mask:0xc bank_mask:0xf
v_fma_f32 v45, v44, s38, v3
v_cmp_lt_f32_e32 vcc, 0, v45
s_nop 0
s_and_b64 s[48:49], vcc, s[40:41]
s_cbranch_scc0 Lc255_%=
s_swappc_b64 s[42:43], s[46:47]
Lc255_%=:
v_exp_f32_e32 v45, v45
s_nop 0
v_cndmask_b32_e64 v45, 0, v45, s[40:41]
v_add_f32_e32 v47, v47, v45
v_cvt_pk_bf16_f32 v44, v45, v45
s_nop 1
v_mov_b32_dpp v44, v44 row_ror:8 row_mask:0xa bank_mask:0xf
s_nop 1
v_and_b32_dpp v28, v44, v4 row_newbcast:0 row_mask:0xf bank_mask:0xf
v_and_b32_dpp v29, v44, v5 row_newbcast:0 row_mask:0xf bank_mask:0xf
v_and_b32_dpp v30, v44, v6 row_newbcast:0 row_mask:0xf bank_mask:0xf
v_and_b32_dpp v31, v44, v7 row_newbcast:0 row_mask:0xf bank_mask:0xf
v_and_b32_dpp v32, v44, v4 row_newbcast:2 row_mask:0xf bank_mask:0xf
v_and_b32_dpp v33, v44, v5 row_newbcast:2 row_mask:0xf bank_mask:0xf
v_and_b32_dpp v34, v44, v6 row_newbcast:2 row_mask:0xf bank_mask:0xf
v_and_b32_dpp v35, v44, v7 row_newbcast:2 row_mask:0xf bank_mask:0xf
v_mfma_f32_16x16x32_bf16 v[40:43], v[28:31], v[48:51], v[40:43]
v_and_b32_dpp v28, v44, v4 row_newbcast:4 row_mask:0xf bank_mask:0xf
v_and_b32_dpp v29, v44, v5 row_newbcast:4 row_mask:0xf bank_mask:0xf
v_and_b32_dpp v30, v44, v6 row_newbcast:4 row_mask:0xf bank_mask:0xf
v_and_b32_dpp v31, v44, v7 row_newbcast:4 row_mask:0xf bank_mask:0xf
v_mfma_f32_16x16x32_bf16 v[40:43], v[32:35], v[52:55], v[40:43]
v_and_b32_dpp v32, v44, v4 row_newbcast:6 row_mask:0xf bank_mask:0xf
v_and_b32_dpp v33, v44, v5 row_newbcast:6 row_mask:0xf bank_mask:0xf
v_and_b32_dpp v34, v44, v6 row_newbcast:6 row_mask:0xf bank_mask:0xf
v_and_b32_dpp v35, v44, v7 row_newbcast:6 row_mask:0xf bank_mask:0xf
v_mfma_f32_16x16x32_bf16 v[40:43], v[28:31], v[56:59], v[40:43]
s_nop 0
v_mfma_f32_16x16x32_bf16 v[40:43], v[32:35], v[60:63], v[40:43]
v_mfma_f32_16x16x32_bf16 v[28:31], v[16:19], v[128:131], 0
v_mfma_f32_16x16x32_bf16 v[32:35], v[16:19], v[136:139], 0
v_mfma_f32_16x16x32_bf16 v[28:31], v[20:23], v[132:135], v[28:31]
v_mfma_f32_16x16x32_bf16 v[32:35], v[20:23], v[140:143], v[32:35]
s_add_u32 s58, s24, 3784704
s_addc_u32 s59, s25, 0
s_waitcnt vmcnt(56)
global_load_dwordx4 v[128:131], v0, s[58:59] offset:0 nt
global_load_dwordx4 v[132:135], v0, s[58:59] offset:128 nt
global_load_dwordx4 v[136:139], v0, s[58:59] offset:2048 nt
global_load_dwordx4 v[140:143], v0, s[58:59] offset:2176 nt
v_add_f32_dpp v44, v29, v28 quad_perm:[1,1,3,3] row_mask:0x3 bank_mask:0xf
s_nop 1
v_add_f32_dpp v44, v33, v32 quad_perm:[1,1,3,3] row_mask:0xc bank_mask:0xf
v_fma_f32 v45, v44, s38, v3
v_cmp_lt_f32_e32 vcc, 0, v45
s_nop 0
s_and_b64 s[48:49], vcc, s[40:41]
s_cbranch_scc0 Lc256_%=
s_swappc_b64 s[42:43], s[46:47]
Lc256_%=:
v_exp_f32_e32 v45, v45
s_nop 0
v_cndmask_b32_e64 v45, 0, v45, s[40:41]
v_add_f32_e32 v47, v47, v45
v_cvt_pk_bf16_f32 v44, v45, v45
s_nop 1
v_mov_b32_dpp v44, v44 row_ror:8 row_mask:0xa bank_mask:0xf
s_nop 1
v_and_b32_dpp v28, v44, v4 row_newbcast:0 row_mask:0xf bank_mask:0xf
v_and_b32_dpp v29, v44, v5 row_newbcast:0 row_mask:0xf bank_mask:0xf
v_and_b32_dpp v30, v44, v6 row_newbcast:0 row_mask:0xf bank_mask:0xf
v_and_b32_dpp v31, v44, v7 row_newbcast:0 row_mask:0xf bank_mask:0xf
v_and_b32_dpp v32, v44, v4 row_newbcast:2 row_mask:0xf bank_mask:0xf
v_and_b32_dpp v33, v44, v5 row_newbcast:2 row_mask:0xf bank_mask:0xf
v_and_b32_dpp v34, v44, v6 row_newbcast:2 row_mask:0xf bank_mask:0xf
v_and_b32_dpp v35, v44, v7 row_newbcast:2 row_mask:0xf bank_mask:0xf
v_mfma_f32_16x16x32_bf16 v[40:43], v[28:31], v[144:147], v[40:43]
v_and_b32_dpp v28, v44, v4 row_newbcast:4 row_mask:0xf bank_mask:0xf
v_and_b32_dpp v29, v44, v5 row_newbcast:4 row_mask:0xf bank_mask:0xf
v_and_b32_dpp v30, v44, v6 row_newbcast:4 row_mask:0xf bank_mask:0xf
v_and_b32_dpp v31, v44, v7 row_newbcast:4 row_mask:0xf bank_mask:0xf
v_mfma_f32_16x16x32_bf16 v[40:43], v[32:35], v[148:151], v[40:43]
v_and_b32_dpp v32, v44, v4 row_newbcast:6 row_mask:0xf bank_mask:0xf
v_and_b32_dpp v33, v44, v5 row_newbcast:6 row_mask:0xf bank_mask:0xf
v_and_b32_dpp v34, v44, v6 row_newbcast:6 row_mask:0xf bank_mask:0xf
v_and_b32_dpp v35, v44, v7 row_newbcast:6 row_mask:0xf bank_mask:0xf
v_mfma_f32_16x16x32_bf16 v[40:43], v[28:31], v[152:155], v[40:43]
s_nop 0
v_mfma_f32_16x16x32_bf16 v[40:43], v[32:35], v[156:159], v[40:43]
s_add_u32 s58, s26, 3784704
s_addc_u32 s59, s27, 0
s_waitcnt vmcnt(56)
global_load_dwordx4 v[144:147], v1, s[58:59] offset:0 nt
global_load_dwordx4 v[148:151], v1, s[58:59] offset:256 nt
global_load_dwordx4 v[152:155], v1, s[58:59] offset:512 nt
global_load_dwordx4 v[156:159], v1, s[58:59] offset:768 nt
v_mfma_f32_16x16x32_bf16 v[28:31], v[16:19], v[160:163], 0
v_mfma_f32_16x16x32_bf16 v[32:35], v[16:19], v[168:171], 0
v_mfma_f32_16x16x32_bf16 v[28:31], v[20:23], v[164:167], v[28:31]
v_mfma_f32_16x16x32_bf16 v[32:35], v[20:23], v[172:175], v[32:35]
s_add_u32 s58, s24, 3899392
s_addc_u32 s59, s25, 0
Lp257_%=:
s_memrealtime s[70:71]
s_waitcnt lgkmcnt(0)
s_sub_u32 s72, s68, s70
s_cmp_gt_i32 s72, 0
s_cbranch_scc0 Lg258_%=
s_sleep 1
s_branch Lp257_%=
Lg258_%=:
s_add_u32 s68, s68, s69
s_waitcnt vmcnt(56)
global_load_dwordx4 v[160:163], v0, s[58:59] offset:0 nt
global_load_dwordx4 v[164:167], v0, s[58:59] offset:128 nt
global_load_dwordx4 v[168:171], v0, s[58:59] offset:2048 nt
global_load_dwordx4 v[172:175], v0, s[58:59] offset:2176 nt
v_add_f32_dpp v44, v29, v28 quad_perm:[1,1,3,3] row_mask:0x3 bank_mask:0xf
s_nop 1
v_add_f32_dpp v44, v33, v32 quad_perm:[1,1,3,3] row_mask:0xc bank_mask:0xf
v_fma_f32 v45, v44, s38, v3
v_cmp_lt_f32_e32 vcc, 0, v45
s_nop 0
s_and_b64 s[48:49], vcc, s[40:41]
s_cbranch_scc0 Lc259_%=
s_swappc_b64 s[42:43], s[46:47]
Lc259_%=:
v_exp_f32_e32 v45, v45
s_nop 0
v_cndmask_b32_e64 v45, 0, v45, s[40:41]
v_add_f32_e32 v47, v47, v45
v_cvt_pk_bf16_f32 v44, v45, v45
s_nop 1
v_mov_b32_dpp v44, v44 row_ror:8 row_mask:0xa bank_mask:0xf
s_nop 1
v_and_b32_dpp v28, v44, v4 row_newbcast:0 row_mask:0xf bank_mask:0xf
v_and_b32_dpp v29, v44, v5 row_newbcast:0 row_mask:0xf bank_mask:0xf
v_and_b32_dpp v30, v44, v6 row_newbcast:0 row_mask:0xf bank_mask:0xf
v_and_b32_dpp v31, v44, v7 row_newbcast:0 row_mask:0xf bank_mask:0xf
v_and_b32_dpp v32, v44, v4 row_newbcast:2 row_mask:0xf bank_mask:0xf
v_and_b32_dpp v33, v44, v5 row_newbcast:2 row_mask:0xf bank_mask:0xf
v_and_b32_dpp v34, v44, v6 row_newbcast:2 row_mask:0xf bank_mask:0xf
v_and_b32_dpp v35, v44, v7 row_newbcast:2 row_mask:0xf bank_mask:0xf
v_mfma_f32_16x16x32_bf16 v[40:43], v[28:31], v[176:179], v[40:43]
v_and_b32_dpp v28, v44, v4 row_newbcast:4 row_mask:0xf bank_mask:0xf
v_and_b32_dpp v29, v44, v5 row_newbcast:4 row_mask:0xf bank_mask:0xf
v_and_b32_dpp v30, v44, v6 row_newbcast:4 row_mask:0xf bank_mask:0xf
v_and_b32_dpp v31, v44, v7 row_newbcast:4 row_mask:0xf bank_mask:0xf
v_mfma_f32_16x16x32_bf16 v[40:43], v[32:35], v[180:183], v[40:43]
v_and_b32_dpp v32, v44, v4 row_newbcast:6 row_mask:0xf bank_mask:0xf
v_and_b32_dpp v33, v44, v5 row_newbcast:6 row_mask:0xf bank_mask:0xf
v_and_b32_dpp v34, v44, v6 row_newbcast:6 row_mask:0xf bank_mask:0xf
v_and_b32_dpp v35, v44, v7 row_newbcast:6 row_mask:0xf bank_mask:0xf
v_mfma_f32_16x16x32_bf16 v[40:43], v[28:31], v[184:187], v[40:43]
s_nop 0
v_mfma_f32_16x16x32_bf16 v[40:43], v[32:35], v[188:191], v[40:43]
s_add_u32 s58, s26, 3899392
s_addc_u32 s59, s27, 0
s_waitcnt vmcnt(56)
global_load_dwordx4 v[176:179], v1, s[58:59] offset:0 nt
global_load_dwordx4 v[180:183], v1, s[58:59] offset:256 nt
global_load_dwordx4 v[184:187], v1, s[58:59] offset:512 nt
global_load_dwordx4 v[188:191], v1, s[58:59] offset:768 nt
v_mfma_f32_16x16x32_bf16 v[28:31], v[16:19], v[192:195], 0
v_mfma_f32_16x16x32_bf16 v[32:35], v[16:19], v[200:203], 0
v_mfma_f32_16x16x32_bf16 v[28:31], v[20:23], v[196:199], v[28:31]
v_mfma_f32_16x16x32_bf16 v[32:35], v[20:23], v[204:207], v[32:35]
s_add_u32 s58, s24, 4014080
s_addc_u32 s59, s25, 0
Lp260_%=:
s_memrealtime s[70:71]
s_waitcnt lgkmcnt(0)
s_sub_u32 s72, s68, s70
s_cmp_gt_i32 s72, 0
s_cbranch_scc0 Lg261_%=
s_sleep 1
s_branch Lp260_%=
Lg261_%=:
s_add_u32 s68, s68, s69
s_waitcnt vmcnt(56)
global_load_dwordx4 v[192:195], v0, s[58:59] offset:0 nt
global_load_dwordx4 v[196:199], v0, s[58:59] offset:128 nt
global_load_dwordx4 v[200:203], v0, s[58:59] offset:2048 nt
global_load_dwordx4 v[204:207], v0, s[58:59] offset:2176 nt
v_add_f32_dpp v44, v29, v28 quad_perm:[1,1,3,3] row_mask:0x3 bank_mask:0xf
s_nop 1
v_add_f32_dpp v44, v33, v32 quad_perm:[1,1,3,3] row_mask:0xc bank_mask:0xf
v_fma_f32 v45, v44, s38, v3
v_cmp_lt_f32_e32 vcc, 0, v45
s_nop 0
s_and_b64 s[48:49], vcc, s[40:41]
s_cbranch_scc0 Lc262_%=
s_swappc_b64 s[42:43], s[46:47]
Lc262_%=:
v_exp_f32_e32 v45, v45
s_nop 0
v_cndmask_b32_e64 v45, 0, v45, s[40:41]
v_add_f32_e32 v47, v47, v45
v_cvt_pk_bf16_f32 v44, v45, v45
s_nop 1
v_mov_b32_dpp v44, v44 row_ror:8 row_mask:0xa bank_mask:0xf
s_nop 1
v_and_b32_dpp v28, v44, v4 row_newbcast:0 row_mask:0xf bank_mask:0xf
v_and_b32_dpp v29, v44, v5 row_newbcast:0 row_mask:0xf bank_mask:0xf
v_and_b32_dpp v30, v44, v6 row_newbcast:0 row_mask:0xf bank_mask:0xf
v_and_b32_dpp v31, v44, v7 row_newbcast:0 row_mask:0xf bank_mask:0xf
v_and_b32_dpp v32, v44, v4 row_newbcast:2 row_mask:0xf bank_mask:0xf
v_and_b32_dpp v33, v44, v5 row_newbcast:2 row_mask:0xf bank_mask:0xf
v_and_b32_dpp v34, v44, v6 row_newbcast:2 row_mask:0xf bank_mask:0xf
v_and_b32_dpp v35, v44, v7 row_newbcast:2 row_mask:0xf bank_mask:0xf
v_mfma_f32_16x16x32_bf16 v[40:43], v[28:31], v[208:211], v[40:43]
v_and_b32_dpp v28, v44, v4 row_newbcast:4 row_mask:0xf bank_mask:0xf
v_and_b32_dpp v29, v44, v5 row_newbcast:4 row_mask:0xf bank_mask:0xf
v_and_b32_dpp v30, v44, v6 row_newbcast:4 row_mask:0xf bank_mask:0xf
v_and_b32_dpp v31, v44, v7 row_newbcast:4 row_mask:0xf bank_mask:0xf
v_mfma_f32_16x16x32_bf16 v[40:43], v[32:35], v[212:215], v[40:43]
v_and_b32_dpp v32, v44, v4 row_newbcast:6 row_mask:0xf bank_mask:0xf
v_and_b32_dpp v33, v44, v5 row_newbcast:6 row_mask:0xf bank_mask:0xf
v_and_b32_dpp v34, v44, v6 row_newbcast:6 row_mask:0xf bank_mask:0xf
v_and_b32_dpp v35, v44, v7 row_newbcast:6 row_mask:0xf bank_mask:0xf
v_mfma_f32_16x16x32_bf16 v[40:43], v[28:31], v[216:219], v[40:43]
s_nop 0
v_mfma_f32_16x16x32_bf16 v[40:43], v[32:35], v[220:223], v[40:43]
s_add_u32 s58, s26, 4014080
s_addc_u32 s59, s27, 0
s_waitcnt vmcnt(56)
global_load_dwordx4 v[208:211], v1, s[58:59] offset:0 nt
global_load_dwordx4 v[212:215], v1, s[58:59] offset:256 nt
global_load_dwordx4 v[216:219], v1, s[58:59] offset:512 nt
global_load_dwordx4 v[220:223], v1, s[58:59] offset:768 nt
v_mfma_f32_16x16x32_bf16 v[28:31], v[16:19], v[224:227], 0
v_mfma_f32_16x16x32_bf16 v[32:35], v[16:19], v[232:235], 0
v_mfma_f32_16x16x32_bf16 v[28:31], v[20:23], v[228:231], v[28:31]
v_mfma_f32_16x16x32_bf16 v[32:35], v[20:23], v[236:239], v[32:35]
s_nop 6
v_add_f32_dpp v44, v29, v28 quad_perm:[1,1,3,3] row_mask:0x3 bank_mask:0xf
s_nop 1
v_add_f32_dpp v44, v33, v32 quad_perm:[1,1,3,3] row_mask:0xc bank_mask:0xf
v_fma_f32 v45, v44, s38, v3
v_cmp_lt_f32_e32 vcc, 0, v45
s_nop 0
s_and_b64 s[48:49], vcc, s[40:41]
s_cbranch_scc0 Lc263_%=
s_swappc_b64 s[42:43], s[46:47]
Lc263_%=:
v_exp_f32_e32 v45, v45
s_nop 0
v_cndmask_b32_e64 v45, 0, v45, s[40:41]
v_add_f32_e32 v47, v47, v45
v_cvt_pk_bf16_f32 v44, v45, v45
s_nop 1
v_mov_b32_dpp v44, v44 row_ror:8 row_mask:0xa bank_mask:0xf
s_nop 1
v_and_b32_dpp v28, v44, v4 row_newbcast:0 row_mask:0xf bank_mask:0xf
v_and_b32_dpp v29, v44, v5 row_newbcast:0 row_mask:0xf bank_mask:0xf
v_and_b32_dpp v30, v44, v6 row_newbcast:0 row_mask:0xf bank_mask:0xf
v_and_b32_dpp v31, v44, v7 row_newbcast:0 row_mask:0xf bank_mask:0xf
v_and_b32_dpp v32, v44, v4 row_newbcast:2 row_mask:0xf bank_mask:0xf
v_and_b32_dpp v33, v44, v5 row_newbcast:2 row_mask:0xf bank_mask:0xf
v_and_b32_dpp v34, v44, v6 row_newbcast:2 row_mask:0xf bank_mask:0xf
v_and_b32_dpp v35, v44, v7 row_newbcast:2 row_mask:0xf bank_mask:0xf
v_mfma_f32_16x16x32_bf16 v[40:43], v[28:31], v[240:243], v[40:43]
v_and_b32_dpp v28, v44, v4 row_newbcast:4 row_mask:0xf bank_mask:0xf
v_and_b32_dpp v29, v44, v5 row_newbcast:4 row_mask:0xf bank_mask:0xf
v_and_b32_dpp v30, v44, v6 row_newbcast:4 row_mask:0xf bank_mask:0xf
v_and_b32_dpp v31, v44, v7 row_newbcast:4 row_mask:0xf bank_mask:0xf
v_mfma_f32_16x16x32_bf16 v[40:43], v[32:35], v[244:247], v[40:43]
v_and_b32_dpp v32, v44, v4 row_newbcast:6 row_mask:0xf bank_mask:0xf
v_and_b32_dpp v33, v44, v5 row_newbcast:6 row_mask:0xf bank_mask:0xf
v_and_b32_dpp v34, v44, v6 row_newbcast:6 row_mask:0xf bank_mask:0xf
v_and_b32_dpp v35, v44, v7 row_newbcast:6 row_mask:0xf bank_mask:0xf
v_mfma_f32_16x16x32_bf16 v[40:43], v[28:31], v[248:251], v[40:43]
s_nop 0
v_mfma_f32_16x16x32_bf16 v[40:43], v[32:35], v[252:255], v[40:43]
v_mfma_f32_16x16x32_bf16 v[28:31], v[16:19], a[0:3], 0
v_mfma_f32_16x16x32_bf16 v[32:35], v[16:19], a[8:11], 0
v_mfma_f32_16x16x32_bf16 v[28:31], v[20:23], a[4:7], v[28:31]
v_mfma_f32_16x16x32_bf16 v[32:35], v[20:23], a[12:15], v[32:35]
s_nop 6
v_add_f32_dpp v44, v29, v28 quad_perm:[1,1,3,3] row_mask:0x3 bank_mask:0xf
s_nop 1
v_add_f32_dpp v44, v33, v32 quad_perm:[1,1,3,3] row_mask:0xc bank_mask:0xf
v_fma_f32 v45, v44, s38, v3
v_cmp_lt_f32_e32 vcc, 0, v45
s_nop 0
s_and_b64 s[48:49], vcc, s[40:41]
s_cbranch_scc0 Lc264_%=
s_swappc_b64 s[42:43], s[46:47]
Lc264_%=:
v_exp_f32_e32 v45, v45
s_nop 0
v_cndmask_b32_e64 v45, 0, v45, s[40:41]
v_add_f32_e32 v47, v47, v45
v_cvt_pk_bf16_f32 v44, v45, v45
s_nop 1
v_mov_b32_dpp v44, v44 row_ror:8 row_mask:0xa bank_mask:0xf
s_nop 1
v_and_b32_dpp v28, v44, v4 row_newbcast:0 row_mask:0xf bank_mask:0xf
v_and_b32_dpp v29, v44, v5 row_newbcast:0 row_mask:0xf bank_mask:0xf
v_and_b32_dpp v30, v44, v6 row_newbcast:0 row_mask:0xf bank_mask:0xf
v_and_b32_dpp v31, v44, v7 row_newbcast:0 row_mask:0xf bank_mask:0xf
v_and_b32_dpp v32, v44, v4 row_newbcast:2 row_mask:0xf bank_mask:0xf
v_and_b32_dpp v33, v44, v5 row_newbcast:2 row_mask:0xf bank_mask:0xf
v_and_b32_dpp v34, v44, v6 row_newbcast:2 row_mask:0xf bank_mask:0xf
v_and_b32_dpp v35, v44, v7 row_newbcast:2 row_mask:0xf bank_mask:0xf
v_mfma_f32_16x16x32_bf16 v[40:43], v[28:31], a[16:19], v[40:43]
v_and_b32_dpp v28, v44, v4 row_newbcast:4 row_mask:0xf bank_mask:0xf
v_and_b32_dpp v29, v44, v5 row_newbcast:4 row_mask:0xf bank_mask:0xf
v_and_b32_dpp v30, v44, v6 row_newbcast:4 row_mask:0xf bank_mask:0xf
v_and_b32_dpp v31, v44, v7 row_newbcast:4 row_mask:0xf bank_mask:0xf
v_mfma_f32_16x16x32_bf16 v[40:43], v[32:35], a[20:23], v[40:43]
v_and_b32_dpp v32, v44, v4 row_newbcast:6 row_mask:0xf bank_mask:0xf
v_and_b32_dpp v33, v44, v5 row_newbcast:6 row_mask:0xf bank_mask:0xf
v_and_b32_dpp v34, v44, v6 row_newbcast:6 row_mask:0xf bank_mask:0xf
v_and_b32_dpp v35, v44, v7 row_newbcast:6 row_mask:0xf bank_mask:0xf
v_mfma_f32_16x16x32_bf16 v[40:43], v[28:31], a[24:27], v[40:43]
s_nop 0
v_mfma_f32_16x16x32_bf16 v[40:43], v[32:35], a[28:31], v[40:43]
v_mfma_f32_16x16x32_bf16 v[28:31], v[16:19], a[32:35], 0
v_mfma_f32_16x16x32_bf16 v[32:35], v[16:19], a[40:43], 0
v_mfma_f32_16x16x32_bf16 v[28:31], v[20:23], a[36:39], v[28:31]
v_mfma_f32_16x16x32_bf16 v[32:35], v[20:23], a[44:47], v[32:35]
s_nop 6
v_add_f32_dpp v44, v29, v28 quad_perm:[1,1,3,3] row_mask:0x3 bank_mask:0xf
s_nop 1
v_add_f32_dpp v44, v33, v32 quad_perm:[1,1,3,3] row_mask:0xc bank_mask:0xf
v_fma_f32 v45, v44, s38, v3
v_cmp_lt_f32_e32 vcc, 0, v45
s_nop 0
s_and_b64 s[48:49], vcc, s[40:41]
s_cbranch_scc0 Lc265_%=
s_swappc_b64 s[42:43], s[46:47]
Lc265_%=:
v_exp_f32_e32 v45, v45
s_nop 0
v_cndmask_b32_e64 v45, 0, v45, s[40:41]
v_add_f32_e32 v47, v47, v45
v_cvt_pk_bf16_f32 v44, v45, v45
s_nop 1
v_mov_b32_dpp v44, v44 row_ror:8 row_mask:0xa bank_mask:0xf
s_nop 1
v_and_b32_dpp v28, v44, v4 row_newbcast:0 row_mask:0xf bank_mask:0xf
v_and_b32_dpp v29, v44, v5 row_newbcast:0 row_mask:0xf bank_mask:0xf
v_and_b32_dpp v30, v44, v6 row_newbcast:0 row_mask:0xf bank_mask:0xf
v_and_b32_dpp v31, v44, v7 row_newbcast:0 row_mask:0xf bank_mask:0xf
v_and_b32_dpp v32, v44, v4 row_newbcast:2 row_mask:0xf bank_mask:0xf
v_and_b32_dpp v33, v44, v5 row_newbcast:2 row_mask:0xf bank_mask:0xf
v_and_b32_dpp v34, v44, v6 row_newbcast:2 row_mask:0xf bank_mask:0xf
v_and_b32_dpp v35, v44, v7 row_newbcast:2 row_mask:0xf bank_mask:0xf
v_mfma_f32_16x16x32_bf16 v[40:43], v[28:31], a[48:51], v[40:43]
v_and_b32_dpp v28, v44, v4 row_newbcast:4 row_mask:0xf bank_mask:0xf
v_and_b32_dpp v29, v44, v5 row_newbcast:4 row_mask:0xf bank_mask:0xf
v_and_b32_dpp v30, v44, v6 row_newbcast:4 row_mask:0xf bank_mask:0xf
v_and_b32_dpp v31, v44, v7 row_newbcast:4 row_mask:0xf bank_mask:0xf
v_mfma_f32_16x16x32_bf16 v[40:43], v[32:35], a[52:55], v[40:43]
v_and_b32_dpp v32, v44, v4 row_newbcast:6 row_mask:0xf bank_mask:0xf
v_and_b32_dpp v33, v44, v5 row_newbcast:6 row_mask:0xf bank_mask:0xf
v_and_b32_dpp v34, v44, v6 row_newbcast:6 row_mask:0xf bank_mask:0xf
v_and_b32_dpp v35, v44, v7 row_newbcast:6 row_mask:0xf bank_mask:0xf
v_mfma_f32_16x16x32_bf16 v[40:43], v[28:31], a[56:59], v[40:43]
s_nop 0
v_mfma_f32_16x16x32_bf16 v[40:43], v[32:35], a[60:63], v[40:43]
v_mfma_f32_16x16x32_bf16 v[28:31], v[16:19], a[64:67], 0
v_mfma_f32_16x16x32_bf16 v[32:35], v[16:19], a[72:75], 0
v_mfma_f32_16x16x32_bf16 v[28:31], v[20:23], a[68:71], v[28:31]
v_mfma_f32_16x16x32_bf16 v[32:35], v[20:23], a[76:79], v[32:35]
s_nop 6
v_add_f32_dpp v44, v29, v28 quad_perm:[1,1,3,3] row_mask:0x3 bank_mask:0xf
s_nop 1
v_add_f32_dpp v44, v33, v32 quad_perm:[1,1,3,3] row_mask:0xc bank_mask:0xf
v_fma_f32 v45, v44, s38, v3
v_cmp_lt_f32_e32 vcc, 0, v45
s_nop 0
s_and_b64 s[48:49], vcc, s[40:41]
s_cbranch_scc0 Lc266_%=
s_swappc_b64 s[42:43], s[46:47]
Lc266_%=:
v_exp_f32_e32 v45, v45
s_nop 0
v_cndmask_b32_e64 v45, 0, v45, s[40:41]
v_add_f32_e32 v47, v47, v45
v_cvt_pk_bf16_f32 v44, v45, v45
s_nop 1
v_mov_b32_dpp v44, v44 row_ror:8 row_mask:0xa bank_mask:0xf
s_nop 1
v_and_b32_dpp v28, v44, v4 row_newbcast:0 row_mask:0xf bank_mask:0xf
v_and_b32_dpp v29, v44, v5 row_newbcast:0 row_mask:0xf bank_mask:0xf
v_and_b32_dpp v30, v44, v6 row_newbcast:0 row_mask:0xf bank_mask:0xf
v_and_b32_dpp v31, v44, v7 row_newbcast:0 row_mask:0xf bank_mask:0xf
v_and_b32_dpp v32, v44, v4 row_newbcast:2 row_mask:0xf bank_mask:0xf
v_and_b32_dpp v33, v44, v5 row_newbcast:2 row_mask:0xf bank_mask:0xf
v_and_b32_dpp v34, v44, v6 row_newbcast:2 row_mask:0xf bank_mask:0xf
v_and_b32_dpp v35, v44, v7 row_newbcast:2 row_mask:0xf bank_mask:0xf
v_mfma_f32_16x16x32_bf16 v[40:43], v[28:31], a[80:83], v[40:43]
v_and_b32_dpp v28, v44, v4 row_newbcast:4 row_mask:0xf bank_mask:0xf
v_and_b32_dpp v29, v44, v5 row_newbcast:4 row_mask:0xf bank_mask:0xf
v_and_b32_dpp v30, v44, v6 row_newbcast:4 row_mask:0xf bank_mask:0xf
v_and_b32_dpp v31, v44, v7 row_newbcast:4 row_mask:0xf bank_mask:0xf
v_mfma_f32_16x16x32_bf16 v[40:43], v[32:35], a[84:87], v[40:43]
v_and_b32_dpp v32, v44, v4 row_newbcast:6 row_mask:0xf bank_mask:0xf
v_and_b32_dpp v33, v44, v5 row_newbcast:6 row_mask:0xf bank_mask:0xf
v_and_b32_dpp v34, v44, v6 row_newbcast:6 row_mask:0xf bank_mask:0xf
v_and_b32_dpp v35, v44, v7 row_newbcast:6 row_mask:0xf bank_mask:0xf
v_mfma_f32_16x16x32_bf16 v[40:43], v[28:31], a[88:91], v[40:43]
s_nop 0
v_mfma_f32_16x16x32_bf16 v[40:43], v[32:35], a[92:95], v[40:43]
v_mfma_f32_16x16x32_bf16 v[28:31], v[16:19], a[96:99], 0
v_mfma_f32_16x16x32_bf16 v[32:35], v[16:19], a[104:107], 0
v_mfma_f32_16x16x32_bf16 v[28:31], v[20:23], a[100:103], v[28:31]
v_mfma_f32_16x16x32_bf16 v[32:35], v[20:23], a[108:111], v[32:35]
s_nop 6
v_add_f32_dpp v44, v29, v28 quad_perm:[1,1,3,3] row_mask:0x3 bank_mask:0xf
s_nop 1
v_add_f32_dpp v44, v33, v32 quad_perm:[1,1,3,3] row_mask:0xc bank_mask:0xf
v_fma_f32 v45, v44, s38, v3
v_cmp_lt_f32_e32 vcc, 0, v45
s_nop 0
s_and_b64 s[48:49], vcc, s[40:41]
s_cbranch_scc0 Lc267_%=
s_swappc_b64 s[42:43], s[46:47]
Lc267_%=:
v_exp_f32_e32 v45, v45
s_nop 0
v_cndmask_b32_e64 v45, 0, v45, s[40:41]
v_add_f32_e32 v47, v47, v45
v_cvt_pk_bf16_f32 v44, v45, v45
s_nop 1
v_mov_b32_dpp v44, v44 row_ror:8 row_mask:0xa bank_mask:0xf
s_nop 1
v_and_b32_dpp v28, v44, v4 row_newbcast:0 row_mask:0xf bank_mask:0xf
v_and_b32_dpp v29, v44, v5 row_newbcast:0 row_mask:0xf bank_mask:0xf
v_and_b32_dpp v30, v44, v6 row_newbcast:0 row_mask:0xf bank_mask:0xf
v_and_b32_dpp v31, v44, v7 row_newbcast:0 row_mask:0xf bank_mask:0xf
v_and_b32_dpp v32, v44, v4 row_newbcast:2 row_mask:0xf bank_mask:0xf
v_and_b32_dpp v33, v44, v5 row_newbcast:2 row_mask:0xf bank_mask:0xf
v_and_b32_dpp v34, v44, v6 row_newbcast:2 row_mask:0xf bank_mask:0xf
v_and_b32_dpp v35, v44, v7 row_newbcast:2 row_mask:0xf bank_mask:0xf
v_mfma_f32_16x16x32_bf16 v[40:43], v[28:31], a[112:115], v[40:43]
v_and_b32_dpp v28, v44, v4 row_newbcast:4 row_mask:0xf bank_mask:0xf
v_and_b32_dpp v29, v44, v5 row_newbcast:4 row_mask:0xf bank_mask:0xf
v_and_b32_dpp v30, v44, v6 row_newbcast:4 row_mask:0xf bank_mask:0xf
v_and_b32_dpp v31, v44, v7 row_newbcast:4 row_mask:0xf bank_mask:0xf
v_mfma_f32_16x16x32_bf16 v[40:43], v[32:35], a[116:119], v[40:43]
v_and_b32_dpp v32, v44, v4 row_newbcast:6 row_mask:0xf bank_mask:0xf
v_and_b32_dpp v33, v44, v5 row_newbcast:6 row_mask:0xf bank_mask:0xf
v_and_b32_dpp v34, v44, v6 row_newbcast:6 row_mask:0xf bank_mask:0xf
v_and_b32_dpp v35, v44, v7 row_newbcast:6 row_mask:0xf bank_mask:0xf
v_mfma_f32_16x16x32_bf16 v[40:43], v[28:31], a[120:123], v[40:43]
s_nop 0
v_mfma_f32_16x16x32_bf16 v[40:43], v[32:35], a[124:127], v[40:43]
v_mfma_f32_16x16x32_bf16 v[28:31], v[16:19], a[128:131], 0
v_mfma_f32_16x16x32_bf16 v[32:35], v[16:19], a[136:139], 0
v_mfma_f32_16x16x32_bf16 v[28:31], v[20:23], a[132:135], v[28:31]
v_mfma_f32_16x16x32_bf16 v[32:35], v[20:23], a[140:143], v[32:35]
s_nop 6
v_add_f32_dpp v44, v29, v28 quad_perm:[1,1,3,3] row_mask:0x3 bank_mask:0xf
s_nop 1
v_add_f32_dpp v44, v33, v32 quad_perm:[1,1,3,3] row_mask:0xc bank_mask:0xf
v_fma_f32 v45, v44, s38, v3
v_cmp_lt_f32_e32 vcc, 0, v45
s_nop 0
s_and_b64 s[48:49], vcc, s[40:41]
s_cbranch_scc0 Lc268_%=
s_swappc_b64 s[42:43], s[46:47]
Lc268_%=:
v_exp_f32_e32 v45, v45
s_nop 0
v_cndmask_b32_e64 v45, 0, v45, s[40:41]
v_add_f32_e32 v47, v47, v45
v_cvt_pk_bf16_f32 v44, v45, v45
s_nop 1
v_mov_b32_dpp v44, v44 row_ror:8 row_mask:0xa bank_mask:0xf
s_nop 1
v_and_b32_dpp v28, v44, v4 row_newbcast:0 row_mask:0xf bank_mask:0xf
v_and_b32_dpp v29, v44, v5 row_newbcast:0 row_mask:0xf bank_mask:0xf
v_and_b32_dpp v30, v44, v6 row_newbcast:0 row_mask:0xf bank_mask:0xf
v_and_b32_dpp v31, v44, v7 row_newbcast:0 row_mask:0xf bank_mask:0xf
v_and_b32_dpp v32, v44, v4 row_newbcast:2 row_mask:0xf bank_mask:0xf
v_and_b32_dpp v33, v44, v5 row_newbcast:2 row_mask:0xf bank_mask:0xf
v_and_b32_dpp v34, v44, v6 row_newbcast:2 row_mask:0xf bank_mask:0xf
v_and_b32_dpp v35, v44, v7 row_newbcast:2 row_mask:0xf bank_mask:0xf
v_mfma_f32_16x16x32_bf16 v[40:43], v[28:31], a[144:147], v[40:43]
v_and_b32_dpp v28, v44, v4 row_newbcast:4 row_mask:0xf bank_mask:0xf
v_and_b32_dpp v29, v44, v5 row_newbcast:4 row_mask:0xf bank_mask:0xf
v_and_b32_dpp v30, v44, v6 row_newbcast:4 row_mask:0xf bank_mask:0xf
v_and_b32_dpp v31, v44, v7 row_newbcast:4 row_mask:0xf bank_mask:0xf
v_mfma_f32_16x16x32_bf16 v[40:43], v[32:35], a[148:151], v[40:43]
v_and_b32_dpp v32, v44, v4 row_newbcast:6 row_mask:0xf bank_mask:0xf
v_and_b32_dpp v33, v44, v5 row_newbcast:6 row_mask:0xf bank_mask:0xf
v_and_b32_dpp v34, v44, v6 row_newbcast:6 row_mask:0xf bank_mask:0xf
v_and_b32_dpp v35, v44, v7 row_newbcast:6 row_mask:0xf bank_mask:0xf
v_mfma_f32_16x16x32_bf16 v[40:43], v[28:31], a[152:155], v[40:43]
s_nop 0
v_mfma_f32_16x16x32_bf16 v[40:43], v[32:35], a[156:159], v[40:43]
s_waitcnt vmcnt(56)
v_mfma_f32_16x16x32_bf16 v[28:31], v[16:19], a[160:163], 0
v_mfma_f32_16x16x32_bf16 v[32:35], v[16:19], a[168:171], 0
v_mfma_f32_16x16x32_bf16 v[28:31], v[20:23], a[164:167], v[28:31]
v_mfma_f32_16x16x32_bf16 v[32:35], v[20:23], a[172:175], v[32:35]
s_nop 6
v_add_f32_dpp v44, v29, v28 quad_perm:[1,1,3,3] row_mask:0x3 bank_mask:0xf
s_nop 1
v_add_f32_dpp v44, v33, v32 quad_perm:[1,1,3,3] row_mask:0xc bank_mask:0xf
v_fma_f32 v45, v44, s38, v3
v_cmp_lt_f32_e32 vcc, 0, v45
s_nop 0
s_and_b64 s[48:49], vcc, s[40:41]
s_cbranch_scc0 Lc269_%=
s_swappc_b64 s[42:43], s[46:47]
Lc269_%=:
v_exp_f32_e32 v45, v45
s_nop 0
v_cndmask_b32_e64 v45, 0, v45, s[40:41]
v_add_f32_e32 v47, v47, v45
v_cvt_pk_bf16_f32 v44, v45, v45
s_nop 1
v_mov_b32_dpp v44, v44 row_ror:8 row_mask:0xa bank_mask:0xf
s_waitcnt vmcnt(52)
s_nop 0
v_and_b32_dpp v28, v44, v4 row_newbcast:0 row_mask:0xf bank_mask:0xf
v_and_b32_dpp v29, v44, v5 row_newbcast:0 row_mask:0xf bank_mask:0xf
v_and_b32_dpp v30, v44, v6 row_newbcast:0 row_mask:0xf bank_mask:0xf
v_and_b32_dpp v31, v44, v7 row_newbcast:0 row_mask:0xf bank_mask:0xf
v_and_b32_dpp v32, v44, v4 row_newbcast:2 row_mask:0xf bank_mask:0xf
v_and_b32_dpp v33, v44, v5 row_newbcast:2 row_mask:0xf bank_mask:0xf
v_and_b32_dpp v34, v44, v6 row_newbcast:2 row_mask:0xf bank_mask:0xf
v_and_b32_dpp v35, v44, v7 row_newbcast:2 row_mask:0xf bank_mask:0xf
v_mfma_f32_16x16x32_bf16 v[40:43], v[28:31], a[176:179], v[40:43]
v_and_b32_dpp v28, v44, v4 row_newbcast:4 row_mask:0xf bank_mask:0xf
v_and_b32_dpp v29, v44, v5 row_newbcast:4 row_mask:0xf bank_mask:0xf
v_and_b32_dpp v30, v44, v6 row_newbcast:4 row_mask:0xf bank_mask:0xf
v_and_b32_dpp v31, v44, v7 row_newbcast:4 row_mask:0xf bank_mask:0xf
v_mfma_f32_16x16x32_bf16 v[40:43], v[32:35], a[180:183], v[40:43]
v_and_b32_dpp v32, v44, v4 row_newbcast:6 row_mask:0xf bank_mask:0xf
v_and_b32_dpp v33, v44, v5 row_newbcast:6 row_mask:0xf bank_mask:0xf
v_and_b32_dpp v34, v44, v6 row_newbcast:6 row_mask:0xf bank_mask:0xf
v_and_b32_dpp v35, v44, v7 row_newbcast:6 row_mask:0xf bank_mask:0xf
v_mfma_f32_16x16x32_bf16 v[40:43], v[28:31], a[184:187], v[40:43]
s_nop 0
v_mfma_f32_16x16x32_bf16 v[40:43], v[32:35], a[188:191], v[40:43]
s_waitcnt vmcnt(48)
v_mfma_f32_16x16x32_bf16 v[28:31], v[16:19], a[192:195], 0
v_mfma_f32_16x16x32_bf16 v[32:35], v[16:19], a[200:203], 0
v_mfma_f32_16x16x32_bf16 v[28:31], v[20:23], a[196:199], v[28:31]
v_mfma_f32_16x16x32_bf16 v[32:35], v[20:23], a[204:207], v[32:35]
s_nop 6
v_add_f32_dpp v44, v29, v28 quad_perm:[1,1,3,3] row_mask:0x3 bank_mask:0xf
s_nop 1
v_add_f32_dpp v44, v33, v32 quad_perm:[1,1,3,3] row_mask:0xc bank_mask:0xf
v_fma_f32 v45, v44, s38, v3
v_cmp_lt_f32_e32 vcc, 0, v45
s_nop 0
s_and_b64 s[48:49], vcc, s[40:41]
s_cbranch_scc0 Lc270_%=
s_swappc_b64 s[42:43], s[46:47]
Lc270_%=:
v_exp_f32_e32 v45, v45
s_nop 0
v_cndmask_b32_e64 v45, 0, v45, s[40:41]
v_add_f32_e32 v47, v47, v45
v_cvt_pk_bf16_f32 v44, v45, v45
s_nop 1
v_mov_b32_dpp v44, v44 row_ror:8 row_mask:0xa bank_mask:0xf
s_waitcnt vmcnt(44)
s_nop 0
v_and_b32_dpp v28, v44, v4 row_newbcast:0 row_mask:0xf bank_mask:0xf
v_and_b32_dpp v29, v44, v5 row_newbcast:0 row_mask:0xf bank_mask:0xf
v_and_b32_dpp v30, v44, v6 row_newbcast:0 row_mask:0xf bank_mask:0xf
v_and_b32_dpp v31, v44, v7 row_newbcast:0 row_mask:0xf bank_mask:0xf
v_and_b32_dpp v32, v44, v4 row_newbcast:2 row_mask:0xf bank_mask:0xf
v_and_b32_dpp v33, v44, v5 row_newbcast:2 row_mask:0xf bank_mask:0xf
v_and_b32_dpp v34, v44, v6 row_newbcast:2 row_mask:0xf bank_mask:0xf
v_and_b32_dpp v35, v44, v7 row_newbcast:2 row_mask:0xf bank_mask:0xf
v_mfma_f32_16x16x32_bf16 v[40:43], v[28:31], a[208:211], v[40:43]
v_and_b32_dpp v28, v44, v4 row_newbcast:4 row_mask:0xf bank_mask:0xf
v_and_b32_dpp v29, v44, v5 row_newbcast:4 row_mask:0xf bank_mask:0xf
v_and_b32_dpp v30, v44, v6 row_newbcast:4 row_mask:0xf bank_mask:0xf
v_and_b32_dpp v31, v44, v7 row_newbcast:4 row_mask:0xf bank_mask:0xf
v_mfma_f32_16x16x32_bf16 v[40:43], v[32:35], a[212:215], v[40:43]
v_and_b32_dpp v32, v44, v4 row_newbcast:6 row_mask:0xf bank_mask:0xf
v_and_b32_dpp v33, v44, v5 row_newbcast:6 row_mask:0xf bank_mask:0xf
v_and_b32_dpp v34, v44, v6 row_newbcast:6 row_mask:0xf bank_mask:0xf
v_and_b32_dpp v35, v44, v7 row_newbcast:6 row_mask:0xf bank_mask:0xf
v_mfma_f32_16x16x32_bf16 v[40:43], v[28:31], a[216:219], v[40:43]
s_nop 0
v_mfma_f32_16x16x32_bf16 v[40:43], v[32:35], a[220:223], v[40:43]
s_waitcnt vmcnt(40)
v_mfma_f32_16x16x32_bf16 v[28:31], v[16:19], a[224:227], 0
v_mfma_f32_16x16x32_bf16 v[32:35], v[16:19], a[232:235], 0
v_mfma_f32_16x16x32_bf16 v[28:31], v[20:23], a[228:231], v[28:31]
v_mfma_f32_16x16x32_bf16 v[32:35], v[20:23], a[236:239], v[32:35]
s_nop 6
v_add_f32_dpp v44, v29, v28 quad_perm:[1,1,3,3] row_mask:0x3 bank_mask:0xf
s_nop 1
v_add_f32_dpp v44, v33, v32 quad_perm:[1,1,3,3] row_mask:0xc bank_mask:0xf
v_fma_f32 v45, v44, s38, v3
v_cmp_lt_f32_e32 vcc, 0, v45
s_nop 0
s_and_b64 s[48:49], vcc, s[40:41]
s_cbranch_scc0 Lc271_%=
s_swappc_b64 s[42:43], s[46:47]
Lc271_%=:
v_exp_f32_e32 v45, v45
s_nop 0
v_cndmask_b32_e64 v45, 0, v45, s[40:41]
v_add_f32_e32 v47, v47, v45
v_cvt_pk_bf16_f32 v44, v45, v45
s_nop 1
v_mov_b32_dpp v44, v44 row_ror:8 row_mask:0xa bank_mask:0xf
s_waitcnt vmcnt(36)
s_nop 0
v_and_b32_dpp v28, v44, v4 row_newbcast:0 row_mask:0xf bank_mask:0xf
v_and_b32_dpp v29, v44, v5 row_newbcast:0 row_mask:0xf bank_mask:0xf
v_and_b32_dpp v30, v44, v6 row_newbcast:0 row_mask:0xf bank_mask:0xf
v_and_b32_dpp v31, v44, v7 row_newbcast:0 row_mask:0xf bank_mask:0xf
v_and_b32_dpp v32, v44, v4 row_newbcast:2 row_mask:0xf bank_mask:0xf
v_and_b32_dpp v33, v44, v5 row_newbcast:2 row_mask:0xf bank_mask:0xf
v_and_b32_dpp v34, v44, v6 row_newbcast:2 row_mask:0xf bank_mask:0xf
v_and_b32_dpp v35, v44, v7 row_newbcast:2 row_mask:0xf bank_mask:0xf
v_mfma_f32_16x16x32_bf16 v[40:43], v[28:31], a[240:243], v[40:43]
v_and_b32_dpp v28, v44, v4 row_newbcast:4 row_mask:0xf bank_mask:0xf
v_and_b32_dpp v29, v44, v5 row_newbcast:4 row_mask:0xf bank_mask:0xf
v_and_b32_dpp v30, v44, v6 row_newbcast:4 row_mask:0xf bank_mask:0xf
v_and_b32_dpp v31, v44, v7 row_newbcast:4 row_mask:0xf bank_mask:0xf
v_mfma_f32_16x16x32_bf16 v[40:43], v[32:35], a[244:247], v[40:43]
v_and_b32_dpp v32, v44, v4 row_newbcast:6 row_mask:0xf bank_mask:0xf
v_and_b32_dpp v33, v44, v5 row_newbcast:6 row_mask:0xf bank_mask:0xf
v_and_b32_dpp v34, v44, v6 row_newbcast:6 row_mask:0xf bank_mask:0xf
v_and_b32_dpp v35, v44, v7 row_newbcast:6 row_mask:0xf bank_mask:0xf
v_mfma_f32_16x16x32_bf16 v[40:43], v[28:31], a[248:251], v[40:43]
s_nop 0
v_mfma_f32_16x16x32_bf16 v[40:43], v[32:35], a[252:255], v[40:43]
s_waitcnt vmcnt(32)
v_mfma_f32_16x16x32_bf16 v[28:31], v[16:19], v[96:99], 0
v_mfma_f32_16x16x32_bf16 v[32:35], v[16:19], v[104:107], 0
v_mfma_f32_16x16x32_bf16 v[28:31], v[20:23], v[100:103], v[28:31]
v_mfma_f32_16x16x32_bf16 v[32:35], v[20:23], v[108:111], v[32:35]
s_nop 6
v_add_f32_dpp v44, v29, v28 quad_perm:[1,1,3,3] row_mask:0x3 bank_mask:0xf
s_nop 1
v_add_f32_dpp v44, v33, v32 quad_perm:[1,1,3,3] row_mask:0xc bank_mask:0xf
v_fma_f32 v45, v44, s38, v3
v_cmp_lt_f32_e32 vcc, 0, v45
s_nop 0
s_and_b64 s[48:49], vcc, s[40:41]
s_cbranch_scc0 Lc272_%=
s_swappc_b64 s[42:43], s[46:47]
Lc272_%=:
v_exp_f32_e32 v45, v45
s_nop 0
v_cndmask_b32_e64 v45, 0, v45, s[40:41]
v_add_f32_e32 v47, v47, v45
v_cvt_pk_bf16_f32 v44, v45, v45
s_nop 1
v_mov_b32_dpp v44, v44 row_ror:8 row_mask:0xa bank_mask:0xf
s_waitcnt vmcnt(28)
s_nop 0
v_and_b32_dpp v28, v44, v4 row_newbcast:0 row_mask:0xf bank_mask:0xf
v_and_b32_dpp v29, v44, v5 row_newbcast:0 row_mask:0xf bank_mask:0xf
v_and_b32_dpp v30, v44, v6 row_newbcast:0 row_mask:0xf bank_mask:0xf
v_and_b32_dpp v31, v44, v7 row_newbcast:0 row_mask:0xf bank_mask:0xf
v_and_b32_dpp v32, v44, v4 row_newbcast:2 row_mask:0xf bank_mask:0xf
v_and_b32_dpp v33, v44, v5 row_newbcast:2 row_mask:0xf bank_mask:0xf
v_and_b32_dpp v34, v44, v6 row_newbcast:2 row_mask:0xf bank_mask:0xf
v_and_b32_dpp v35, v44, v7 row_newbcast:2 row_mask:0xf bank_mask:0xf
v_mfma_f32_16x16x32_bf16 v[40:43], v[28:31], v[112:115], v[40:43]
v_and_b32_dpp v28, v44, v4 row_newbcast:4 row_mask:0xf bank_mask:0xf
v_and_b32_dpp v29, v44, v5 row_newbcast:4 row_mask:0xf bank_mask:0xf
v_and_b32_dpp v30, v44, v6 row_newbcast:4 row_mask:0xf bank_mask:0xf
v_and_b32_dpp v31, v44, v7 row_newbcast:4 row_mask:0xf bank_mask:0xf
v_mfma_f32_16x16x32_bf16 v[40:43], v[32:35], v[116:119], v[40:43]
v_and_b32_dpp v32, v44, v4 row_newbcast:6 row_mask:0xf bank_mask:0xf
v_and_b32_dpp v33, v44, v5 row_newbcast:6 row_mask:0xf bank_mask:0xf
v_and_b32_dpp v34, v44, v6 row_newbcast:6 row_mask:0xf bank_mask:0xf
v_and_b32_dpp v35, v44, v7 row_newbcast:6 row_mask:0xf bank_mask:0xf
v_mfma_f32_16x16x32_bf16 v[40:43], v[28:31], v[120:123], v[40:43]
s_nop 0
v_mfma_f32_16x16x32_bf16 v[40:43], v[32:35], v[124:127], v[40:43]
v_mfma_f32_16x16x32_bf16 v[28:31], v[16:19], v[64:67], 0
v_mfma_f32_16x16x32_bf16 v[32:35], v[16:19], v[72:75], 0
v_mfma_f32_16x16x32_bf16 v[28:31], v[20:23], v[68:71], v[28:31]
v_mfma_f32_16x16x32_bf16 v[32:35], v[20:23], v[76:79], v[32:35]
s_nop 6
v_add_f32_dpp v44, v29, v28 quad_perm:[1,1,3,3] row_mask:0x3 bank_mask:0xf
s_nop 1
v_add_f32_dpp v44, v33, v32 quad_perm:[1,1,3,3] row_mask:0xc bank_mask:0xf
v_fma_f32 v45, v44, s38, v3
v_cmp_lt_f32_e32 vcc, 0, v45
s_nop 0
s_and_b64 s[48:49], vcc, s[40:41]
s_cbranch_scc0 Lc273_%=
s_swappc_b64 s[42:43], s[46:47]
Lc273_%=:
v_exp_f32_e32 v45, v45
s_nop 0
v_cndmask_b32_e64 v45, 0, v45, s[40:41]
v_add_f32_e32 v47, v47, v45
v_cvt_pk_bf16_f32 v44, v45, v45
s_nop 1
v_mov_b32_dpp v44, v44 row_ror:8 row_mask:0xa bank_mask:0xf
s_waitcnt vmcnt(24)
s_nop 0
v_and_b32_dpp v28, v44, v4 row_newbcast:0 row_mask:0xf bank_mask:0xf
v_and_b32_dpp v29, v44, v5 row_newbcast:0 row_mask:0xf bank_mask:0xf
v_and_b32_dpp v30, v44, v6 row_newbcast:0 row_mask:0xf bank_mask:0xf
v_and_b32_dpp v31, v44, v7 row_newbcast:0 row_mask:0xf bank_mask:0xf
v_and_b32_dpp v32, v44, v4 row_newbcast:2 row_mask:0xf bank_mask:0xf
v_and_b32_dpp v33, v44, v5 row_newbcast:2 row_mask:0xf bank_mask:0xf
v_and_b32_dpp v34, v44, v6 row_newbcast:2 row_mask:0xf bank_mask:0xf
v_and_b32_dpp v35, v44, v7 row_newbcast:2 row_mask:0xf bank_mask:0xf
v_mfma_f32_16x16x32_bf16 v[40:43], v[28:31], v[80:83], v[40:43]
v_and_b32_dpp v28, v44, v4 row_newbcast:4 row_mask:0xf bank_mask:0xf
v_and_b32_dpp v29, v44, v5 row_newbcast:4 row_mask:0xf bank_mask:0xf
v_and_b32_dpp v30, v44, v6 row_newbcast:4 row_mask:0xf bank_mask:0xf
v_and_b32_dpp v31, v44, v7 row_newbcast:4 row_mask:0xf bank_mask:0xf
v_mfma_f32_16x16x32_bf16 v[40:43], v[32:35], v[84:87], v[40:43]
v_and_b32_dpp v32, v44, v4 row_newbcast:6 row_mask:0xf bank_mask:0xf
v_and_b32_dpp v33, v44, v5 row_newbcast:6 row_mask:0xf bank_mask:0xf
v_and_b32_dpp v34, v44, v6 row_newbcast:6 row_mask:0xf bank_mask:0xf
v_and_b32_dpp v35, v44, v7 row_newbcast:6 row_mask:0xf bank_mask:0xf
v_mfma_f32_16x16x32_bf16 v[40:43], v[28:31], v[88:91], v[40:43]
s_nop 0
v_mfma_f32_16x16x32_bf16 v[40:43], v[32:35], v[92:95], v[40:43]
s_waitcnt vmcnt(16)
v_mfma_f32_16x16x32_bf16 v[28:31], v[16:19], v[128:131], 0
v_mfma_f32_16x16x32_bf16 v[32:35], v[16:19], v[136:139], 0
v_mfma_f32_16x16x32_bf16 v[28:31], v[20:23], v[132:135], v[28:31]
v_mfma_f32_16x16x32_bf16 v[32:35], v[20:23], v[140:143], v[32:35]
s_nop 6
v_add_f32_dpp v44, v29, v28 quad_perm:[1,1,3,3] row_mask:0x3 bank_mask:0xf
s_nop 1
v_add_f32_dpp v44, v33, v32 quad_perm:[1,1,3,3] row_mask:0xc bank_mask:0xf
v_fma_f32 v45, v44, s38, v3
v_cmp_lt_f32_e32 vcc, 0, v45
s_nop 0
s_and_b64 s[48:49], vcc, s[40:41]
s_cbranch_scc0 Lc274_%=
s_swappc_b64 s[42:43], s[46:47]
Lc274_%=:
v_exp_f32_e32 v45, v45
s_nop 0
v_cndmask_b32_e64 v45, 0, v45, s[40:41]
v_add_f32_e32 v47, v47, v45
v_cvt_pk_bf16_f32 v44, v45, v45
s_nop 1
v_mov_b32_dpp v44, v44 row_ror:8 row_mask:0xa bank_mask:0xf
s_nop 1
v_and_b32_dpp v28, v44, v4 row_newbcast:0 row_mask:0xf bank_mask:0xf
v_and_b32_dpp v29, v44, v5 row_newbcast:0 row_mask:0xf bank_mask:0xf
v_and_b32_dpp v30, v44, v6 row_newbcast:0 row_mask:0xf bank_mask:0xf
v_and_b32_dpp v31, v44, v7 row_newbcast:0 row_mask:0xf bank_mask:0xf
v_and_b32_dpp v32, v44, v4 row_newbcast:2 row_mask:0xf bank_mask:0xf
v_and_b32_dpp v33, v44, v5 row_newbcast:2 row_mask:0xf bank_mask:0xf
v_and_b32_dpp v34, v44, v6 row_newbcast:2 row_mask:0xf bank_mask:0xf
v_and_b32_dpp v35, v44, v7 row_newbcast:2 row_mask:0xf bank_mask:0xf
v_mfma_f32_16x16x32_bf16 v[40:43], v[28:31], v[144:147], v[40:43]
v_and_b32_dpp v28, v44, v4 row_newbcast:4 row_mask:0xf bank_mask:0xf
v_and_b32_dpp v29, v44, v5 row_newbcast:4 row_mask:0xf bank_mask:0xf
v_and_b32_dpp v30, v44, v6 row_newbcast:4 row_mask:0xf bank_mask:0xf
v_and_b32_dpp v31, v44, v7 row_newbcast:4 row_mask:0xf bank_mask:0xf
v_mfma_f32_16x16x32_bf16 v[40:43], v[32:35], v[148:151], v[40:43]
v_and_b32_dpp v32, v44, v4 row_newbcast:6 row_mask:0xf bank_mask:0xf
v_and_b32_dpp v33, v44, v5 row_newbcast:6 row_mask:0xf bank_mask:0xf
v_and_b32_dpp v34, v44, v6 row_newbcast:6 row_mask:0xf bank_mask:0xf
v_and_b32_dpp v35, v44, v7 row_newbcast:6 row_mask:0xf bank_mask:0xf
v_mfma_f32_16x16x32_bf16 v[40:43], v[28:31], v[152:155], v[40:43]
s_nop 0
v_mfma_f32_16x16x32_bf16 v[40:43], v[32:35], v[156:159], v[40:43]
s_waitcnt vmcnt(8)
v_mfma_f32_16x16x32_bf16 v[28:31], v[16:19], v[160:163], 0
v_mfma_f32_16x16x32_bf16 v[32:35], v[16:19], v[168:171], 0
v_mfma_f32_16x16x32_bf16 v[28:31], v[20:23], v[164:167], v[28:31]
v_mfma_f32_16x16x32_bf16 v[32:35], v[20:23], v[172:175], v[32:35]
s_nop 6
v_add_f32_dpp v44, v29, v28 quad_perm:[1,1,3,3] row_mask:0x3 bank_mask:0xf
s_nop 1
v_add_f32_dpp v44, v33, v32 quad_perm:[1,1,3,3] row_mask:0xc bank_mask:0xf
v_fma_f32 v45, v44, s38, v3
v_cmp_lt_f32_e32 vcc, 0, v45
s_nop 0
s_and_b64 s[48:49], vcc, s[40:41]
s_cbranch_scc0 Lc275_%=
s_swappc_b64 s[42:43], s[46:47]
Lc275_%=:
v_exp_f32_e32 v45, v45
s_nop 0
v_cndmask_b32_e64 v45, 0, v45, s[40:41]
v_add_f32_e32 v47, v47, v45
v_cvt_pk_bf16_f32 v44, v45, v45
s_nop 1
v_mov_b32_dpp v44, v44 row_ror:8 row_mask:0xa bank_mask:0xf
s_nop 1
v_and_b32_dpp v28, v44, v4 row_newbcast:0 row_mask:0xf bank_mask:0xf
v_and_b32_dpp v29, v44, v5 row_newbcast:0 row_mask:0xf bank_mask:0xf
v_and_b32_dpp v30, v44, v6 row_newbcast:0 row_mask:0xf bank_mask:0xf
v_and_b32_dpp v31, v44, v7 row_newbcast:0 row_mask:0xf bank_mask:0xf
v_and_b32_dpp v32, v44, v4 row_newbcast:2 row_mask:0xf bank_mask:0xf
v_and_b32_dpp v33, v44, v5 row_newbcast:2 row_mask:0xf bank_mask:0xf
v_and_b32_dpp v34, v44, v6 row_newbcast:2 row_mask:0xf bank_mask:0xf
v_and_b32_dpp v35, v44, v7 row_newbcast:2 row_mask:0xf bank_mask:0xf
v_mfma_f32_16x16x32_bf16 v[40:43], v[28:31], v[176:179], v[40:43]
v_and_b32_dpp v28, v44, v4 row_newbcast:4 row_mask:0xf bank_mask:0xf
v_and_b32_dpp v29, v44, v5 row_newbcast:4 row_mask:0xf bank_mask:0xf
v_and_b32_dpp v30, v44, v6 row_newbcast:4 row_mask:0xf bank_mask:0xf
v_and_b32_dpp v31, v44, v7 row_newbcast:4 row_mask:0xf bank_mask:0xf
v_mfma_f32_16x16x32_bf16 v[40:43], v[32:35], v[180:183], v[40:43]
v_and_b32_dpp v32, v44, v4 row_newbcast:6 row_mask:0xf bank_mask:0xf
v_and_b32_dpp v33, v44, v5 row_newbcast:6 row_mask:0xf bank_mask:0xf
v_and_b32_dpp v34, v44, v6 row_newbcast:6 row_mask:0xf bank_mask:0xf
v_and_b32_dpp v35, v44, v7 row_newbcast:6 row_mask:0xf bank_mask:0xf
v_mfma_f32_16x16x32_bf16 v[40:43], v[28:31], v[184:187], v[40:43]
s_nop 0
v_mfma_f32_16x16x32_bf16 v[40:43], v[32:35], v[188:191], v[40:43]
s_waitcnt vmcnt(0)
v_mfma_f32_16x16x32_bf16 v[28:31], v[16:19], v[192:195], 0
v_mfma_f32_16x16x32_bf16 v[32:35], v[16:19], v[200:203], 0
v_mfma_f32_16x16x32_bf16 v[28:31], v[20:23], v[196:199], v[28:31]
v_mfma_f32_16x16x32_bf16 v[32:35], v[20:23], v[204:207], v[32:35]
s_nop 6
v_add_f32_dpp v44, v29, v28 quad_perm:[1,1,3,3] row_mask:0x3 bank_mask:0xf
s_nop 1
v_add_f32_dpp v44, v33, v32 quad_perm:[1,1,3,3] row_mask:0xc bank_mask:0xf
v_fma_f32 v45, v44, s38, v3
v_cmp_lt_f32_e32 vcc, 0, v45
s_nop 0
s_and_b64 s[48:49], vcc, s[40:41]
s_cbranch_scc0 Lc276_%=
s_swappc_b64 s[42:43], s[46:47]
Lc276_%=:
v_exp_f32_e32 v45, v45
s_nop 0
v_cndmask_b32_e64 v45, 0, v45, s[40:41]
v_add_f32_e32 v47, v47, v45
v_cvt_pk_bf16_f32 v44, v45, v45
s_nop 1
v_mov_b32_dpp v44, v44 row_ror:8 row_mask:0xa bank_mask:0xf
s_nop 1
v_and_b32_dpp v28, v44, v4 row_newbcast:0 row_mask:0xf bank_mask:0xf
v_and_b32_dpp v29, v44, v5 row_newbcast:0 row_mask:0xf bank_mask:0xf
v_and_b32_dpp v30, v44, v6 row_newbcast:0 row_mask:0xf bank_mask:0xf
v_and_b32_dpp v31, v44, v7 row_newbcast:0 row_mask:0xf bank_mask:0xf
v_and_b32_dpp v32, v44, v4 row_newbcast:2 row_mask:0xf bank_mask:0xf
v_and_b32_dpp v33, v44, v5 row_newbcast:2 row_mask:0xf bank_mask:0xf
v_and_b32_dpp v34, v44, v6 row_newbcast:2 row_mask:0xf bank_mask:0xf
v_and_b32_dpp v35, v44, v7 row_newbcast:2 row_mask:0xf bank_mask:0xf
v_mfma_f32_16x16x32_bf16 v[40:43], v[28:31], v[208:211], v[40:43]
v_and_b32_dpp v28, v44, v4 row_newbcast:4 row_mask:0xf bank_mask:0xf
v_and_b32_dpp v29, v44, v5 row_newbcast:4 row_mask:0xf bank_mask:0xf
v_and_b32_dpp v30, v44, v6 row_newbcast:4 row_mask:0xf bank_mask:0xf
v_and_b32_dpp v31, v44, v7 row_newbcast:4 row_mask:0xf bank_mask:0xf
v_mfma_f32_16x16x32_bf16 v[40:43], v[32:35], v[212:215], v[40:43]
v_and_b32_dpp v32, v44, v4 row_newbcast:6 row_mask:0xf bank_mask:0xf
v_and_b32_dpp v33, v44, v5 row_newbcast:6 row_mask:0xf bank_mask:0xf
v_and_b32_dpp v34, v44, v6 row_newbcast:6 row_mask:0xf bank_mask:0xf
v_and_b32_dpp v35, v44, v7 row_newbcast:6 row_mask:0xf bank_mask:0xf
v_mfma_f32_16x16x32_bf16 v[40:43], v[28:31], v[216:219], v[40:43]
s_nop 0
v_mfma_f32_16x16x32_bf16 v[40:43], v[32:35], v[220:223], v[40:43]
v_add_f32_dpp v29, v47, v47 row_ror:8 row_mask:0xf bank_mask:0xf
s_nop 1
v_add_f32_dpp v29, v29, v29 row_ror:4 row_mask:0xf bank_mask:0xf
s_nop 1
v_add_f32_dpp v29, v29, v29 row_ror:2 row_mask:0xf bank_mask:0xf
s_nop 1
v_add_f32_dpp v29, v29, v29 row_ror:1 row_mask:0xf bank_mask:0xf
s_nop 0
v_readlane_b32 s48, v29, 0
v_readlane_b32 s49, v29, 16
v_readlane_b32 s50, v29, 32
v_readlane_b32 s51, v29, 48
v_mov_b32_e32 v29, s48
v_add_f32_e32 v29, s49, v29
v_add_f32_e32 v29, s50, v29
v_add_f32_e32 v29, s51, v29
v_mov_b32_e32 v45, s74
s_nop 1
Ld277_%=:
ds_read_b32 v44, v45 offset:2116
s_waitcnt lgkmcnt(0)
v_readfirstlane_b32 s48, v44
s_nop 3
s_cmp_ge_u32 s48, 3
s_cbranch_scc1 Ldg278_%=
s_sub_u32 s56, s56, 1
s_cbranch_scc0 Ld277_%=
s_mov_b32 s56, 0
Ldg278_%=:
s_nop 1
v_mbcnt_lo_u32_b32 v45, -1, 0
v_mbcnt_hi_u32_b32 v45, -1, v45
v_lshlrev_b32_e32 v45, 4, v45
v_add_u32_e32 v45, s75, v45
v_sub_f32_e32 v28, 0, v3
s_sub_u32 s49, s75, s74
s_lshr_b32 s49, s49, 6
s_add_u32 s49, s49, s74
v_mov_b32_e32 v44, s49
s_mov_b32 exec_hi, 0
ds_write_b128 v45, v[40:43]
s_mov_b32 exec_lo, 1
ds_write_b64 v44, v[28:29] offset:2048
s_waitcnt lgkmcnt(0)
v_mov_b32_e32 v45, s74
v_mov_b32_e32 v44, 1
ds_add_rtn_u32 v44, v45, v44 offset:2112
s_waitcnt lgkmcnt(0)
v_readfirstlane_b32 s48, v44
s_mov_b64 exec, -1
s_and_b32 s48, s48, 3
s_cmp_eq_u32 s48, 3
s_nop 1
s_cbranch_scc0 Lnp279_%=
v_mbcnt_lo_u32_b32 v45, -1, 0
v_mbcnt_hi_u32_b32 v45, -1, v45
v_lshlrev_b32_e32 v45, 4, v45
v_add_u32_e32 v45, s74, v45
ds_read_b128 v[28:31], v45
ds_read_b128 v[16:19], v45 offset:512
ds_read_b128 v[20:23], v45 offset:1024
v_mbcnt_lo_u32_b32 v44, -1, 0
v_mbcnt_hi_u32_b32 v44, -1, v44
v_and_b32_e32 v44, 3, v44
v_lshlrev_b32_e32 v44, 3, v44
v_add_u32_e32 v44, s74, v44
ds_read_b64 v[32:33], v44 offset:2048
s_waitcnt lgkmcnt(0)
v_max_f32_dpp v34, v32, v32 quad_perm:[1,0,3,2] row_mask:0xf bank_mask:0xf
s_nop 1
v_max_f32_dpp v34, v34, v34 quad_perm:[2,3,0,1] row_mask:0xf bank_mask:0xf
v_sub_f32_e32 v44, v32, v34
v_exp_f32_e32 v44, v44
s_nop 0
v_mul_f32_e32 v35, v44, v33
s_nop 1
v_add_f32_dpp v35, v35, v35 quad_perm:[1,0,3,2] row_mask:0xf bank_mask:0xf
s_nop 1
v_add_f32_dpp v35, v35, v35 quad_perm:[2,3,0,1] row_mask:0xf bank_mask:0xf
v_mul_f32_dpp v40, v44, v28 quad_perm:[0,0,0,0] row_mask:0xf bank_mask:0xf
v_mul_f32_dpp v41, v44, v29 quad_perm:[0,0,0,0] row_mask:0xf bank_mask:0xf
v_mul_f32_dpp v42, v44, v30 quad_perm:[0,0,0,0] row_mask:0xf bank_mask:0xf
v_mul_f32_dpp v43, v44, v31 quad_perm:[0,0,0,0] row_mask:0xf bank_mask:0xf
ds_read_b128 v[28:31], v45 offset:1536
v_fmac_f32_dpp v40, v44, v16 quad_perm:[1,1,1,1] row_mask:0xf bank_mask:0xf
v_fmac_f32_dpp v41, v44, v17 quad_perm:[1,1,1,1] row_mask:0xf bank_mask:0xf
v_fmac_f32_dpp v42, v44, v18 quad_perm:[1,1,1,1] row_mask:0xf bank_mask:0xf
v_fmac_f32_dpp v43, v44, v19 quad_perm:[1,1,1,1] row_mask:0xf bank_mask:0xf
v_fmac_f32_dpp v40, v44, v20 quad_perm:[2,2,2,2] row_mask:0xf bank_mask:0xf
v_fmac_f32_dpp v41, v44, v21 quad_perm:[2,2,2,2] row_mask:0xf bank_mask:0xf
v_fmac_f32_dpp v42, v44, v22 quad_perm:[2,2,2,2] row_mask:0xf bank_mask:0xf
v_fmac_f32_dpp v43, v44, v23 quad_perm:[2,2,2,2] row_mask:0xf bank_mask:0xf
s_waitcnt lgkmcnt(0)
v_fmac_f32_dpp v40, v44, v28 quad_perm:[3,3,3,3] row_mask:0xf bank_mask:0xf
v_fmac_f32_dpp v41, v44, v29 quad_perm:[3,3,3,3] row_mask:0xf bank_mask:0xf
v_fmac_f32_dpp v42, v44, v30 quad_perm:[3,3,3,3] row_mask:0xf bank_mask:0xf
v_fmac_f32_dpp v43, v44, v31 quad_perm:[3,3,3,3] row_mask:0xf bank_mask:0xf
v_mov_b32_e32 v45, s74
v_mov_b32_e32 v44, 4
ds_write_b32 v45, v44 offset:2116
s_add_u32 s58, s66, 884736
s_addc_u32 s59, s67, 0
v_permlane32_swap_b32 v40, v42
v_permlane32_swap_b32 v41, v43
v_mov_b32_e32 v28, v40
v_mov_b32_e32 v29, s37
v_mov_b32_e32 v30, v41
v_mov_b32_e32 v31, s37
v_mov_b32_e32 v16, v34
v_mov_b32_e32 v17, s37
v_mov_b32_e32 v18, v35
v_mov_b32_e32 v19, s37
s_waitcnt lgkmcnt(0)
v_mbcnt_lo_u32_b32 v45, -1, 0
v_mbcnt_hi_u32_b32 v45, -1, v45
v_lshlrev_b32_e32 v45, 4, v45
global_store_dwordx4 v45, v[28:31], s[58:59] offset:0 sc1
s_mov_b64 exec, 0xff
global_store_dwordx4 v45, v[16:19], s[58:59] offset:1024 sc1
s_mov_b64 exec, -1
s_nop 3
Lnp279_%=:
s_nop 1
v_mov_b32_e32 v40, 0
v_mov_b32_e32 v41, 0
v_mov_b32_e32 v42, 0
v_mov_b32_e32 v43, 0
v_mov_b32_e32 v47, 0
v_mov_b32_e32 v3, 0x7f800000
s_waitcnt vmcnt(0) lgkmcnt(0)
s_branch Lend_%=
LrareA_%=:
s_nop 9
v_mov_b32_e32 v29, 0xff800000
v_cndmask_b32_e64 v29, v29, v44, s[40:41]
s_nop 1
v_max_f32_dpp v28, v29, v29 row_ror:8 row_mask:0xf bank_mask:0xf
s_nop 1
v_max_f32_dpp v28, v28, v28 row_ror:4 row_mask:0xf bank_mask:0xf
s_nop 1
v_max_f32_dpp v28, v28, v28 row_ror:2 row_mask:0xf bank_mask:0xf
s_nop 1
v_max_f32_dpp v28, v28, v28 row_ror:1 row_mask:0xf bank_mask:0xf
s_nop 0
v_readlane_b32 s48, v28, 0
v_readlane_b32 s49, v28, 16
v_readlane_b32 s50, v28, 32
v_readlane_b32 s51, v28, 48
v_mov_b32_e32 v28, s48
v_max_f32_e32 v28, s49, v28
v_max_f32_e32 v28, s50, v28
v_max_f32_e32 v28, s51, v28
v_mul_f32_e64 v28, s38, -v28
v_min_f32_e32 v28, v28, v2
v_sub_f32_e32 v29, v28, v2
v_exp_f32_e32 v29, v29
s_nop 0
v_mul_f32_e32 v36, v36, v29
v_mul_f32_e32 v37, v37, v29
v_mul_f32_e32 v38, v38, v29
v_mul_f32_e32 v39, v39, v29
v_mul_f32_e32 v46, v46, v29
v_mov_b32_e32 v2, v28
v_fma_f32 v45, v44, s38, v2
s_nop 3
s_setpc_b64 s[42:43]
LrareB_%=:
s_nop 9
v_mov_b32_e32 v29, 0xff800000
v_cndmask_b32_e64 v29, v29, v44, s[40:41]
s_nop 1
v_max_f32_dpp v28, v29, v29 row_ror:8 row_mask:0xf bank_mask:0xf
s_nop 1
v_max_f32_dpp v28, v28, v28 row_ror:4 row_mask:0xf bank_mask:0xf
s_nop 1
v_max_f32_dpp v28, v28, v28 row_ror:2 row_mask:0xf bank_mask:0xf
s_nop 1
v_max_f32_dpp v28, v28, v28 row_ror:1 row_mask:0xf bank_mask:0xf
s_nop 0
v_readlane_b32 s48, v28, 0
v_readlane_b32 s49, v28, 16
v_readlane_b32 s50, v28, 32
v_readlane_b32 s51, v28, 48
v_mov_b32_e32 v28, s48
v_max_f32_e32 v28, s49, v28
v_max_f32_e32 v28, s50, v28
v_max_f32_e32 v28, s51, v28
v_mul_f32_e64 v28, s38, -v28
v_min_f32_e32 v28, v28, v3
v_sub_f32_e32 v29, v28, v3
v_exp_f32_e32 v29, v29
s_nop 0
v_mul_f32_e32 v40, v40, v29
v_mul_f32_e32 v41, v41, v29
v_mul_f32_e32 v42, v42, v29
v_mul_f32_e32 v43, v43, v29
v_mul_f32_e32 v47, v47, v29
v_mov_b32_e32 v3, v28
v_fma_f32 v45, v44, s38, v3
s_nop 3
s_setpc_b64 s[42:43]
Lend_%=:
)ASM"
    :: "s"(kb), "s"(vb), "s"(qp), "s"(pp), "s"(nn), "s"(p.ws), "s"(lb), "s"(p.ws), "s"(xb), "s"(w), "s"(rp), "s"(rm)
    : "v0", "v1", "v2", "v3", "v4", "v5", "v6", "v7", "v8", "v9", "v10", "v11", "v12", "v13", "v14", "v15", "v16", "v17", "v18", "v19", "v20", "v21", "v22", "v23", "v24", "v25", "v26", "v27", "v28", "v29", "v30", "v31", "v32", "v33", "v34", "v35", "v36", "v37", "v38", "v39", "v40", "v41", "v42", "v43", "v44", "v45", "v46", "v48", "v49", "v50", "v51", "v52", "v53", "v54", "v55", "v56", "v57", "v58", "v59", "v60", "v61", "v62", "v63", "v64", "v65", "v66", "v67", "v68", "v69", "v70", "v71", "v72", "v73", "v74", "v75", "v76", "v77", "v78", "v79", "v80", "v81", "v82", "v83", "v84", "v85", "v86", "v87", "v88", "v89", "v90", "v91", "v92", "v93", "v94", "v95", "v96", "v97", "v98", "v99", "v100", "v101", "v102", "v103", "v104", "v105", "v106", "v107", "v108", "v109", "v110", "v111", "v112", "v113", "v114", "v115", "v116", "v117", "v118", "v119", "v120", "v121", "v122", "v123", "v124", "v125", "v126", "v127", "v128", "v129", "v130", "v131", "v132", "v133", "v134", "v135", "v136", "v137", "v138", "v139", "v140", "v141", "v142", "v143", "v144", "v145", "v146", "v147", "v148", "v149", "v150", "v151", "v152", "v153", "v154", "v155", "v156", "v157", "v158", "v159", "v160", "v161", "v162", "v163", "v164", "v165", "v166", "v167", "v168", "v169", "v170", "v171", "v172", "v173", "v174", "v175", "v176", "v177", "v178", "v179", "v180", "v181", "v182", "v183", "v184", "v185", "v186", "v187", "v188", "v189", "v190", "v191", "v192", "v193", "v194", "v195", "v196", "v197", "v198", "v199", "v200", "v201", "v202", "v203", "v204", "v205", "v206", "v207", "v208", "v209", "v210", "v211", "v212", "v213", "v214", "v215", "v216", "v217", "v218", "v219", "v220", "v221", "v222", "v223", "v224", "v225", "v226", "v227", "v228", "v229", "v230", "v231", "v232", "v233", "v234", "v235", "v236", "v237", "v238", "v239", "v240", "v241", "v242", "v243", "v244", "v245", "v246", "v247", "v248", "v249", "v250", "v251", "v252", "v253", "v254", "v255", "a0", "a1", "a2", "a3", "a4", "a5", "a6", "a7", "a8", "a9", "a10", "a11", "a12", "a13", "a14", "a15", "a16", "a17", "a18", "a19", "a20", "a21", "a22", "a23", "a24", "a25", "a26", "a27", "a28", "a29", "a30", "a31", "a32", "a33", "a34", "a35", "a36", "a37", "a38", "a39", "a40", "a41", "a42", "a43", "a44", "a45", "a46", "a47", "a48", "a49", "a50", "a51", "a52", "a53", "a54", "a55", "a56", "a57", "a58", "a59", "a60", "a61", "a62", "a63", "a64", "a65", "a66", "a67", "a68", "a69", "a70", "a71", "a72", "a73", "a74", "a75", "a76", "a77", "a78", "a79", "a80", "a81", "a82", "a83", "a84", "a85", "a86", "a87", "a88", "a89", "a90", "a91", "a92", "a93", "a94", "a95", "a96", "a97", "a98", "a99", "a100", "a101", "a102", "a103", "a104", "a105", "a106", "a107", "a108", "a109", "a110", "a111", "a112", "a113", "a114", "a115", "a116", "a117", "a118", "a119", "a120", "a121", "a122", "a123", "a124", "a125", "a126", "a127", "a128", "a129", "a130", "a131", "a132", "a133", "a134", "a135", "a136", "a137", "a138", "a139", "a140", "a141", "a142", "a143", "a144", "a145", "a146", "a147", "a148", "a149", "a150", "a151", "a152", "a153", "a154", "a155", "a156", "a157", "a158", "a159", "a160", "a161", "a162", "a163", "a164", "a165", "a166", "a167", "a168", "a169", "a170", "a171", "a172", "a173", "a174", "a175", "a176", "a177", "a178", "a179", "a180", "a181", "a182", "a183", "a184", "a185", "a186", "a187", "a188", "a189", "a190", "a191", "a192", "a193", "a194", "a195", "a196", "a197", "a198", "a199", "a200", "a201", "a202", "a203", "a204", "a205", "a206", "a207", "a208", "a209", "a210", "a211", "a212", "a213", "a214", "a215", "a216", "a217", "a218", "a219", "a220", "a221", "a222", "a223", "a224", "a225", "a226", "a227", "a228", "a229", "a230", "a231", "a232", "a233", "a234", "a235", "a236", "a237", "a238", "a239", "a240", "a241", "a242", "a243", "a244", "a245", "a246", "a247", "a248", "a249", "a250", "a251", "a252", "a253", "a254", "a255", "s20", "s21", "s22", "s23", "s24", "s25", "s26", "s27", "s28", "s29", "s30", "s31", "s36", "s37", "s38", "s39", "s40", "s41", "s42", "s43", "s44", "s45", "s46", "s47", "s48", "s49", "s50", "s51", "s56", "s57", "s58", "s59", "s60", "s61", "s62", "s63", "s64", "s65", "s66", "s67", "s68", "s69", "s70", "s71", "s72", "s73", "s74", "s75", "s76", "s77", "s78", "s79", "s80", "s81", "vcc", "scc", "memory");
  __builtin_amdgcn_endpgm();
}
}

extern "C" void kernel_launch(void* const* d_in, const int* in_sizes, int n_in, void* d_out, int out_size, void* d_ws, size_t ws_size, hipStream_t stream) {
  Params p;
  p.x = (const u16*)d_in[0]; p.K = (const unsigned char*)d_in[1]; p.V = (const unsigned char*)d_in[2];
  p.wq = (const u16*)d_in[3]; p.wo = (const u16*)d_in[4]; p.wg = (const u16*)d_in[6]; p.wu = (const u16*)d_in[7]; p.wd = (const u16*)d_in[8];
  p.out = (u16*)d_out; p.ws = (unsigned char*)d_ws;
  (void)hipFuncSetAttribute((const void*)k_main, hipFuncAttributeMaxDynamicSharedMemorySize, LDS_BYTES);
  k_main<<<256, 256, LDS_BYTES, stream>>>(p);
}
```
